```python
import torch

def _softmax(x, axis=-1):
    return torch.softmax(x, dim=axis)

def _gelu(x):
    return torch.nn.functional.gelu(x, approximate='tanh')

def _silu(x):
    return torch.nn.functional.silu(x)

def _sigmoid(x):
    return torch.sigmoid(x)

def _layernorm(x, axis=-1, eps=1e-05):
    m = x.mean(dim=axis, keepdim=True)
    v = x.var(dim=axis, keepdim=True, unbiased=False)
    return (x - m) / torch.sqrt(v + eps)

def _scatter_add(tgt, idx, src, axis=0):
    return tgt.clone().index_add_(axis, idx, src)

def _segment_sum(src, seg, n):
    out = torch.zeros((n,) + src.shape[1:], dtype=src.dtype)
    return out.index_add_(0, seg, src)

def _pad(x, pw):
    flat = tuple((v for pair in reversed(pw) for v in pair))
    return torch.nn.functional.pad(x, flat)
kernel_name = 'adaLN_cond__sliding_block_cached__mixer_gmlp__prompt'

def setup_inputs():

    _w = lambda a: (lambda t: t.to(torch.bfloat16) if t.is_floating_point() else t)(torch.as_tensor(a))

    def _A_setup():
        B, Sq, D = (1, 2048, 448)
        Hq, Hkv, Sk, Dh = (16, 8, 2048, 256)
        assert Hq % Hkv == 0, (Hq, Hkv)
        return {'x': _w((lambda *s: torch.randn(*s, dtype=torch.bfloat16) * 0.1)(B, Sq, D)), 'k_cache': _w((lambda *s: torch.randn(*s, dtype=torch.bfloat16) * 0.8)(B, Hkv, Sk, Dh)), 'v_cache': _w((lambda *s: torch.randn(*s, dtype=torch.bfloat16) * 1.6)(B, Hkv, Sk, Dh)), 'wq': _w((lambda *s: torch.randn(*s, dtype=torch.bfloat16) * 0.8)(D, Hq * Dh) * D ** (-0.5)), 'wo': _w((lambda *s: torch.randn(*s, dtype=torch.bfloat16) * 1.6)(Hq * Dh, D) * (Hq * Dh) ** (-0.5))}
    _A_in = _A_setup()

    def _B_setup():
        B, S, D, F = (1, 2048, 448, 1792)
        Fh = F // 2
        return {'x': _w((lambda *s: torch.randn(*s, dtype=torch.bfloat16) * 0.1)(B, S, D)), 'w_in': _w((lambda *s: torch.randn(*s, dtype=torch.bfloat16) * 1.6)(D, 2 * Fh)), 'w_sp': _w(0.01 * (lambda *s: torch.randn(*s, dtype=torch.bfloat16) * 1.6)(S, S)), 'b_sp': _w(1.0 + 0.01 * (lambda *s: torch.randn(*s, dtype=torch.bfloat16) * 0.2)(S)), 'w_out': _w((lambda *s: torch.randn(*s, dtype=torch.bfloat16) * 0.2)(Fh, D))}
    _B_in = _B_setup()
    return {'A_x': _A_in['x'], 'A_k_cache': _A_in['k_cache'], 'A_v_cache': _A_in['v_cache'], 'A_wq': _A_in['wq'], 'A_wo': _A_in['wo'], 'B_x': _B_in['x'], 'B_w_in': _B_in['w_in'], 'B_w_sp': _B_in['w_sp'], 'B_b_sp': _B_in['b_sp'], 'B_w_out': _B_in['w_out'], 'c': _w((lambda *s: torch.randn(*s, dtype=torch.bfloat16) * 0.8)(1, 448) * 8.0), 'w_mod': _w((lambda *s: torch.randn(*s, dtype=torch.bfloat16) * 0.8)(448, 6 * 448) * 448 ** (-0.5) * 8.0)}

def reference(A_x, A_k_cache, A_v_cache, A_wq, A_wo, B_x, B_w_in, B_w_sp, B_b_sp, B_w_out, c, w_mod):

    def _A_ref(x, k_cache, v_cache, wq, wo):
        B, Sq, D = x.shape
        Hq, Dh = (16, 256)
        Hkv, Sk = (k_cache.shape[1], k_cache.shape[2])
        W = min(4096, max(256, Sk // 4))
        rep = Hq // Hkv
        q = (lambda x, *d: x.permute(*d))((x @ wq).reshape(B, Sq, Hq, Dh), 0, 2, 1, 3)
        k = (lambda x, n, axis: torch.repeat_interleave(x, n, dim=axis))(k_cache[:, :, Sk - W:, :], rep, axis=1)
        v = (lambda x, n, axis: torch.repeat_interleave(x, n, dim=axis))(v_cache[:, :, Sk - W:, :], rep, axis=1)
        scores = torch.einsum('bhqd,bhkd->bhqk', q, k) * Dh ** (-0.5)
        p = _softmax(scores, axis=-1)
        o = torch.einsum('bhqk,bhkd->bhqd', p, v)
        return (lambda x, *d: x.permute(*d))(o, 0, 2, 1, 3).reshape(B, Sq, Hq * Dh) @ wo

    def _B_ref(x, w_in, w_sp, b_sp, w_out):
        B, S, D = x.shape
        F = w_in.shape[-1]
        Fh = F // 2
        z = _gelu(torch.einsum('bsd,df->bsf', x, w_in))
        z1 = z[..., :Fh].clone()
        z2 = _layernorm(z[..., Fh:], axis=-1)
        g = torch.einsum('ts,bsf->btf', w_sp, z2) + b_sp[None, :, None]
        return torch.einsum('bsf,fd->bsd', z1 * g, w_out)
    x = A_x
    mod = (_silu(c) @ w_mod).reshape(c.shape[0], 6, 1, -1)
    sa, ca, ga, sb, cb, gb = (mod[:, 0].clone(), mod[:, 1].clone(), mod[:, 2].clone(), mod[:, 3].clone(), mod[:, 4].clone(), mod[:, 5].clone())
    h = _layernorm(x, axis=-1) * (1 + ca) + sa
    x = x + ga * _A_ref(h, A_k_cache, A_v_cache, A_wq, A_wo)
    h = _layernorm(x, axis=-1) * (1 + cb) + sb
    x = x + gb * _B_ref(h, B_w_in, B_w_sp, B_b_sp, B_w_out)
    return x
```

```cpp
#include <hip/hip_runtime.h>
#include <hip/hip_bf16.h>
#include <stdint.h>
typedef unsigned u32x4 __attribute__((ext_vector_type(4)));
typedef unsigned u32x2 __attribute__((ext_vector_type(2)));
typedef float f32x4 __attribute__((ext_vector_type(4)));
typedef float f32x2 __attribute__((ext_vector_type(2)));
typedef __bf16 bf16x2_t __attribute__((ext_vector_type(2)));
typedef _Float16 f16x2_t __attribute__((ext_vector_type(2)));
#define DI __device__ __forceinline__
DI float bflo(unsigned u) { return __uint_as_float(u << 16); }
DI float bfhi(unsigned u) { return __uint_as_float(u & 0xffff0000u); }
DI unsigned pkbf(float lo, float hi) { f32x2 v = {lo, hi}; bf16x2_t b = __builtin_convertvector(v, bf16x2_t); return __builtin_bit_cast(unsigned, b); }
DI unsigned pkh(float lo, float hi) { f32x2 v = {lo, hi}; f16x2_t b = __builtin_convertvector(v, f16x2_t); return __builtin_bit_cast(unsigned, b); }
constexpr size_t WS_MODV = 0;
constexpr size_t WS_MOD  = 4096;
constexpr size_t WS_XH   = 16384;
constexpr size_t WS_KT   = WS_XH + 1835008;
constexpr size_t WS_VT   = WS_KT + 2097152;
constexpr size_t WS_PART = WS_VT + 2097152;
constexpr size_t WS_WINF = WS_PART + 29360128;
constexpr size_t WS_WOF  = WS_WINF + 1605632;
constexpr size_t WS_H2F  = WS_WOF + 802816;
constexpr size_t WS_X1   = WS_H2F + 1835008;
constexpr size_t WS_STAT = WS_X1 + 1835008;
constexpr size_t WS_Z2F  = WS_STAT + 131072;
constexpr size_t WS_UF   = WS_Z2F + 3670016;
constexpr size_t WS_FLAG = WS_UF + 3670016;
constexpr size_t WS_FLAG2 = WS_FLAG + 32768;
constexpr size_t WS_WSPF = WS_FLAG2 + 16384;
constexpr size_t WS_END  = WS_WSPF + 8388608;
DI void st16(char* base, size_t off, u32x4 v) { *(u32x4*)(base + off) = v; }
constexpr int K0_MOD = 84, K0_KV = 256, K0_LN = 256, K0_WIN = 224, K0_WOUT = 196;
constexpr unsigned K1_CODE_BYTES = 41 * 1024, T_CODE_BYTES = 27 * 1024;
constexpr int K0_GRID = K0_MOD + K0_KV + K0_LN + K0_WIN + K0_WOUT;
__global__ __launch_bounds__(256) void k0_prep(const unsigned short* __restrict__ x, const unsigned short* __restrict__ kc, const unsigned short* __restrict__ vc,
                                               const unsigned short* __restrict__ cvec, const unsigned short* __restrict__ wmod, char* __restrict__ ws,
                                               const unsigned short* __restrict__ win, const unsigned short* __restrict__ wout) {
  __shared__ __attribute__((aligned(16))) char sm[16384];
  const int tid = threadIdx.x, lane = tid & 63, w = __builtin_amdgcn_readfirstlane(tid >> 6);
  int b = blockIdx.x;
  if (b < K0_MOD) {
    const int cq = tid & 3, rg = tid >> 2;
    u32x4 wm[7]; unsigned short cr[7];
#pragma unroll
    for (int i = 0; i < 7; ++i) wm[i] = *(const u32x4*)(wmod + (size_t)(rg * 7 + i) * 2688 + 32 * b + 8 * cq);
#pragma unroll
    for (int i = 0; i < 7; ++i) cr[i] = cvec[rg * 7 + i];
    float a[8] = {0, 0, 0, 0, 0, 0, 0, 0};
#pragma unroll
    for (int i = 0; i < 7; ++i) {
      const float cs = __uint_as_float((unsigned)cr[i] << 16), sc = cs / (1.f + __expf(-cs));
#pragma unroll
      for (int k = 0; k < 4; ++k) { a[2 * k] += sc * bflo(wm[i][k]); a[2 * k + 1] += sc * bfhi(wm[i][k]); }
    }
#pragma unroll
    for (int k = 0; k < 8; ++k) { a[k] += __shfl_xor(a[k], 4); a[k] += __shfl_xor(a[k], 8); a[k] += __shfl_xor(a[k], 16); a[k] += __shfl_xor(a[k], 32); }
    float* rm = (float*)sm;
    if (lane < 4) {
#pragma unroll
      for (int k = 0; k < 8; ++k) rm[w * 32 + lane * 8 + k] = a[k];
    }
    __syncthreads();
    if (tid < 32) {
      const float m = rm[tid] + rm[32 + tid] + rm[64 + tid] + rm[96 + tid];
      const int col = 32 * b + tid;
      ((float*)(ws + WS_MOD))[col] = m;
      const float cq_ = 0.09016844005556021f;
      if (col < 448) ((_Float16*)(ws + WS_MODV))[2 * col + 1] = (_Float16)(m * cq_);
      else if (col < 896) ((_Float16*)(ws + WS_MODV))[2 * (col - 448)] = (_Float16)((1.f + m) * cq_);
    }
  } else if ((b -= K0_MOD) < K0_LN) {
    const int r = tid & 7, cc = tid >> 3, row = 8 * b + r;
    const u32x4 xa = *(const u32x4*)(x + (size_t)row * 448 + 8 * cc), xb = *(const u32x4*)(x + (size_t)row * 448 + 8 * (cc < 24 ? cc + 32 : cc));
    unsigned cpf = 0;
    if (b < 8 * 24) {
      const char* p;
      const unsigned u = (unsigned)(b >> 3) * 4u + (unsigned)w;
      if (u < 48) asm volatile("s_getpc_b64 s[40:41]\n\ts_add_u32 s40, s40, k1_attn@rel32@lo+4\n\ts_addc_u32 s41, s41, k1_attn@rel32@hi+12\n\ts_mov_b64 %0, s[40:41]" : "=s"(p) :: "s40", "s41", "scc");
      else asm volatile("s_getpc_b64 s[40:41]\n\ts_add_u32 s40, s40, t_tail@rel32@lo+4\n\ts_addc_u32 s41, s41, t_tail@rel32@hi+12\n\ts_mov_b64 %0, s[40:41]" : "=s"(p) :: "s40", "s41", "scc");
      const unsigned off = (u < 48 ? u : u - 48) * 1024u;
      if (off + 1024u <= (u < 48 ? K1_CODE_BYTES : T_CODE_BYTES)) cpf = *(const __attribute__((address_space(1))) unsigned*)(size_t)(p + off + lane * 16);
    }
    { const u32x4 ones = {0xffffffffu, 0xffffffffu, 0xffffffffu, 0xffffffffu};
      st16(ws, WS_H2F + (size_t)b * 7168 + tid * 16, ones);
      if (tid < 192) st16(ws, WS_H2F + (size_t)b * 7168 + 4096 + tid * 16, ones);
      if (tid < 8) st16(ws, WS_FLAG + (size_t)b * 128 + tid * 16, u32x4{0, 0, 0, 0});
    }
    float v[16];
#pragma unroll
    for (int i = 0; i < 4; ++i) { v[2 * i] = bflo(xa[i]); v[2 * i + 1] = bfhi(xa[i]); v[8 + 2 * i] = cc < 24 ? bflo(xb[i]) : 0.f; v[9 + 2 * i] = cc < 24 ? bfhi(xb[i]) : 0.f; }
    float* red = (float*)sm;
    float s1 = 0.f;
#pragma unroll
    for (int i = 0; i < 16; ++i) s1 += v[i];
    s1 += __shfl_xor(s1, 8); s1 += __shfl_xor(s1, 16); s1 += __shfl_xor(s1, 32);
    if (lane < 8) red[w * 8 + lane] = s1;
    __syncthreads();
    const float mean = (red[r] + red[8 + r] + red[16 + r] + red[24 + r]) * (1.f / 448.f);
    float s2 = 0.f;
#pragma unroll
    for (int i = 0; i < 8; ++i) { v[i] -= mean; s2 += v[i] * v[i]; }
    if (cc < 24) {
#pragma unroll
      for (int i = 8; i < 16; ++i) { v[i] -= mean; s2 += v[i] * v[i]; }
    }
    s2 += __shfl_xor(s2, 8); s2 += __shfl_xor(s2, 16); s2 += __shfl_xor(s2, 32);
    if (lane < 8) red[32 + w * 8 + lane] = s2;
    __syncthreads();
    const float rstd = 1.f / sqrtf((red[32 + r] + red[40 + r] + red[48 + r] + red[56 + r]) * (1.f / 448.f) + 1e-5f);
    char* dst = ws + WS_XH + (size_t)(row >> 5) * 28672 + (row & 31) * 16;
    st16(ws, (dst - ws) + ((cc >> 1) * 1024 + (cc & 1) * 512), u32x4{pkh(v[0] * rstd, v[1] * rstd), pkh(v[2] * rstd, v[3] * rstd), pkh(v[4] * rstd, v[5] * rstd), pkh(v[6] * rstd, v[7] * rstd)});
    if (cc < 24)
      st16(ws, (dst - ws) + (((cc + 32) >> 1) * 1024 + (cc & 1) * 512), u32x4{pkh(v[8] * rstd, v[9] * rstd), pkh(v[10] * rstd, v[11] * rstd), pkh(v[12] * rstd, v[13] * rstd), pkh(v[14] * rstd, v[15] * rstd)});
    asm volatile("" :: "v"(cpf));
  } else if ((b -= K0_LN) < K0_KV) {
    const int g = (b >> 4) & 7, t = b & 15;
    const unsigned short* src = (b < 128 ? kc : vc) + ((size_t)g * 2048 + 1536 + 32 * t) * 256;
    u32x4 kv[4];
#pragma unroll
    for (int i = 0; i < 4; ++i) { const int n = tid + 256 * i, s = n >> 6, l = n & 63; kv[i] = *(const u32x4*)(src + (b < 128 ? (l & 31) * 256 + 16 * s + 8 * (l >> 5) : n * 8)); }
    if (b < 128) {
      char* dst = ws + WS_KT + ((size_t)g * 16 + t) * 16384;
#pragma unroll
      for (int i = 0; i < 4; ++i) st16(ws, (dst - ws) + ((tid + 256 * i) * 16), kv[i]);
    } else {
#pragma unroll
      for (int i = 0; i < 4; ++i) *(u32x4*)(sm + (tid + 256 * i) * 16) = kv[i];
      __syncthreads();
      char* dst = ws + WS_VT + ((size_t)g * 16 + t) * 16384;
      const unsigned short* sv = (const unsigned short*)sm;
#pragma unroll
      for (int i = 0; i < 4; ++i) {
        const int n = tid + 256 * i, l = n & 63, T = (n >> 6) & 7, kp = n >> 9;
        unsigned e[8];
#pragma unroll
        for (int j = 0; j < 8; ++j) e[j] = sv[(16 * kp + 8 * (j >> 2) + 4 * (l >> 5) + (j & 3)) * 256 + 32 * T + (l & 31)];
        st16(ws, (dst - ws) + (n * 16), u32x4{e[0] | (e[1] << 16), e[2] | (e[3] << 16), e[4] | (e[5] << 16), e[6] | (e[7] << 16)});
      }
    }
  }
  else if ((b -= K0_KV) < K0_WIN) {
    const int ks = b % 14, pj = b / 14;
    const unsigned short* src = win + (size_t)(32 * ks) * 1792 + ((pj >> 3) ? 0 : 896) + 112 * (pj & 7);
    const int n1 = min(tid + 256, 447);
    const u32x4 a0 = *(const u32x4*)(src + (tid / 14) * 1792 + (tid % 14) * 8), a1 = *(const u32x4*)(src + (n1 / 14) * 1792 + (n1 % 14) * 8);
    *(u32x4*)(sm + tid * 16) = a0;
    if (tid + 256 < 448) *(u32x4*)(sm + n1 * 16) = a1;
    __syncthreads();
    const unsigned short* pi = (const unsigned short*)sm;
#pragma unroll
    for (int i = 0; i < 2; ++i) {
      const int ct = w + 4 * i;
      if (ct < 7) {
        unsigned e[8];
#pragma unroll
        for (int j = 0; j < 8; ++j) e[j] = pi[(8 * (lane >> 4) + j) * 112 + 16 * ct + (lane & 15)];
        st16(ws, WS_WINF + ((size_t)(pj * 14 + ks) * 7 + ct) * 1024 + lane * 16, u32x4{e[0] | (e[1] << 16), e[2] | (e[3] << 16), e[4] | (e[5] << 16), e[6] | (e[7] << 16)});
      }
    }
  } else {
    b -= K0_WIN;
    const int ks = b % 28, jo = b / 28;
    *(u32x4*)(sm + tid * 16) = *(const u32x4*)(wout + (size_t)(32 * ks + (tid >> 3)) * 448 + 64 * jo + (tid & 7) * 8);
    __syncthreads();
    const unsigned short* po = (const unsigned short*)sm;
    unsigned e[8];
#pragma unroll
    for (int j = 0; j < 8; ++j) e[j] = po[(8 * (lane >> 4) + j) * 64 + 16 * w + (lane & 15)];
    st16(ws, WS_WOF + ((size_t)(jo * 28 + ks) * 4 + w) * 1024 + lane * 16, u32x4{e[0] | (e[1] << 16), e[2] | (e[3] << 16), e[4] | (e[5] << 16), e[6] | (e[7] << 16)});
  }
}

extern "C" __global__ __launch_bounds__(256, 1) void k1_attn(void* ws, const void* wq, const void* wo, const void* wsp, const void* win, const void* wout) {
  __shared__ __attribute__((aligned(16))) char lds[163840];
  const unsigned wid = __builtin_amdgcn_readfirstlane(threadIdx.x >> 6);
  const unsigned blk = blockIdx.x;
  const unsigned lb = (unsigned)(size_t)lds;
  asm volatile(
    "s_mov_b64 s[78:79], %0\n"
    "s_mov_b32 s94, %4\n"
    "s_mov_b32 s95, %5\n"
    "v_mbcnt_lo_u32_b32 v9, -1, 0\n"
    "v_mbcnt_hi_u32_b32 v9, -1, v9\n"
    "s_lshr_b32 s69, s95, 3\n"
    "s_lshr_b32 s98, s69, 1\n"
    "v_lshlrev_b32 v0, 4, v9\n"
    "s_lshl_b32 s70, s98, 2\n"
    "s_add_u32 s70, s70, s94\n"
    "s_mul_i32 s70, s70, 28672\n"
    "s_add_u32 s70, s70, 0x4000\n"
    "s_add_u32 s60, s78, s70\n"
    "s_addc_u32 s61, s79, 0\n"
    "s_and_b32 s61, s61, 0xffff\n"
    "s_mov_b32 s62, 28672\n"
    "s_mov_b32 s63, 0x20000\n"
    "buffer_load_dwordx4 a[128:131], v0, s[60:63], 0 offen offset:0\n"
    "buffer_load_dwordx4 a[132:135], v0, s[60:63], 0 offen offset:1024\n"
    "s_mov_b64 s[80:81], %1\n"
    "s_mov_b32 s71, %6\n"
    "s_and_b32 s96, s95, 7\n"
    "s_and_b32 s70, s69, 1\n"
    "s_lshl_b32 s97, s96, 1\n"
    "s_add_u32 s97, s97, s70\n"
    "s_lshl_b32 s70, s97, 9\n"
    "s_add_u32 s40, s80, s70\n"
    "s_addc_u32 s41, s81, 0\n"
    "s_and_b32 s41, s41, 0xffff\n"
    "s_mov_b32 s42, 3662336\n"
    "s_mov_b32 s43, 0x20000\n"
    "s_lshl_b32 s66, s94, 16\n"
    "s_lshl_b32 s67, s94, 12\n"
    "s_add_u32 s65, s71, s67\n"
    "v_lshrrev_b32 v10, 5, v9\n"
    "v_and_b32 v16, 31, v9\n"
    "v_add_u32 v17, 0, v10\n"
    "v_and_b32 v18, 3, v17\n"
    "v_lshlrev_b32 v18, 2, v18\n"
    "v_xor_b32 v18, v18, v16\n"
    "v_lshlrev_b32 v18, 4, v18\n"
    "v_lshl_add_u32 v80, v17, 13, v18\n"
    "v_add_u32 v17, 2, v10\n"
    "v_and_b32 v18, 3, v17\n"
    "v_lshlrev_b32 v18, 2, v18\n"
    "v_xor_b32 v18, v18, v16\n"
    "v_lshlrev_b32 v18, 4, v18\n"
    "v_lshl_add_u32 v81, v17, 13, v18\n"
    "v_subrev_u32 v81, 0x400, v81\n"
    "v_add_u32 v17, 4, v10\n"
    "v_and_b32 v18, 3, v17\n"
    "v_lshlrev_b32 v18, 2, v18\n"
    "v_xor_b32 v18, v18, v16\n"
    "v_lshlrev_b32 v18, 4, v18\n"
    "v_lshl_add_u32 v82, v17, 13, v18\n"
    "v_subrev_u32 v82, 0x800, v82\n"
    "v_add_u32 v17, 6, v10\n"
    "v_and_b32 v18, 3, v17\n"
    "v_lshlrev_b32 v18, 2, v18\n"
    "v_xor_b32 v18, v18, v16\n"
    "v_lshlrev_b32 v18, 4, v18\n"
    "v_lshl_add_u32 v83, v17, 13, v18\n"
    "v_subrev_u32 v83, 0xc00, v83\n"
    "s_add_u32 m0, s65, 0x0\n"
    "s_add_u32 s64, s66, 0x0\n"
    "s_nop 0\n"
    "buffer_load_dwordx4 v80, s[40:43], s64 offen offset:0 lds\n"
    "buffer_load_dwordx4 v81, s[40:43], s64 offen offset:1024 lds\n"
    "buffer_load_dwordx4 v82, s[40:43], s64 offen offset:2048 lds\n"
    "buffer_load_dwordx4 v83, s[40:43], s64 offen offset:3072 lds\n"
    "s_add_u32 s72, s78, 0\n"
    "s_addc_u32 s73, s79, 0\n"
    "s_and_b32 s73, s73, 0xffff\n"
    "s_mov_b32 s74, 1792\n"
    "s_mov_b32 s75, 0x20000\n"
    "s_lshl_b32 s69, s94, 10\n"
    "s_add_u32 m0, s71, s69\n"
    "s_add_u32 m0, m0, 0x20000\n"
    "s_nop 0\n"
    "buffer_load_dwordx4 v0, s[72:75], s69 offen offset:0 lds\n"
    "buffer_load_dwordx4 a[136:139], v0, s[60:63], 0 offen offset:2048\n"
    "s_add_u32 m0, s65, 0x4000\n"
    "s_add_u32 s64, s66, 0x40000\n"
    "s_nop 0\n"
    "buffer_load_dwordx4 v80, s[40:43], s64 offen offset:0 lds\n"
    "buffer_load_dwordx4 v81, s[40:43], s64 offen offset:1024 lds\n"
    "buffer_load_dwordx4 v82, s[40:43], s64 offen offset:2048 lds\n"
    "buffer_load_dwordx4 v83, s[40:43], s64 offen offset:3072 lds\n"
    "buffer_load_dwordx4 a[140:143], v0, s[60:63], 0 offen offset:3072\n"
    "s_mov_b64 s[82:83], %2\n"
    "v_lshrrev_b32 v17, 3, v9\n"
    "v_mul_u32_u24 v11, 0x380, v17\n"
    "v_bfe_u32 v18, v9, 4, 1\n"
    "v_lshlrev_b32 v18, 2, v18\n"
    "v_and_b32 v17, 7, v9\n"
    "v_xor_b32 v18, v18, v17\n"
    "v_lshl_add_u32 v11, v18, 4, v11\n"
    "v_add_u32 v12, 0x1800, v11\n"
    "v_add_u32 v13, 0x3000, v11\n"
    "v_add_u32 v14, 0x4800, v11\n"
    "s_lshl_b32 s70, s96, 18\n"
    "s_add_u32 s70, s70, 0x1c4000\n"
    "s_add_u32 s44, s78, s70\n"
    "s_addc_u32 s45, s79, 0\n"
    "s_and_b32 s45, s45, 0xffff\n"
    "s_mov_b32 s46, 262144\n"
    "s_mov_b32 s47, 0x20000\n"
    "s_lshl_b32 s70, s96, 18\n"
    "s_add_u32 s70, s70, 0x3c4000\n"
    "s_add_u32 s48, s78, s70\n"
    "s_addc_u32 s49, s79, 0\n"
    "s_and_b32 s49, s49, 0xffff\n"
    "s_mov_b32 s50, 262144\n"
    "s_mov_b32 s51, 0x20000\n"
    "s_mul_i32 s70, s97, 229376\n"
    "s_add_u32 s52, s82, s70\n"
    "s_addc_u32 s53, s83, 0\n"
    "s_and_b32 s53, s53, 0xffff\n"
    "s_mov_b32 s54, 229376\n"
    "s_mov_b32 s55, 0x20000\n"
    "s_lshl_b32 s69, s98, 4\n"
    "s_add_u32 s69, s69, s97\n"
    "s_lshl_b32 s69, s69, 2\n"
    "s_add_u32 s69, s69, s94\n"
    "s_mul_i32 s70, s69, 28672\n"
    "s_add_u32 s70, s70, 0x5c4000\n"
    "s_add_u32 s56, s78, s70\n"
    "s_addc_u32 s57, s79, 0\n"
    "s_and_b32 s57, s57, 0xffff\n"
    "s_mov_b32 s58, 28672\n"
    "s_mov_b32 s59, 0x20000\n"
    "s_mul_i32 s68, s94, 28672\n"
    "s_lshr_b32 s69, s95, 3\n"
    "s_lshl_b32 s69, s69, 2\n"
    "s_add_u32 s69, s69, s94\n"
    "v_lshrrev_b32 v9, 4, v0\n"
    "v_mov_b32 v107, 0x7ff00000\n"
    "s_mul_i32 s70, s69, 28\n"
    "v_add_u32 v10, s70, v9\n"
    "v_cmp_gt_u32 vcc, 28, v9\n"
    "v_lshrrev_b32 v105, 3, v10\n"
    "v_lshlrev_b32 v105, 13, v105\n"
    "v_and_b32 v106, 7, v10\n"
    "v_lshl_add_u32 v105, v106, 7, v105\n"
    "v_cndmask_b32 v105, v107, v105, vcc\n"
    "s_lshl_b32 s70, s96, 10\n"
    "s_add_u32 s24, s80, s70\n"
    "s_addc_u32 s25, s81, 0\n"
    "s_and_b32 s25, s25, 0xffff\n"
    "s_mov_b32 s26, 3662848\n"
    "s_mov_b32 s27, 0x20000\n"
    "buffer_load_dword a124, v105, s[24:27], 0 offen offset:0\n"
    "v_lshlrev_b32 v105, 7, v10\n"
    "v_cndmask_b32 v105, v107, v105, vcc\n"
    "v_accvgpr_write_b32 a117, v105\n"
    "s_lshl_b32 s99, s69, 11\n"
    "v_lshlrev_b32 v108, 7, v9\n"
    "v_cmp_gt_u32 vcc, 16, v9\n"
    "s_nop 1\n"
    "v_cndmask_b32 v108, v107, v108, vcc\n"
    "v_accvgpr_write_b32 a116, v108\n"
    "s_mul_i32 s70, s96, 458752\n"
    "s_add_u32 s28, s82, s70\n"
    "s_addc_u32 s29, s83, 0\n"
    "s_and_b32 s29, s29, 0xffff\n"
    "s_mov_b32 s30, 458752\n"
    "s_mov_b32 s31, 0x20000\n"
    "s_lshl_b32 s69, s95, 2\n"
    "s_add_u32 s69, s69, s94\n"
    "s_lshr_b32 s70, s69, 5\n"
    "s_lshl_b32 s70, s70, 18\n"
    "s_bfe_u32 s76, s69, 0x20003\n"
    "s_lshl_b32 s76, s76, 10\n"
    "s_add_u32 s70, s70, s76\n"
    "s_and_b32 s76, s69, 7\n"
    "s_lshl_b32 s76, s76, 7\n"
    "s_add_u32 s70, s70, s76\n"
    "s_mov_b64 s[24:25], %3\n"
    "s_add_u32 s24, s24, s70\n"
    "s_addc_u32 s25, s25, 0\n"
    "s_and_b32 s25, s25, 0xffff\n"
    "s_mov_b32 s26, 258176\n"
    "s_mov_b32 s27, 0x20000\n"
    "s_lshr_b32 s70, s69, 3\n"
    "s_lshl_b32 s70, s70, 16\n"
    "s_and_b32 s76, s69, 7\n"
    "s_lshl_b32 s76, s76, 13\n"
    "s_add_u32 s70, s70, s76\n"
    "s_mov_b64 s[88:89], %0\n"
    "s_add_u32 s70, s70, 0x2ebc000\n"
    "s_add_u32 s88, s88, s70\n"
    "s_addc_u32 s89, s89, 0\n"
    "s_and_b32 s89, s89, 0xffff\n"
    "s_mov_b32 s90, 8192\n"
    "s_mov_b32 s91, 0x20000\n"
    "s_mov_b32 s86, 0\n"
    "s_mov_b32 s87, 4096\n"
    "s_lshl_b32 s70, s94, 12\n"
    "s_add_u32 s84, s71, s70\n"
    "s_add_u32 s84, s84, 0x21000\n"
    "v_lshrrev_b32 v9, 4, v0\n"
    "v_lshrrev_b32 v105, 3, v9\n"
    "v_lshlrev_b32 v105, 12, v105\n"
    "v_lshrrev_b32 v106, 4, v9\n"
    "v_and_b32 v107, 7, v9\n"
    "v_add_u32 v10, 0, v106\n"
    "v_xor_b32 v10, v10, v107\n"
    "v_lshl_add_u32 v10, v10, 4, v105\n"
    "v_accvgpr_write_b32 a118, v10\n"
    "v_add_u32 v10, 4, v106\n"
    "v_xor_b32 v10, v10, v107\n"
    "v_lshl_add_u32 v10, v10, 4, v105\n"
    "v_accvgpr_write_b32 a119, v10\n"
    "v_and_b32 v105, 15, v9\n"
    "v_bfe_u32 v107, v105, 1, 3\n"
    "v_xor_b32 v107, v107, v106\n"
    "v_lshlrev_b32 v105, 7, v105\n"
    "v_lshl_add_u32 v105, v107, 4, v105\n"
    "v_add_u32 v105, s84, v105\n"
    "v_accvgpr_write_b32 a120, v105\n"
    "v_xor_b32 v105, 64, v105\n"
    "v_accvgpr_write_b32 a121, v105\n"
    "s_getpc_b64 s[72:73]\n"
    "L_pc_%=:\n"
    "s_lshr_b32 s69, s95, 3\n"
    "s_lshl_b32 s69, s69, 2\n"
    "s_add_u32 s69, s69, s94\n"
    "s_and_b32 s69, s69, 127\n"
    "s_lshl_b32 s69, s69, 10\n"
    "s_min_u32 s69, s69, L_end_%=-L_pc_%=-1024\n"
    "v_add_u32 v104, s69, v0\n"
    "global_load_dword v109, v104, s[72:73]\n"
    "v_add_u32 v1, s71, v0\n"
    "v_add_u32 v2, 0x10000, v1\n"
    "v_lshrrev_b32 v84, 9, v0\n"
    "v_lshlrev_b32 v84, 5, v84\n"
    "v_add_u32 v84, s71, v84\n"
    "v_add_u32 v84, 0x20000, v84\n"
    "v_lshrrev_b32 v9, 4, v0\n"
    "v_bfe_u32 v98, v9, 2, 2\n"
    "v_lshrrev_b32 v99, 5, v9\n"
    "v_lshl_add_u32 v99, v99, 2, v98\n"
    "v_lshlrev_b32 v99, 7, v99\n"
    "v_and_b32 v10, 3, v9\n"
    "v_lshl_add_u32 v99, v10, 3, v99\n"
    "v_bfe_u32 v10, v9, 4, 1\n"
    "v_lshl_add_u32 v99, v10, 5, v99\n"
    "v_add_u32 v99, s71, v99\n"
    "v_bfe_u32 v98, v98, 1, 1\n"
    "v_lshl_add_u32 v96, v98, 6, v99\n"
    "v_xor_b32 v98, 1, v98\n"
    "v_lshl_add_u32 v97, v98, 6, v99\n"
    "v_add_u32 v98, 0x10000, v96\n"
    "v_add_u32 v99, 0x10000, v97\n"
    "v_accvgpr_write_b32 a112, v96\n"
    "v_accvgpr_write_b32 a113, v97\n"
    "v_accvgpr_write_b32 a114, v98\n"
    "v_accvgpr_write_b32 a115, v99\n"
    "v_lshrrev_b32 v9, 4, v0\n"
    "v_bfe_u32 v76, v9, 2, 2\n"
    "v_and_b32 v77, 3, v9\n"
    "v_and_b32 v78, 1, v77\n"
    "v_lshlrev_b32 v78, 1, v78\n"
    "v_lshrrev_b32 v77, 1, v77\n"
    "v_or_b32 v77, v77, v78\n"
    "v_lshlrev_b32 v79, 3, v77\n"
    "v_bfe_u32 v78, v9, 4, 1\n"
    "v_lshl_add_u32 v79, v78, 5, v79\n"
    "v_lshrrev_b32 v78, 5, v9\n"
    "v_lshlrev_b32 v78, 3, v78\n"
    "v_add_u32 v78, v78, v76\n"
    "v_lshl_add_u32 v79, v78, 9, v79\n"
    "v_add_u32 v10, s71, v79\n"
    "v_mov_b32 v9, v76\n"
    "v_xor_b32 v72, 0, v9\n"
    "v_lshl_add_u32 v72, v72, 6, v10\n"
    "v_xor_b32 v73, 1, v9\n"
    "v_lshl_add_u32 v73, v73, 6, v10\n"
    "v_xor_b32 v74, 2, v9\n"
    "v_lshl_add_u32 v74, v74, 6, v10\n"
    "v_xor_b32 v75, 3, v9\n"
    "v_lshl_add_u32 v75, v75, 6, v10\n"
    "v_add_u32 v76, 0x10000, v72\n"
    "v_add_u32 v77, 0x10000, v73\n"
    "v_add_u32 v78, 0x10000, v74\n"
    "v_add_u32 v79, 0x10000, v75\n"
    "s_waitcnt vmcnt(3)\n"
    "s_barrier\n"
    "ds_read_b128 v[40:43], v84 offset:0\n"
    "ds_read_b128 v[44:47], v84 offset:16\n"
    "ds_read_b64_tr_b16 a[64:65], v72 offset:0\n"
    "ds_read_b64_tr_b16 a[66:67], v72 offset:2048\n"
    "ds_read_b64_tr_b16 a[68:69], v73 offset:0\n"
    "ds_read_b64_tr_b16 a[70:71], v73 offset:2048\n"
    "ds_read_b64_tr_b16 a[72:73], v74 offset:0\n"
    "ds_read_b64_tr_b16 a[74:75], v74 offset:2048\n"
    "ds_read_b64_tr_b16 a[76:77], v75 offset:0\n"
    "ds_read_b64_tr_b16 a[78:79], v75 offset:2048\n"
    "s_waitcnt lgkmcnt(8)\n"
    "v_accvgpr_read_b32 v16, a128\n"
    "v_accvgpr_read_b32 v17, a129\n"
    "v_accvgpr_read_b32 v18, a130\n"
    "v_accvgpr_read_b32 v19, a131\n"
    "v_fma_mix_f32 v24, v16, v40, v40 op_sel:[0,0,1] op_sel_hi:[1,1,1]\n"
    "v_fma_mix_f32 v25, v16, v41, v41 op_sel:[1,0,1] op_sel_hi:[1,1,1]\n"
    "v_fma_mix_f32 v26, v17, v42, v42 op_sel:[0,0,1] op_sel_hi:[1,1,1]\n"
    "v_fma_mix_f32 v27, v17, v43, v43 op_sel:[1,0,1] op_sel_hi:[1,1,1]\n"
    "v_fma_mix_f32 v28, v18, v44, v44 op_sel:[0,0,1] op_sel_hi:[1,1,1]\n"
    "v_fma_mix_f32 v29, v18, v45, v45 op_sel:[1,0,1] op_sel_hi:[1,1,1]\n"
    "v_fma_mix_f32 v30, v19, v46, v46 op_sel:[0,0,1] op_sel_hi:[1,1,1]\n"
    "v_fma_mix_f32 v31, v19, v47, v47 op_sel:[1,0,1] op_sel_hi:[1,1,1]\n"
    "v_cvt_pk_bf16_f32 v32, v24, v25\n"
    "v_cvt_pk_bf16_f32 v33, v26, v27\n"
    "v_cvt_pk_bf16_f32 v34, v28, v29\n"
    "v_cvt_pk_bf16_f32 v35, v30, v31\n"
    "ds_read_b128 v[56:59], v84 offset:64\n"
    "ds_read_b128 v[60:63], v84 offset:80\n"
    "s_waitcnt lgkmcnt(8)\n"
    "v_mfma_f32_32x32x16_bf16 v[128:143], a[64:67], v[32:35], 0\n"
    "s_waitcnt lgkmcnt(6)\n"
    "s_mov_b32 s77, 0x1000\n"
    "ds_read_b64_tr_b16 a[80:81], v72 offset:256\n"
    "ds_read_b64_tr_b16 a[82:83], v72 offset:2304\n"
    "buffer_load_dwordx4 a[144:147], v0, s[60:63], s77 offen offset:0\n"
    "v_mfma_f32_32x32x16_bf16 v[144:159], a[68:71], v[32:35], 0\n"
    "s_waitcnt lgkmcnt(6)\n"
    "s_add_u32 m0, s65, 0x8000\n"
    "ds_read_b64_tr_b16 a[84:85], v73 offset:256\n"
    "ds_read_b64_tr_b16 a[86:87], v73 offset:2304\n"
    "v_accvgpr_read_b32 v20, a132\n"
    "v_accvgpr_read_b32 v21, a133\n"
    "v_accvgpr_read_b32 v22, a134\n"
    "v_mfma_f32_32x32x16_bf16 v[160:175], a[72:75], v[32:35], 0\n"
    "s_waitcnt lgkmcnt(5)\n"
    "s_add_u32 s64, s66, 0x80000\n"
    "ds_read_b64_tr_b16 a[88:89], v74 offset:256\n"
    "ds_read_b64_tr_b16 a[90:91], v74 offset:2304\n"
    "v_accvgpr_read_b32 v23, a135\n"
    "v_fma_mix_f32 v24, v20, v56, v56 op_sel:[0,0,1] op_sel_hi:[1,1,1]\n"
    "v_fma_mix_f32 v25, v20, v57, v57 op_sel:[1,0,1] op_sel_hi:[1,1,1]\n"
    "v_mfma_f32_32x32x16_bf16 v[176:191], a[76:79], v[32:35], 0\n"
    "s_waitcnt lgkmcnt(4)\n"
    "ds_read_b64_tr_b16 a[92:93], v75 offset:256\n"
    "ds_read_b64_tr_b16 a[94:95], v75 offset:2304\n"
    "buffer_load_dwordx4 v80, s[40:43], s64 offen offset:0 lds\n"
    "v_fma_mix_f32 v26, v21, v58, v58 op_sel:[0,0,1] op_sel_hi:[1,1,1]\n"
    "v_fma_mix_f32 v27, v21, v59, v59 op_sel:[1,0,1] op_sel_hi:[1,1,1]\n"
    "v_mfma_f32_32x32x16_bf16 v[192:207], a[80:83], v[32:35], 0\n"
    "s_waitcnt lgkmcnt(4)\n"
    "ds_read_b64_tr_b16 a[64:65], v72 offset:8192\n"
    "ds_read_b64_tr_b16 a[66:67], v72 offset:10240\n"
    "ds_read_b128 v[40:43], v84 offset:128\n"
    "v_fma_mix_f32 v28, v22, v60, v60 op_sel:[0,0,1] op_sel_hi:[1,1,1]\n"
    "v_fma_mix_f32 v29, v22, v61, v61 op_sel:[1,0,1] op_sel_hi:[1,1,1]\n"
    "v_fma_mix_f32 v30, v23, v62, v62 op_sel:[0,0,1] op_sel_hi:[1,1,1]\n"
    "v_mfma_f32_32x32x16_bf16 v[208:223], a[84:87], v[32:35], 0\n"
    "s_waitcnt lgkmcnt(5)\n"
    "ds_read_b64_tr_b16 a[68:69], v73 offset:8192\n"
    "ds_read_b64_tr_b16 a[70:71], v73 offset:10240\n"
    "buffer_load_dwordx4 v81, s[40:43], s64 offen offset:1024 lds\n"
    "v_fma_mix_f32 v31, v23, v63, v63 op_sel:[1,0,1] op_sel_hi:[1,1,1]\n"
    "v_cvt_pk_bf16_f32 v36, v24, v25\n"
    "v_mfma_f32_32x32x16_bf16 v[224:239], a[88:91], v[32:35], 0\n"
    "s_waitcnt lgkmcnt(5)\n"
    "ds_read_b64_tr_b16 a[72:73], v74 offset:8192\n"
    "ds_read_b64_tr_b16 a[74:75], v74 offset:10240\n"
    "ds_read_b128 v[44:47], v84 offset:144\n"
    "v_cvt_pk_bf16_f32 v37, v26, v27\n"
    "v_cvt_pk_bf16_f32 v38, v28, v29\n"
    "v_cvt_pk_bf16_f32 v39, v30, v31\n"
    "v_mfma_f32_32x32x16_bf16 v[240:255], a[92:95], v[32:35], 0\n"
    "s_waitcnt lgkmcnt(6)\n"
    "ds_read_b64_tr_b16 a[76:77], v75 offset:8192\n"
    "ds_read_b64_tr_b16 a[78:79], v75 offset:10240\n"
    "buffer_load_dwordx4 v82, s[40:43], s64 offen offset:2048 lds\n"
    "v_mfma_f32_32x32x16_bf16 v[128:143], a[64:67], v[36:39], v[128:143]\n"
    "s_waitcnt lgkmcnt(5)\n"
    "ds_read_b64_tr_b16 a[80:81], v72 offset:8448\n"
    "ds_read_b64_tr_b16 a[82:83], v72 offset:10496\n"
    "v_mfma_f32_32x32x16_bf16 v[144:159], a[68:71], v[36:39], v[144:159]\n"
    "s_waitcnt lgkmcnt(5)\n"
    "ds_read_b64_tr_b16 a[84:85], v73 offset:8448\n"
    "ds_read_b64_tr_b16 a[86:87], v73 offset:10496\n"
    "buffer_load_dwordx4 v83, s[40:43], s64 offen offset:3072 lds\n"
    "v_accvgpr_read_b32 v16, a136\n"
    "v_accvgpr_read_b32 v17, a137\n"
    "v_mfma_f32_32x32x16_bf16 v[160:175], a[72:75], v[36:39], v[160:175]\n"
    "s_waitcnt lgkmcnt(4)\n"
    "s_add_u32 m0, s65, 0xc000\n"
    "ds_read_b64_tr_b16 a[88:89], v74 offset:8448\n"
    "ds_read_b64_tr_b16 a[90:91], v74 offset:10496\n"
    "v_accvgpr_read_b32 v18, a138\n"
    "v_accvgpr_read_b32 v19, a139\n"
    "v_fma_mix_f32 v24, v16, v40, v40 op_sel:[0,0,1] op_sel_hi:[1,1,1]\n"
    "v_mfma_f32_32x32x16_bf16 v[176:191], a[76:79], v[36:39], v[176:191]\n"
    "s_waitcnt lgkmcnt(4)\n"
    "s_add_u32 s64, s66, 0xc0000\n"
    "ds_read_b64_tr_b16 a[92:93], v75 offset:8448\n"
    "ds_read_b64_tr_b16 a[94:95], v75 offset:10496\n"
    "v_fma_mix_f32 v25, v16, v41, v41 op_sel:[1,0,1] op_sel_hi:[1,1,1]\n"
    "v_fma_mix_f32 v26, v17, v42, v42 op_sel:[0,0,1] op_sel_hi:[1,1,1]\n"
    "v_fma_mix_f32 v27, v17, v43, v43 op_sel:[1,0,1] op_sel_hi:[1,1,1]\n"
    "v_mfma_f32_32x32x16_bf16 v[192:207], a[80:83], v[36:39], v[192:207]\n"
    "s_waitcnt lgkmcnt(4)\n"
    "ds_read_b64_tr_b16 a[64:65], v72 offset:16384\n"
    "ds_read_b64_tr_b16 a[66:67], v72 offset:18432\n"
    "buffer_load_dwordx4 v80, s[40:43], s64 offen offset:0 lds\n"
    "ds_read_b128 v[56:59], v84 offset:192\n"
    "v_fma_mix_f32 v28, v18, v44, v44 op_sel:[0,0,1] op_sel_hi:[1,1,1]\n"
    "v_mfma_f32_32x32x16_bf16 v[208:223], a[84:87], v[36:39], v[208:223]\n"
    "s_waitcnt lgkmcnt(5)\n"
    "ds_read_b64_tr_b16 a[68:69], v73 offset:16384\n"
    "ds_read_b64_tr_b16 a[70:71], v73 offset:18432\n"
    "v_fma_mix_f32 v29, v18, v45, v45 op_sel:[1,0,1] op_sel_hi:[1,1,1]\n"
    "v_fma_mix_f32 v30, v19, v46, v46 op_sel:[0,0,1] op_sel_hi:[1,1,1]\n"
    "v_fma_mix_f32 v31, v19, v47, v47 op_sel:[1,0,1] op_sel_hi:[1,1,1]\n"
    "v_cvt_pk_bf16_f32 v32, v24, v25\n"
    "v_mfma_f32_32x32x16_bf16 v[224:239], a[88:91], v[36:39], v[224:239]\n"
    "s_waitcnt lgkmcnt(5)\n"
    "ds_read_b64_tr_b16 a[72:73], v74 offset:16384\n"
    "ds_read_b64_tr_b16 a[74:75], v74 offset:18432\n"
    "buffer_load_dwordx4 v81, s[40:43], s64 offen offset:1024 lds\n"
    "ds_read_b128 v[60:63], v84 offset:208\n"
    "v_cvt_pk_bf16_f32 v33, v26, v27\n"
    "v_cvt_pk_bf16_f32 v34, v28, v29\n"
    "v_cvt_pk_bf16_f32 v35, v30, v31\n"
    "v_mfma_f32_32x32x16_bf16 v[240:255], a[92:95], v[36:39], v[240:255]\n"
    "s_waitcnt lgkmcnt(6)\n"
    "ds_read_b64_tr_b16 a[76:77], v75 offset:16384\n"
    "ds_read_b64_tr_b16 a[78:79], v75 offset:18432\n"
    "v_mfma_f32_32x32x16_bf16 v[128:143], a[64:67], v[32:35], v[128:143]\n"
    "s_waitcnt lgkmcnt(5)\n"
    "ds_read_b64_tr_b16 a[80:81], v72 offset:16640\n"
    "ds_read_b64_tr_b16 a[82:83], v72 offset:18688\n"
    "buffer_load_dwordx4 v82, s[40:43], s64 offen offset:2048 lds\n"
    "v_mfma_f32_32x32x16_bf16 v[144:159], a[68:71], v[32:35], v[144:159]\n"
    "s_waitcnt vmcnt(10) lgkmcnt(5)\n"
    "ds_read_b64_tr_b16 a[84:85], v73 offset:16640\n"
    "ds_read_b64_tr_b16 a[86:87], v73 offset:18688\n"
    "v_accvgpr_read_b32 v20, a140\n"
    "v_accvgpr_read_b32 v21, a141\n"
    "v_accvgpr_read_b32 v22, a142\n"
    "v_accvgpr_read_b32 v23, a143\n"
    "v_mfma_f32_32x32x16_bf16 v[160:175], a[72:75], v[32:35], v[160:175]\n"
    "s_waitcnt lgkmcnt(4)\n"
    "ds_read_b64_tr_b16 a[88:89], v74 offset:16640\n"
    "ds_read_b64_tr_b16 a[90:91], v74 offset:18688\n"
    "buffer_load_dwordx4 v83, s[40:43], s64 offen offset:3072 lds\n"
    "v_fma_mix_f32 v24, v20, v56, v56 op_sel:[0,0,1] op_sel_hi:[1,1,1]\n"
    "v_fma_mix_f32 v25, v20, v57, v57 op_sel:[1,0,1] op_sel_hi:[1,1,1]\n"
    "v_mfma_f32_32x32x16_bf16 v[176:191], a[76:79], v[32:35], v[176:191]\n"
    "s_waitcnt lgkmcnt(4)\n"
    "ds_read_b64_tr_b16 a[92:93], v75 offset:16640\n"
    "ds_read_b64_tr_b16 a[94:95], v75 offset:18688\n"
    "v_fma_mix_f32 v26, v21, v58, v58 op_sel:[0,0,1] op_sel_hi:[1,1,1]\n"
    "v_fma_mix_f32 v27, v21, v59, v59 op_sel:[1,0,1] op_sel_hi:[1,1,1]\n"
    "v_fma_mix_f32 v28, v22, v60, v60 op_sel:[0,0,1] op_sel_hi:[1,1,1]\n"
    "v_fma_mix_f32 v29, v22, v61, v61 op_sel:[1,0,1] op_sel_hi:[1,1,1]\n"
    "v_mfma_f32_32x32x16_bf16 v[192:207], a[80:83], v[32:35], v[192:207]\n"
    "s_waitcnt lgkmcnt(4)\n"
    "s_mov_b32 s77, 0x1400\n"
    "ds_read_b64_tr_b16 a[64:65], v72 offset:24576\n"
    "ds_read_b64_tr_b16 a[66:67], v72 offset:26624\n"
    "buffer_load_dwordx4 a[148:151], v0, s[60:63], s77 offen offset:0\n"
    "ds_read_b128 v[40:43], v84 offset:256\n"
    "v_mfma_f32_32x32x16_bf16 v[208:223], a[84:87], v[32:35], v[208:223]\n"
    "s_waitcnt lgkmcnt(5)\n"
    "ds_read_b64_tr_b16 a[68:69], v73 offset:24576\n"
    "ds_read_b64_tr_b16 a[70:71], v73 offset:26624\n"
    "v_fma_mix_f32 v30, v23, v62, v62 op_sel:[0,0,1] op_sel_hi:[1,1,1]\n"
    "v_fma_mix_f32 v31, v23, v63, v63 op_sel:[1,0,1] op_sel_hi:[1,1,1]\n"
    "v_cvt_pk_bf16_f32 v36, v24, v25\n"
    "v_cvt_pk_bf16_f32 v37, v26, v27\n"
    "v_mfma_f32_32x32x16_bf16 v[224:239], a[88:91], v[32:35], v[224:239]\n"
    "s_waitcnt lgkmcnt(5)\n"
    "s_mov_b32 s77, 0x1800\n"
    "ds_read_b64_tr_b16 a[72:73], v74 offset:24576\n"
    "ds_read_b64_tr_b16 a[74:75], v74 offset:26624\n"
    "buffer_load_dwordx4 a[152:155], v0, s[60:63], s77 offen offset:0\n"
    "ds_read_b128 v[44:47], v84 offset:272\n"
    "v_cvt_pk_bf16_f32 v38, v28, v29\n"
    "v_cvt_pk_bf16_f32 v39, v30, v31\n"
    "v_mfma_f32_32x32x16_bf16 v[240:255], a[92:95], v[32:35], v[240:255]\n"
    "s_waitcnt lgkmcnt(6)\n"
    "ds_read_b64_tr_b16 a[76:77], v75 offset:24576\n"
    "ds_read_b64_tr_b16 a[78:79], v75 offset:26624\n"
    "v_mfma_f32_32x32x16_bf16 v[128:143], a[64:67], v[36:39], v[128:143]\n"
    "s_waitcnt lgkmcnt(5)\n"
    "s_mov_b32 s77, 0x1c00\n"
    "ds_read_b64_tr_b16 a[80:81], v72 offset:24832\n"
    "ds_read_b64_tr_b16 a[82:83], v72 offset:26880\n"
    "buffer_load_dwordx4 a[156:159], v0, s[60:63], s77 offen offset:0\n"
    "v_mfma_f32_32x32x16_bf16 v[144:159], a[68:71], v[36:39], v[144:159]\n"
    "s_waitcnt vmcnt(11) lgkmcnt(5)\n"
    "ds_read_b64_tr_b16 a[84:85], v73 offset:24832\n"
    "ds_read_b64_tr_b16 a[86:87], v73 offset:26880\n"
    "v_accvgpr_read_b32 v16, a144\n"
    "v_accvgpr_read_b32 v17, a145\n"
    "v_accvgpr_read_b32 v18, a146\n"
    "v_accvgpr_read_b32 v19, a147\n"
    "v_mfma_f32_32x32x16_bf16 v[160:175], a[72:75], v[36:39], v[160:175]\n"
    "s_waitcnt lgkmcnt(4)\n"
    "s_mov_b32 s77, 0x2000\n"
    "ds_read_b64_tr_b16 a[88:89], v74 offset:24832\n"
    "ds_read_b64_tr_b16 a[90:91], v74 offset:26880\n"
    "buffer_load_dwordx4 a[160:163], v0, s[60:63], s77 offen offset:0\n"
    "v_fma_mix_f32 v24, v16, v40, v40 op_sel:[0,0,1] op_sel_hi:[1,1,1]\n"
    "v_mfma_f32_32x32x16_bf16 v[176:191], a[76:79], v[36:39], v[176:191]\n"
    "s_waitcnt lgkmcnt(4)\n"
    "s_add_u32 m0, s65, 0x10000\n"
    "ds_read_b64_tr_b16 a[92:93], v75 offset:24832\n"
    "ds_read_b64_tr_b16 a[94:95], v75 offset:26880\n"
    "v_fma_mix_f32 v25, v16, v41, v41 op_sel:[1,0,1] op_sel_hi:[1,1,1]\n"
    "v_fma_mix_f32 v26, v17, v42, v42 op_sel:[0,0,1] op_sel_hi:[1,1,1]\n"
    "v_fma_mix_f32 v27, v17, v43, v43 op_sel:[1,0,1] op_sel_hi:[1,1,1]\n"
    "v_mfma_f32_32x32x16_bf16 v[192:207], a[80:83], v[36:39], v[192:207]\n"
    "s_waitcnt vmcnt(4) lgkmcnt(4)\n"
    "s_barrier\n"
    "s_add_u32 s64, s66, 0x100000\n"
    "ds_read_b64_tr_b16 a[64:65], v72 offset:32768\n"
    "ds_read_b64_tr_b16 a[66:67], v72 offset:34816\n"
    "ds_read_b128 v[56:59], v84 offset:320\n"
    "v_fma_mix_f32 v28, v18, v44, v44 op_sel:[0,0,1] op_sel_hi:[1,1,1]\n"
    "v_mfma_f32_32x32x16_bf16 v[208:223], a[84:87], v[36:39], v[208:223]\n"
    "s_waitcnt lgkmcnt(5)\n"
    "ds_read_b64_tr_b16 a[68:69], v73 offset:32768\n"
    "ds_read_b64_tr_b16 a[70:71], v73 offset:34816\n"
    "buffer_load_dwordx4 v80, s[40:43], s64 offen offset:0 lds\n"
    "v_fma_mix_f32 v29, v18, v45, v45 op_sel:[1,0,1] op_sel_hi:[1,1,1]\n"
    "v_fma_mix_f32 v30, v19, v46, v46 op_sel:[0,0,1] op_sel_hi:[1,1,1]\n"
    "v_mfma_f32_32x32x16_bf16 v[224:239], a[88:91], v[36:39], v[224:239]\n"
    "s_waitcnt lgkmcnt(5)\n"
    "ds_read_b64_tr_b16 a[72:73], v74 offset:32768\n"
    "ds_read_b64_tr_b16 a[74:75], v74 offset:34816\n"
    "ds_read_b128 v[60:63], v84 offset:336\n"
    "v_fma_mix_f32 v31, v19, v47, v47 op_sel:[1,0,1] op_sel_hi:[1,1,1]\n"
    "v_cvt_pk_bf16_f32 v32, v24, v25\n"
    "v_cvt_pk_bf16_f32 v33, v26, v27\n"
    "v_cvt_pk_bf16_f32 v34, v28, v29\n"
    "v_cvt_pk_bf16_f32 v35, v30, v31\n"
    "v_mfma_f32_32x32x16_bf16 v[240:255], a[92:95], v[36:39], v[240:255]\n"
    "s_waitcnt lgkmcnt(6)\n"
    "ds_read_b64_tr_b16 a[76:77], v75 offset:32768\n"
    "ds_read_b64_tr_b16 a[78:79], v75 offset:34816\n"
    "buffer_load_dwordx4 v81, s[40:43], s64 offen offset:1024 lds\n"
    "v_mfma_f32_32x32x16_bf16 v[128:143], a[64:67], v[32:35], v[128:143]\n"
    "s_waitcnt lgkmcnt(5)\n"
    "ds_read_b64_tr_b16 a[80:81], v72 offset:33024\n"
    "ds_read_b64_tr_b16 a[82:83], v72 offset:35072\n"
    "v_mfma_f32_32x32x16_bf16 v[144:159], a[68:71], v[32:35], v[144:159]\n"
    "s_waitcnt vmcnt(5) lgkmcnt(5)\n"
    "ds_read_b64_tr_b16 a[84:85], v73 offset:33024\n"
    "ds_read_b64_tr_b16 a[86:87], v73 offset:35072\n"
    "buffer_load_dwordx4 v82, s[40:43], s64 offen offset:2048 lds\n"
    "v_accvgpr_read_b32 v20, a148\n"
    "v_accvgpr_read_b32 v21, a149\n"
    "v_mfma_f32_32x32x16_bf16 v[160:175], a[72:75], v[32:35], v[160:175]\n"
    "s_waitcnt lgkmcnt(4)\n"
    "ds_read_b64_tr_b16 a[88:89], v74 offset:33024\n"
    "ds_read_b64_tr_b16 a[90:91], v74 offset:35072\n"
    "v_accvgpr_read_b32 v22, a150\n"
    "v_accvgpr_read_b32 v23, a151\n"
    "v_fma_mix_f32 v24, v20, v56, v56 op_sel:[0,0,1] op_sel_hi:[1,1,1]\n"
    "v_fma_mix_f32 v25, v20, v57, v57 op_sel:[1,0,1] op_sel_hi:[1,1,1]\n"
    "v_mfma_f32_32x32x16_bf16 v[176:191], a[76:79], v[32:35], v[176:191]\n"
    "s_waitcnt lgkmcnt(4)\n"
    "ds_read_b64_tr_b16 a[92:93], v75 offset:33024\n"
    "ds_read_b64_tr_b16 a[94:95], v75 offset:35072\n"
    "buffer_load_dwordx4 v83, s[40:43], s64 offen offset:3072 lds\n"
    "v_fma_mix_f32 v26, v21, v58, v58 op_sel:[0,0,1] op_sel_hi:[1,1,1]\n"
    "v_fma_mix_f32 v27, v21, v59, v59 op_sel:[1,0,1] op_sel_hi:[1,1,1]\n"
    "v_mfma_f32_32x32x16_bf16 v[192:207], a[80:83], v[32:35], v[192:207]\n"
    "s_waitcnt lgkmcnt(4)\n"
    "s_add_u32 m0, s65, 0x14000\n"
    "ds_read_b64_tr_b16 a[64:65], v72 offset:40960\n"
    "ds_read_b64_tr_b16 a[66:67], v72 offset:43008\n"
    "ds_read_b128 v[40:43], v84 offset:384\n"
    "v_fma_mix_f32 v28, v22, v60, v60 op_sel:[0,0,1] op_sel_hi:[1,1,1]\n"
    "v_fma_mix_f32 v29, v22, v61, v61 op_sel:[1,0,1] op_sel_hi:[1,1,1]\n"
    "v_mfma_f32_32x32x16_bf16 v[208:223], a[84:87], v[32:35], v[208:223]\n"
    "s_waitcnt lgkmcnt(5)\n"
    "s_add_u32 s64, s66, 0x140000\n"
    "ds_read_b64_tr_b16 a[68:69], v73 offset:40960\n"
    "ds_read_b64_tr_b16 a[70:71], v73 offset:43008\n"
    "v_fma_mix_f32 v30, v23, v62, v62 op_sel:[0,0,1] op_sel_hi:[1,1,1]\n"
    "v_fma_mix_f32 v31, v23, v63, v63 op_sel:[1,0,1] op_sel_hi:[1,1,1]\n"
    "v_cvt_pk_bf16_f32 v36, v24, v25\n"
    "v_mfma_f32_32x32x16_bf16 v[224:239], a[88:91], v[32:35], v[224:239]\n"
    "s_waitcnt lgkmcnt(5)\n"
    "ds_read_b64_tr_b16 a[72:73], v74 offset:40960\n"
    "ds_read_b64_tr_b16 a[74:75], v74 offset:43008\n"
    "buffer_load_dwordx4 v80, s[40:43], s64 offen offset:0 lds\n"
    "ds_read_b128 v[44:47], v84 offset:400\n"
    "v_cvt_pk_bf16_f32 v37, v26, v27\n"
    "v_cvt_pk_bf16_f32 v38, v28, v29\n"
    "v_cvt_pk_bf16_f32 v39, v30, v31\n"
    "v_mfma_f32_32x32x16_bf16 v[240:255], a[92:95], v[32:35], v[240:255]\n"
    "s_waitcnt lgkmcnt(6)\n"
    "ds_read_b64_tr_b16 a[76:77], v75 offset:40960\n"
    "ds_read_b64_tr_b16 a[78:79], v75 offset:43008\n"
    "v_mfma_f32_32x32x16_bf16 v[128:143], a[64:67], v[36:39], v[128:143]\n"
    "s_waitcnt lgkmcnt(5)\n"
    "ds_read_b64_tr_b16 a[80:81], v72 offset:41216\n"
    "ds_read_b64_tr_b16 a[82:83], v72 offset:43264\n"
    "buffer_load_dwordx4 v81, s[40:43], s64 offen offset:1024 lds\n"
    "v_mfma_f32_32x32x16_bf16 v[144:159], a[68:71], v[36:39], v[144:159]\n"
    "s_waitcnt vmcnt(8) lgkmcnt(5)\n"
    "ds_read_b64_tr_b16 a[84:85], v73 offset:41216\n"
    "ds_read_b64_tr_b16 a[86:87], v73 offset:43264\n"
    "v_accvgpr_read_b32 v16, a152\n"
    "v_accvgpr_read_b32 v17, a153\n"
    "v_accvgpr_read_b32 v18, a154\n"
    "v_accvgpr_read_b32 v19, a155\n"
    "v_mfma_f32_32x32x16_bf16 v[160:175], a[72:75], v[36:39], v[160:175]\n"
    "s_waitcnt lgkmcnt(4)\n"
    "ds_read_b64_tr_b16 a[88:89], v74 offset:41216\n"
    "ds_read_b64_tr_b16 a[90:91], v74 offset:43264\n"
    "buffer_load_dwordx4 v82, s[40:43], s64 offen offset:2048 lds\n"
    "v_fma_mix_f32 v24, v16, v40, v40 op_sel:[0,0,1] op_sel_hi:[1,1,1]\n"
    "v_fma_mix_f32 v25, v16, v41, v41 op_sel:[1,0,1] op_sel_hi:[1,1,1]\n"
    "v_mfma_f32_32x32x16_bf16 v[176:191], a[76:79], v[36:39], v[176:191]\n"
    "s_waitcnt lgkmcnt(4)\n"
    "ds_read_b64_tr_b16 a[92:93], v75 offset:41216\n"
    "ds_read_b64_tr_b16 a[94:95], v75 offset:43264\n"
    "v_fma_mix_f32 v26, v17, v42, v42 op_sel:[0,0,1] op_sel_hi:[1,1,1]\n"
    "v_fma_mix_f32 v27, v17, v43, v43 op_sel:[1,0,1] op_sel_hi:[1,1,1]\n"
    "v_fma_mix_f32 v28, v18, v44, v44 op_sel:[0,0,1] op_sel_hi:[1,1,1]\n"
    "v_fma_mix_f32 v29, v18, v45, v45 op_sel:[1,0,1] op_sel_hi:[1,1,1]\n"
    "v_mfma_f32_32x32x16_bf16 v[192:207], a[80:83], v[36:39], v[192:207]\n"
    "s_waitcnt lgkmcnt(4)\n"
    "ds_read_b64_tr_b16 a[64:65], v72 offset:49152\n"
    "ds_read_b64_tr_b16 a[66:67], v72 offset:51200\n"
    "buffer_load_dwordx4 v83, s[40:43], s64 offen offset:3072 lds\n"
    "ds_read_b128 v[56:59], v84 offset:448\n"
    "v_fma_mix_f32 v30, v19, v46, v46 op_sel:[0,0,1] op_sel_hi:[1,1,1]\n"
    "v_mfma_f32_32x32x16_bf16 v[208:223], a[84:87], v[36:39], v[208:223]\n"
    "s_waitcnt lgkmcnt(5)\n"
    "ds_read_b64_tr_b16 a[68:69], v73 offset:49152\n"
    "ds_read_b64_tr_b16 a[70:71], v73 offset:51200\n"
    "v_fma_mix_f32 v31, v19, v47, v47 op_sel:[1,0,1] op_sel_hi:[1,1,1]\n"
    "v_cvt_pk_bf16_f32 v32, v24, v25\n"
    "v_cvt_pk_bf16_f32 v33, v26, v27\n"
    "v_cvt_pk_bf16_f32 v34, v28, v29\n"
    "v_mfma_f32_32x32x16_bf16 v[224:239], a[88:91], v[36:39], v[224:239]\n"
    "s_waitcnt lgkmcnt(5)\n"
    "s_mov_b32 s77, 0x2400\n"
    "ds_read_b64_tr_b16 a[72:73], v74 offset:49152\n"
    "ds_read_b64_tr_b16 a[74:75], v74 offset:51200\n"
    "buffer_load_dwordx4 a[164:167], v0, s[60:63], s77 offen offset:0\n"
    "ds_read_b128 v[60:63], v84 offset:464\n"
    "v_cvt_pk_bf16_f32 v35, v30, v31\n"
    "v_mfma_f32_32x32x16_bf16 v[240:255], a[92:95], v[36:39], v[240:255]\n"
    "s_waitcnt lgkmcnt(6)\n"
    "ds_read_b64_tr_b16 a[76:77], v75 offset:49152\n"
    "ds_read_b64_tr_b16 a[78:79], v75 offset:51200\n"
    "v_mfma_f32_32x32x16_bf16 v[128:143], a[64:67], v[32:35], v[128:143]\n"
    "s_waitcnt lgkmcnt(5)\n"
    "s_mov_b32 s77, 0x2800\n"
    "ds_read_b64_tr_b16 a[80:81], v72 offset:49408\n"
    "ds_read_b64_tr_b16 a[82:83], v72 offset:51456\n"
    "buffer_load_dwordx4 a[168:171], v0, s[60:63], s77 offen offset:0\n"
    "v_mfma_f32_32x32x16_bf16 v[144:159], a[68:71], v[32:35], v[144:159]\n"
    "s_waitcnt vmcnt(11) lgkmcnt(5)\n"
    "ds_read_b64_tr_b16 a[84:85], v73 offset:49408\n"
    "ds_read_b64_tr_b16 a[86:87], v73 offset:51456\n"
    "v_accvgpr_read_b32 v20, a156\n"
    "v_accvgpr_read_b32 v21, a157\n"
    "v_accvgpr_read_b32 v22, a158\n"
    "v_accvgpr_read_b32 v23, a159\n"
    "v_mfma_f32_32x32x16_bf16 v[160:175], a[72:75], v[32:35], v[160:175]\n"
    "s_waitcnt lgkmcnt(4)\n"
    "s_mov_b32 s77, 0x2c00\n"
    "ds_read_b64_tr_b16 a[88:89], v74 offset:49408\n"
    "ds_read_b64_tr_b16 a[90:91], v74 offset:51456\n"
    "buffer_load_dwordx4 a[172:175], v0, s[60:63], s77 offen offset:0\n"
    "v_fma_mix_f32 v24, v20, v56, v56 op_sel:[0,0,1] op_sel_hi:[1,1,1]\n"
    "v_mfma_f32_32x32x16_bf16 v[176:191], a[76:79], v[32:35], v[176:191]\n"
    "s_waitcnt lgkmcnt(4)\n"
    "ds_read_b64_tr_b16 a[92:93], v75 offset:49408\n"
    "ds_read_b64_tr_b16 a[94:95], v75 offset:51456\n"
    "v_fma_mix_f32 v25, v20, v57, v57 op_sel:[1,0,1] op_sel_hi:[1,1,1]\n"
    "v_fma_mix_f32 v26, v21, v58, v58 op_sel:[0,0,1] op_sel_hi:[1,1,1]\n"
    "v_fma_mix_f32 v27, v21, v59, v59 op_sel:[1,0,1] op_sel_hi:[1,1,1]\n"
    "v_fma_mix_f32 v28, v22, v60, v60 op_sel:[0,0,1] op_sel_hi:[1,1,1]\n"
    "v_mfma_f32_32x32x16_bf16 v[192:207], a[80:83], v[32:35], v[192:207]\n"
    "s_waitcnt lgkmcnt(4)\n"
    "s_mov_b32 s77, 0x3000\n"
    "ds_read_b64_tr_b16 a[64:65], v72 offset:57344\n"
    "ds_read_b64_tr_b16 a[66:67], v72 offset:59392\n"
    "buffer_load_dwordx4 a[176:179], v0, s[60:63], s77 offen offset:0\n"
    "ds_read_b128 v[40:43], v84 offset:512\n"
    "v_mfma_f32_32x32x16_bf16 v[208:223], a[84:87], v[32:35], v[208:223]\n"
    "s_waitcnt lgkmcnt(5)\n"
    "s_add_u32 m0, s65, 0x18000\n"
    "ds_read_b64_tr_b16 a[68:69], v73 offset:57344\n"
    "ds_read_b64_tr_b16 a[70:71], v73 offset:59392\n"
    "v_fma_mix_f32 v29, v22, v61, v61 op_sel:[1,0,1] op_sel_hi:[1,1,1]\n"
    "v_fma_mix_f32 v30, v23, v62, v62 op_sel:[0,0,1] op_sel_hi:[1,1,1]\n"
    "v_fma_mix_f32 v31, v23, v63, v63 op_sel:[1,0,1] op_sel_hi:[1,1,1]\n"
    "v_mfma_f32_32x32x16_bf16 v[224:239], a[88:91], v[32:35], v[224:239]\n"
    "s_waitcnt lgkmcnt(5)\n"
    "s_add_u32 s64, s66, 0x180000\n"
    "ds_read_b64_tr_b16 a[72:73], v74 offset:57344\n"
    "ds_read_b64_tr_b16 a[74:75], v74 offset:59392\n"
    "ds_read_b128 v[44:47], v84 offset:528\n"
    "v_cvt_pk_bf16_f32 v36, v24, v25\n"
    "v_cvt_pk_bf16_f32 v37, v26, v27\n"
    "v_cvt_pk_bf16_f32 v38, v28, v29\n"
    "v_cvt_pk_bf16_f32 v39, v30, v31\n"
    "v_mfma_f32_32x32x16_bf16 v[240:255], a[92:95], v[32:35], v[240:255]\n"
    "s_waitcnt lgkmcnt(6)\n"
    "ds_read_b64_tr_b16 a[76:77], v75 offset:57344\n"
    "ds_read_b64_tr_b16 a[78:79], v75 offset:59392\n"
    "buffer_load_dwordx4 v80, s[40:43], s64 offen offset:0 lds\n"
    "v_mfma_f32_32x32x16_bf16 v[128:143], a[64:67], v[36:39], v[128:143]\n"
    "s_waitcnt lgkmcnt(5)\n"
    "ds_read_b64_tr_b16 a[80:81], v72 offset:57600\n"
    "ds_read_b64_tr_b16 a[82:83], v72 offset:59648\n"
    "v_mfma_f32_32x32x16_bf16 v[144:159], a[68:71], v[36:39], v[144:159]\n"
    "s_waitcnt vmcnt(13) lgkmcnt(5)\n"
    "ds_read_b64_tr_b16 a[84:85], v73 offset:57600\n"
    "ds_read_b64_tr_b16 a[86:87], v73 offset:59648\n"
    "buffer_load_dwordx4 v81, s[40:43], s64 offen offset:1024 lds\n"
    "v_accvgpr_read_b32 v16, a160\n"
    "v_accvgpr_read_b32 v17, a161\n"
    "v_mfma_f32_32x32x16_bf16 v[160:175], a[72:75], v[36:39], v[160:175]\n"
    "s_waitcnt lgkmcnt(4)\n"
    "ds_read_b64_tr_b16 a[88:89], v74 offset:57600\n"
    "ds_read_b64_tr_b16 a[90:91], v74 offset:59648\n"
    "v_accvgpr_read_b32 v18, a162\n"
    "v_accvgpr_read_b32 v19, a163\n"
    "v_fma_mix_f32 v24, v16, v40, v40 op_sel:[0,0,1] op_sel_hi:[1,1,1]\n"
    "v_fma_mix_f32 v25, v16, v41, v41 op_sel:[1,0,1] op_sel_hi:[1,1,1]\n"
    "v_mfma_f32_32x32x16_bf16 v[176:191], a[76:79], v[36:39], v[176:191]\n"
    "s_waitcnt lgkmcnt(4)\n"
    "ds_read_b64_tr_b16 a[92:93], v75 offset:57600\n"
    "ds_read_b64_tr_b16 a[94:95], v75 offset:59648\n"
    "buffer_load_dwordx4 v82, s[40:43], s64 offen offset:2048 lds\n"
    "v_fma_mix_f32 v26, v17, v42, v42 op_sel:[0,0,1] op_sel_hi:[1,1,1]\n"
    "v_fma_mix_f32 v27, v17, v43, v43 op_sel:[1,0,1] op_sel_hi:[1,1,1]\n"
    "v_mfma_f32_32x32x16_bf16 v[192:207], a[80:83], v[36:39], v[192:207]\n"
    "s_waitcnt vmcnt(7) lgkmcnt(4)\n"
    "s_barrier\n"
    "ds_read_b64_tr_b16 a[64:65], v76 offset:0\n"
    "ds_read_b64_tr_b16 a[66:67], v76 offset:2048\n"
    "ds_read_b128 v[56:59], v84 offset:576\n"
    "v_fma_mix_f32 v28, v18, v44, v44 op_sel:[0,0,1] op_sel_hi:[1,1,1]\n"
    "v_fma_mix_f32 v29, v18, v45, v45 op_sel:[1,0,1] op_sel_hi:[1,1,1]\n"
    "v_mfma_f32_32x32x16_bf16 v[208:223], a[84:87], v[36:39], v[208:223]\n"
    "s_waitcnt lgkmcnt(5)\n"
    "ds_read_b64_tr_b16 a[68:69], v77 offset:0\n"
    "ds_read_b64_tr_b16 a[70:71], v77 offset:2048\n"
    "buffer_load_dwordx4 v83, s[40:43], s64 offen offset:3072 lds\n"
    "v_fma_mix_f32 v30, v19, v46, v46 op_sel:[0,0,1] op_sel_hi:[1,1,1]\n"
    "v_fma_mix_f32 v31, v19, v47, v47 op_sel:[1,0,1] op_sel_hi:[1,1,1]\n"
    "v_mfma_f32_32x32x16_bf16 v[224:239], a[88:91], v[36:39], v[224:239]\n"
    "s_waitcnt lgkmcnt(5)\n"
    "s_add_u32 m0, s65, 0x1c000\n"
    "ds_read_b64_tr_b16 a[72:73], v78 offset:0\n"
    "ds_read_b64_tr_b16 a[74:75], v78 offset:2048\n"
    "ds_read_b128 v[60:63], v84 offset:592\n"
    "v_cvt_pk_bf16_f32 v32, v24, v25\n"
    "v_cvt_pk_bf16_f32 v33, v26, v27\n"
    "v_cvt_pk_bf16_f32 v34, v28, v29\n"
    "v_cvt_pk_bf16_f32 v35, v30, v31\n"
    "v_mfma_f32_32x32x16_bf16 v[240:255], a[92:95], v[36:39], v[240:255]\n"
    "s_waitcnt lgkmcnt(6)\n"
    "s_add_u32 s64, s66, 0x1c0000\n"
    "ds_read_b64_tr_b16 a[76:77], v79 offset:0\n"
    "ds_read_b64_tr_b16 a[78:79], v79 offset:2048\n"
    "v_mfma_f32_32x32x16_bf16 v[128:143], a[64:67], v[32:35], v[128:143]\n"
    "s_waitcnt lgkmcnt(5)\n"
    "ds_read_b64_tr_b16 a[80:81], v76 offset:256\n"
    "ds_read_b64_tr_b16 a[82:83], v76 offset:2304\n"
    "buffer_load_dwordx4 v80, s[40:43], s64 offen offset:0 lds\n"
    "v_mfma_f32_32x32x16_bf16 v[144:159], a[68:71], v[32:35], v[144:159]\n"
    "s_waitcnt vmcnt(8) lgkmcnt(5)\n"
    "ds_read_b64_tr_b16 a[84:85], v77 offset:256\n"
    "ds_read_b64_tr_b16 a[86:87], v77 offset:2304\n"
    "v_accvgpr_read_b32 v20, a164\n"
    "v_accvgpr_read_b32 v21, a165\n"
    "v_accvgpr_read_b32 v22, a166\n"
    "v_accvgpr_read_b32 v23, a167\n"
    "v_mfma_f32_32x32x16_bf16 v[160:175], a[72:75], v[32:35], v[160:175]\n"
    "s_waitcnt lgkmcnt(4)\n"
    "ds_read_b64_tr_b16 a[88:89], v78 offset:256\n"
    "ds_read_b64_tr_b16 a[90:91], v78 offset:2304\n"
    "buffer_load_dwordx4 v81, s[40:43], s64 offen offset:1024 lds\n"
    "v_fma_mix_f32 v24, v20, v56, v56 op_sel:[0,0,1] op_sel_hi:[1,1,1]\n"
    "v_fma_mix_f32 v25, v20, v57, v57 op_sel:[1,0,1] op_sel_hi:[1,1,1]\n"
    "v_mfma_f32_32x32x16_bf16 v[176:191], a[76:79], v[32:35], v[176:191]\n"
    "s_waitcnt lgkmcnt(4)\n"
    "ds_read_b64_tr_b16 a[92:93], v79 offset:256\n"
    "ds_read_b64_tr_b16 a[94:95], v79 offset:2304\n"
    "v_fma_mix_f32 v26, v21, v58, v58 op_sel:[0,0,1] op_sel_hi:[1,1,1]\n"
    "v_fma_mix_f32 v27, v21, v59, v59 op_sel:[1,0,1] op_sel_hi:[1,1,1]\n"
    "v_fma_mix_f32 v28, v22, v60, v60 op_sel:[0,0,1] op_sel_hi:[1,1,1]\n"
    "v_fma_mix_f32 v29, v22, v61, v61 op_sel:[1,0,1] op_sel_hi:[1,1,1]\n"
    "v_mfma_f32_32x32x16_bf16 v[192:207], a[80:83], v[32:35], v[192:207]\n"
    "s_waitcnt lgkmcnt(4)\n"
    "ds_read_b64_tr_b16 a[64:65], v76 offset:8192\n"
    "ds_read_b64_tr_b16 a[66:67], v76 offset:10240\n"
    "buffer_load_dwordx4 v82, s[40:43], s64 offen offset:2048 lds\n"
    "ds_read_b128 v[40:43], v84 offset:640\n"
    "v_fma_mix_f32 v30, v23, v62, v62 op_sel:[0,0,1] op_sel_hi:[1,1,1]\n"
    "v_mfma_f32_32x32x16_bf16 v[208:223], a[84:87], v[32:35], v[208:223]\n"
    "s_waitcnt lgkmcnt(5)\n"
    "ds_read_b64_tr_b16 a[68:69], v77 offset:8192\n"
    "ds_read_b64_tr_b16 a[70:71], v77 offset:10240\n"
    "v_fma_mix_f32 v31, v23, v63, v63 op_sel:[1,0,1] op_sel_hi:[1,1,1]\n"
    "v_cvt_pk_bf16_f32 v36, v24, v25\n"
    "v_cvt_pk_bf16_f32 v37, v26, v27\n"
    "v_cvt_pk_bf16_f32 v38, v28, v29\n"
    "v_mfma_f32_32x32x16_bf16 v[224:239], a[88:91], v[32:35], v[224:239]\n"
    "s_waitcnt lgkmcnt(5)\n"
    "ds_read_b64_tr_b16 a[72:73], v78 offset:8192\n"
    "ds_read_b64_tr_b16 a[74:75], v78 offset:10240\n"
    "buffer_load_dwordx4 v83, s[40:43], s64 offen offset:3072 lds\n"
    "ds_read_b128 v[44:47], v84 offset:656\n"
    "v_cvt_pk_bf16_f32 v39, v30, v31\n"
    "v_mfma_f32_32x32x16_bf16 v[240:255], a[92:95], v[32:35], v[240:255]\n"
    "s_waitcnt lgkmcnt(6)\n"
    "ds_read_b64_tr_b16 a[76:77], v79 offset:8192\n"
    "ds_read_b64_tr_b16 a[78:79], v79 offset:10240\n"
    "v_mfma_f32_32x32x16_bf16 v[128:143], a[64:67], v[36:39], v[128:143]\n"
    "s_waitcnt lgkmcnt(5)\n"
    "s_mov_b32 s77, 0x3400\n"
    "ds_read_b64_tr_b16 a[80:81], v76 offset:8448\n"
    "ds_read_b64_tr_b16 a[82:83], v76 offset:10496\n"
    "buffer_load_dwordx4 a[180:183], v0, s[60:63], s77 offen offset:0\n"
    "v_mfma_f32_32x32x16_bf16 v[144:159], a[68:71], v[36:39], v[144:159]\n"
    "s_waitcnt vmcnt(11) lgkmcnt(5)\n"
    "ds_read_b64_tr_b16 a[84:85], v77 offset:8448\n"
    "ds_read_b64_tr_b16 a[86:87], v77 offset:10496\n"
    "v_accvgpr_read_b32 v16, a168\n"
    "v_accvgpr_read_b32 v17, a169\n"
    "v_accvgpr_read_b32 v18, a170\n"
    "v_accvgpr_read_b32 v19, a171\n"
    "v_mfma_f32_32x32x16_bf16 v[160:175], a[72:75], v[36:39], v[160:175]\n"
    "s_waitcnt lgkmcnt(4)\n"
    "s_mov_b32 s77, 0x3800\n"
    "ds_read_b64_tr_b16 a[88:89], v78 offset:8448\n"
    "ds_read_b64_tr_b16 a[90:91], v78 offset:10496\n"
    "buffer_load_dwordx4 a[184:187], v0, s[60:63], s77 offen offset:0\n"
    "v_fma_mix_f32 v24, v16, v40, v40 op_sel:[0,0,1] op_sel_hi:[1,1,1]\n"
    "v_mfma_f32_32x32x16_bf16 v[176:191], a[76:79], v[36:39], v[176:191]\n"
    "s_waitcnt lgkmcnt(4)\n"
    "ds_read_b64_tr_b16 a[92:93], v79 offset:8448\n"
    "ds_read_b64_tr_b16 a[94:95], v79 offset:10496\n"
    "v_fma_mix_f32 v25, v16, v41, v41 op_sel:[1,0,1] op_sel_hi:[1,1,1]\n"
    "v_fma_mix_f32 v26, v17, v42, v42 op_sel:[0,0,1] op_sel_hi:[1,1,1]\n"
    "v_fma_mix_f32 v27, v17, v43, v43 op_sel:[1,0,1] op_sel_hi:[1,1,1]\n"
    "v_fma_mix_f32 v28, v18, v44, v44 op_sel:[0,0,1] op_sel_hi:[1,1,1]\n"
    "v_mfma_f32_32x32x16_bf16 v[192:207], a[80:83], v[36:39], v[192:207]\n"
    "s_waitcnt lgkmcnt(4)\n"
    "s_mov_b32 s77, 0x3c00\n"
    "ds_read_b64_tr_b16 a[64:65], v76 offset:16384\n"
    "ds_read_b64_tr_b16 a[66:67], v76 offset:18432\n"
    "buffer_load_dwordx4 a[188:191], v0, s[60:63], s77 offen offset:0\n"
    "ds_read_b128 v[56:59], v84 offset:704\n"
    "v_mfma_f32_32x32x16_bf16 v[208:223], a[84:87], v[36:39], v[208:223]\n"
    "s_waitcnt lgkmcnt(5)\n"
    "ds_read_b64_tr_b16 a[68:69], v77 offset:16384\n"
    "ds_read_b64_tr_b16 a[70:71], v77 offset:18432\n"
    "v_fma_mix_f32 v29, v18, v45, v45 op_sel:[1,0,1] op_sel_hi:[1,1,1]\n"
    "v_fma_mix_f32 v30, v19, v46, v46 op_sel:[0,0,1] op_sel_hi:[1,1,1]\n"
    "v_fma_mix_f32 v31, v19, v47, v47 op_sel:[1,0,1] op_sel_hi:[1,1,1]\n"
    "v_cvt_pk_bf16_f32 v32, v24, v25\n"
    "v_mfma_f32_32x32x16_bf16 v[224:239], a[88:91], v[36:39], v[224:239]\n"
    "s_waitcnt lgkmcnt(5)\n"
    "s_mov_b32 s77, 0x4000\n"
    "ds_read_b64_tr_b16 a[72:73], v78 offset:16384\n"
    "ds_read_b64_tr_b16 a[74:75], v78 offset:18432\n"
    "buffer_load_dwordx4 a[192:195], v0, s[60:63], s77 offen offset:0\n"
    "ds_read_b128 v[60:63], v84 offset:720\n"
    "v_cvt_pk_bf16_f32 v33, v26, v27\n"
    "v_cvt_pk_bf16_f32 v34, v28, v29\n"
    "v_cvt_pk_bf16_f32 v35, v30, v31\n"
    "v_mfma_f32_32x32x16_bf16 v[240:255], a[92:95], v[36:39], v[240:255]\n"
    "s_waitcnt lgkmcnt(6)\n"
    "s_add_u32 m0, s65, 0x0\n"
    "ds_read_b64_tr_b16 a[76:77], v79 offset:16384\n"
    "ds_read_b64_tr_b16 a[78:79], v79 offset:18432\n"
    "v_mfma_f32_32x32x16_bf16 v[128:143], a[64:67], v[32:35], v[128:143]\n"
    "s_waitcnt lgkmcnt(5)\n"
    "s_add_u32 s64, s66, 0x200000\n"
    "ds_read_b64_tr_b16 a[80:81], v76 offset:16640\n"
    "ds_read_b64_tr_b16 a[82:83], v76 offset:18688\n"
    "v_mfma_f32_32x32x16_bf16 v[144:159], a[68:71], v[32:35], v[144:159]\n"
    "s_waitcnt vmcnt(13) lgkmcnt(5)\n"
    "ds_read_b64_tr_b16 a[84:85], v77 offset:16640\n"
    "ds_read_b64_tr_b16 a[86:87], v77 offset:18688\n"
    "buffer_load_dwordx4 v80, s[40:43], s64 offen offset:0 lds\n"
    "v_accvgpr_read_b32 v20, a172\n"
    "v_accvgpr_read_b32 v21, a173\n"
    "v_mfma_f32_32x32x16_bf16 v[160:175], a[72:75], v[32:35], v[160:175]\n"
    "s_waitcnt lgkmcnt(4)\n"
    "ds_read_b64_tr_b16 a[88:89], v78 offset:16640\n"
    "ds_read_b64_tr_b16 a[90:91], v78 offset:18688\n"
    "v_accvgpr_read_b32 v22, a174\n"
    "v_accvgpr_read_b32 v23, a175\n"
    "v_fma_mix_f32 v24, v20, v56, v56 op_sel:[0,0,1] op_sel_hi:[1,1,1]\n"
    "v_fma_mix_f32 v25, v20, v57, v57 op_sel:[1,0,1] op_sel_hi:[1,1,1]\n"
    "v_mfma_f32_32x32x16_bf16 v[176:191], a[76:79], v[32:35], v[176:191]\n"
    "s_waitcnt lgkmcnt(4)\n"
    "ds_read_b64_tr_b16 a[92:93], v79 offset:16640\n"
    "ds_read_b64_tr_b16 a[94:95], v79 offset:18688\n"
    "buffer_load_dwordx4 v81, s[40:43], s64 offen offset:1024 lds\n"
    "v_fma_mix_f32 v26, v21, v58, v58 op_sel:[0,0,1] op_sel_hi:[1,1,1]\n"
    "v_fma_mix_f32 v27, v21, v59, v59 op_sel:[1,0,1] op_sel_hi:[1,1,1]\n"
    "v_mfma_f32_32x32x16_bf16 v[192:207], a[80:83], v[32:35], v[192:207]\n"
    "s_waitcnt lgkmcnt(4)\n"
    "ds_read_b64_tr_b16 a[64:65], v76 offset:24576\n"
    "ds_read_b64_tr_b16 a[66:67], v76 offset:26624\n"
    "ds_read_b128 v[40:43], v84 offset:768\n"
    "v_fma_mix_f32 v28, v22, v60, v60 op_sel:[0,0,1] op_sel_hi:[1,1,1]\n"
    "v_fma_mix_f32 v29, v22, v61, v61 op_sel:[1,0,1] op_sel_hi:[1,1,1]\n"
    "v_fma_mix_f32 v30, v23, v62, v62 op_sel:[0,0,1] op_sel_hi:[1,1,1]\n"
    "v_mfma_f32_32x32x16_bf16 v[208:223], a[84:87], v[32:35], v[208:223]\n"
    "s_waitcnt lgkmcnt(5)\n"
    "ds_read_b64_tr_b16 a[68:69], v77 offset:24576\n"
    "ds_read_b64_tr_b16 a[70:71], v77 offset:26624\n"
    "buffer_load_dwordx4 v82, s[40:43], s64 offen offset:2048 lds\n"
    "v_fma_mix_f32 v31, v23, v63, v63 op_sel:[1,0,1] op_sel_hi:[1,1,1]\n"
    "v_cvt_pk_bf16_f32 v36, v24, v25\n"
    "v_mfma_f32_32x32x16_bf16 v[224:239], a[88:91], v[32:35], v[224:239]\n"
    "s_waitcnt lgkmcnt(5)\n"
    "ds_read_b64_tr_b16 a[72:73], v78 offset:24576\n"
    "ds_read_b64_tr_b16 a[74:75], v78 offset:26624\n"
    "ds_read_b128 v[44:47], v84 offset:784\n"
    "v_cvt_pk_bf16_f32 v37, v26, v27\n"
    "v_cvt_pk_bf16_f32 v38, v28, v29\n"
    "v_cvt_pk_bf16_f32 v39, v30, v31\n"
    "v_mfma_f32_32x32x16_bf16 v[240:255], a[92:95], v[32:35], v[240:255]\n"
    "s_waitcnt lgkmcnt(6)\n"
    "ds_read_b64_tr_b16 a[76:77], v79 offset:24576\n"
    "ds_read_b64_tr_b16 a[78:79], v79 offset:26624\n"
    "buffer_load_dwordx4 v83, s[40:43], s64 offen offset:3072 lds\n"
    "v_mfma_f32_32x32x16_bf16 v[128:143], a[64:67], v[36:39], v[128:143]\n"
    "s_waitcnt lgkmcnt(5)\n"
    "s_add_u32 m0, s65, 0x4000\n"
    "ds_read_b64_tr_b16 a[80:81], v76 offset:24832\n"
    "ds_read_b64_tr_b16 a[82:83], v76 offset:26880\n"
    "v_mfma_f32_32x32x16_bf16 v[144:159], a[68:71], v[36:39], v[144:159]\n"
    "s_waitcnt vmcnt(16) lgkmcnt(5)\n"
    "s_add_u32 s64, s66, 0x240000\n"
    "ds_read_b64_tr_b16 a[84:85], v77 offset:24832\n"
    "ds_read_b64_tr_b16 a[86:87], v77 offset:26880\n"
    "v_accvgpr_read_b32 v16, a176\n"
    "v_accvgpr_read_b32 v17, a177\n"
    "v_accvgpr_read_b32 v18, a178\n"
    "v_mfma_f32_32x32x16_bf16 v[160:175], a[72:75], v[36:39], v[160:175]\n"
    "s_waitcnt lgkmcnt(4)\n"
    "ds_read_b64_tr_b16 a[88:89], v78 offset:24832\n"
    "ds_read_b64_tr_b16 a[90:91], v78 offset:26880\n"
    "buffer_load_dwordx4 v80, s[40:43], s64 offen offset:0 lds\n"
    "v_accvgpr_read_b32 v19, a179\n"
    "v_fma_mix_f32 v24, v16, v40, v40 op_sel:[0,0,1] op_sel_hi:[1,1,1]\n"
    "v_mfma_f32_32x32x16_bf16 v[176:191], a[76:79], v[36:39], v[176:191]\n"
    "s_waitcnt lgkmcnt(4)\n"
    "ds_read_b64_tr_b16 a[92:93], v79 offset:24832\n"
    "ds_read_b64_tr_b16 a[94:95], v79 offset:26880\n"
    "v_fma_mix_f32 v25, v16, v41, v41 op_sel:[1,0,1] op_sel_hi:[1,1,1]\n"
    "v_fma_mix_f32 v26, v17, v42, v42 op_sel:[0,0,1] op_sel_hi:[1,1,1]\n"
    "v_fma_mix_f32 v27, v17, v43, v43 op_sel:[1,0,1] op_sel_hi:[1,1,1]\n"
    "v_fma_mix_f32 v28, v18, v44, v44 op_sel:[0,0,1] op_sel_hi:[1,1,1]\n"
    "v_mfma_f32_32x32x16_bf16 v[192:207], a[80:83], v[36:39], v[192:207]\n"
    "s_waitcnt vmcnt(9) lgkmcnt(4)\n"
    "s_barrier\n"
    "ds_read_b64_tr_b16 a[64:65], v76 offset:32768\n"
    "ds_read_b64_tr_b16 a[66:67], v76 offset:34816\n"
    "buffer_load_dwordx4 v81, s[40:43], s64 offen offset:1024 lds\n"
    "ds_read_b128 v[56:59], v84 offset:832\n"
    "v_mfma_f32_32x32x16_bf16 v[208:223], a[84:87], v[36:39], v[208:223]\n"
    "s_waitcnt lgkmcnt(5)\n"
    "ds_read_b64_tr_b16 a[68:69], v77 offset:32768\n"
    "ds_read_b64_tr_b16 a[70:71], v77 offset:34816\n"
    "v_fma_mix_f32 v29, v18, v45, v45 op_sel:[1,0,1] op_sel_hi:[1,1,1]\n"
    "v_fma_mix_f32 v30, v19, v46, v46 op_sel:[0,0,1] op_sel_hi:[1,1,1]\n"
    "v_fma_mix_f32 v31, v19, v47, v47 op_sel:[1,0,1] op_sel_hi:[1,1,1]\n"
    "v_cvt_pk_bf16_f32 v32, v24, v25\n"
    "v_mfma_f32_32x32x16_bf16 v[224:239], a[88:91], v[36:39], v[224:239]\n"
    "s_waitcnt lgkmcnt(5)\n"
    "ds_read_b64_tr_b16 a[72:73], v78 offset:32768\n"
    "ds_read_b64_tr_b16 a[74:75], v78 offset:34816\n"
    "buffer_load_dwordx4 v82, s[40:43], s64 offen offset:2048 lds\n"
    "ds_read_b128 v[60:63], v84 offset:848\n"
    "v_cvt_pk_bf16_f32 v33, v26, v27\n"
    "v_cvt_pk_bf16_f32 v34, v28, v29\n"
    "v_cvt_pk_bf16_f32 v35, v30, v31\n"
    "v_mfma_f32_32x32x16_bf16 v[240:255], a[92:95], v[36:39], v[240:255]\n"
    "s_waitcnt lgkmcnt(6)\n"
    "ds_read_b64_tr_b16 a[76:77], v79 offset:32768\n"
    "ds_read_b64_tr_b16 a[78:79], v79 offset:34816\n"
    "v_mfma_f32_32x32x16_bf16 v[128:143], a[64:67], v[32:35], v[128:143]\n"
    "s_waitcnt lgkmcnt(5)\n"
    "ds_read_b64_tr_b16 a[80:81], v76 offset:33024\n"
    "ds_read_b64_tr_b16 a[82:83], v76 offset:35072\n"
    "buffer_load_dwordx4 v83, s[40:43], s64 offen offset:3072 lds\n"
    "v_accvgpr_read_b32 v15, a116\n"
    "v_mfma_f32_32x32x16_bf16 v[144:159], a[68:71], v[32:35], v[144:159]\n"
    "s_waitcnt vmcnt(11) lgkmcnt(5)\n"
    "ds_read_b64_tr_b16 a[84:85], v77 offset:33024\n"
    "ds_read_b64_tr_b16 a[86:87], v77 offset:35072\n"
    "buffer_load_dword a125, v15, s[44:47], s99 offen offset:0\n"
    "v_accvgpr_read_b32 v20, a180\n"
    "v_accvgpr_read_b32 v21, a181\n"
    "v_mfma_f32_32x32x16_bf16 v[160:175], a[72:75], v[32:35], v[160:175]\n"
    "s_waitcnt lgkmcnt(4)\n"
    "s_mov_b32 s77, 0x4400\n"
    "ds_read_b64_tr_b16 a[88:89], v78 offset:33024\n"
    "ds_read_b64_tr_b16 a[90:91], v78 offset:35072\n"
    "buffer_load_dwordx4 a[196:199], v0, s[60:63], s77 offen offset:0\n"
    "v_accvgpr_read_b32 v22, a182\n"
    "v_mfma_f32_32x32x16_bf16 v[176:191], a[76:79], v[32:35], v[176:191]\n"
    "s_waitcnt lgkmcnt(4)\n"
    "ds_read_b64_tr_b16 a[92:93], v79 offset:33024\n"
    "ds_read_b64_tr_b16 a[94:95], v79 offset:35072\n"
    "v_accvgpr_read_b32 v23, a183\n"
    "v_fma_mix_f32 v24, v20, v56, v56 op_sel:[0,0,1] op_sel_hi:[1,1,1]\n"
    "v_fma_mix_f32 v25, v20, v57, v57 op_sel:[1,0,1] op_sel_hi:[1,1,1]\n"
    "v_fma_mix_f32 v26, v21, v58, v58 op_sel:[0,0,1] op_sel_hi:[1,1,1]\n"
    "v_mfma_f32_32x32x16_bf16 v[192:207], a[80:83], v[32:35], v[192:207]\n"
    "s_waitcnt lgkmcnt(4)\n"
    "s_mov_b32 s77, 0x4800\n"
    "ds_read_b64_tr_b16 a[64:65], v76 offset:40960\n"
    "ds_read_b64_tr_b16 a[66:67], v76 offset:43008\n"
    "buffer_load_dwordx4 a[200:203], v0, s[60:63], s77 offen offset:0\n"
    "ds_read_b128 v[40:43], v84 offset:896\n"
    "v_mfma_f32_32x32x16_bf16 v[208:223], a[84:87], v[32:35], v[208:223]\n"
    "s_waitcnt lgkmcnt(5)\n"
    "ds_read_b64_tr_b16 a[68:69], v77 offset:40960\n"
    "ds_read_b64_tr_b16 a[70:71], v77 offset:43008\n"
    "v_fma_mix_f32 v27, v21, v59, v59 op_sel:[1,0,1] op_sel_hi:[1,1,1]\n"
    "v_fma_mix_f32 v28, v22, v60, v60 op_sel:[0,0,1] op_sel_hi:[1,1,1]\n"
    "v_fma_mix_f32 v29, v22, v61, v61 op_sel:[1,0,1] op_sel_hi:[1,1,1]\n"
    "v_fma_mix_f32 v30, v23, v62, v62 op_sel:[0,0,1] op_sel_hi:[1,1,1]\n"
    "v_mfma_f32_32x32x16_bf16 v[224:239], a[88:91], v[32:35], v[224:239]\n"
    "s_waitcnt lgkmcnt(5)\n"
    "s_mov_b32 s77, 0x4c00\n"
    "ds_read_b64_tr_b16 a[72:73], v78 offset:40960\n"
    "ds_read_b64_tr_b16 a[74:75], v78 offset:43008\n"
    "buffer_load_dwordx4 a[204:207], v0, s[60:63], s77 offen offset:0\n"
    "ds_read_b128 v[44:47], v84 offset:912\n"
    "v_fma_mix_f32 v31, v23, v63, v63 op_sel:[1,0,1] op_sel_hi:[1,1,1]\n"
    "v_cvt_pk_bf16_f32 v36, v24, v25\n"
    "v_cvt_pk_bf16_f32 v37, v26, v27\n"
    "v_cvt_pk_bf16_f32 v38, v28, v29\n"
    "v_cvt_pk_bf16_f32 v39, v30, v31\n"
    "v_mfma_f32_32x32x16_bf16 v[240:255], a[92:95], v[32:35], v[240:255]\n"
    "s_waitcnt lgkmcnt(6)\n"
    "ds_read_b64_tr_b16 a[76:77], v79 offset:40960\n"
    "ds_read_b64_tr_b16 a[78:79], v79 offset:43008\n"
    "v_mfma_f32_32x32x16_bf16 v[128:143], a[64:67], v[36:39], v[128:143]\n"
    "s_waitcnt lgkmcnt(5)\n"
    "s_mov_b32 s77, 0x5000\n"
    "ds_read_b64_tr_b16 a[80:81], v76 offset:41216\n"
    "ds_read_b64_tr_b16 a[82:83], v76 offset:43264\n"
    "buffer_load_dwordx4 a[208:211], v0, s[60:63], s77 offen offset:0\n"
    "v_mfma_f32_32x32x16_bf16 v[144:159], a[68:71], v[36:39], v[144:159]\n"
    "s_waitcnt vmcnt(15) lgkmcnt(5)\n"
    "s_add_u32 m0, s65, 0x8000\n"
    "ds_read_b64_tr_b16 a[84:85], v77 offset:41216\n"
    "ds_read_b64_tr_b16 a[86:87], v77 offset:43264\n"
    "v_accvgpr_read_b32 v16, a184\n"
    "v_accvgpr_read_b32 v17, a185\n"
    "v_accvgpr_read_b32 v18, a186\n"
    "v_mfma_f32_32x32x16_bf16 v[160:175], a[72:75], v[36:39], v[160:175]\n"
    "s_waitcnt lgkmcnt(4)\n"
    "s_add_u32 s64, s66, 0x280000\n"
    "ds_read_b64_tr_b16 a[88:89], v78 offset:41216\n"
    "ds_read_b64_tr_b16 a[90:91], v78 offset:43264\n"
    "v_accvgpr_read_b32 v19, a187\n"
    "v_fma_mix_f32 v24, v16, v40, v40 op_sel:[0,0,1] op_sel_hi:[1,1,1]\n"
    "v_fma_mix_f32 v25, v16, v41, v41 op_sel:[1,0,1] op_sel_hi:[1,1,1]\n"
    "v_mfma_f32_32x32x16_bf16 v[176:191], a[76:79], v[36:39], v[176:191]\n"
    "s_waitcnt lgkmcnt(4)\n"
    "ds_read_b64_tr_b16 a[92:93], v79 offset:41216\n"
    "ds_read_b64_tr_b16 a[94:95], v79 offset:43264\n"
    "buffer_load_dwordx4 v80, s[40:43], s64 offen offset:0 lds\n"
    "v_fma_mix_f32 v26, v17, v42, v42 op_sel:[0,0,1] op_sel_hi:[1,1,1]\n"
    "v_fma_mix_f32 v27, v17, v43, v43 op_sel:[1,0,1] op_sel_hi:[1,1,1]\n"
    "v_mfma_f32_32x32x16_bf16 v[192:207], a[80:83], v[36:39], v[192:207]\n"
    "s_waitcnt lgkmcnt(4)\n"
    "ds_read_b64_tr_b16 a[64:65], v76 offset:49152\n"
    "ds_read_b64_tr_b16 a[66:67], v76 offset:51200\n"
    "ds_read_b128 v[56:59], v84 offset:960\n"
    "v_fma_mix_f32 v28, v18, v44, v44 op_sel:[0,0,1] op_sel_hi:[1,1,1]\n"
    "v_fma_mix_f32 v29, v18, v45, v45 op_sel:[1,0,1] op_sel_hi:[1,1,1]\n"
    "v_fma_mix_f32 v30, v19, v46, v46 op_sel:[0,0,1] op_sel_hi:[1,1,1]\n"
    "v_mfma_f32_32x32x16_bf16 v[208:223], a[84:87], v[36:39], v[208:223]\n"
    "s_waitcnt lgkmcnt(5)\n"
    "ds_read_b64_tr_b16 a[68:69], v77 offset:49152\n"
    "ds_read_b64_tr_b16 a[70:71], v77 offset:51200\n"
    "buffer_load_dwordx4 v81, s[40:43], s64 offen offset:1024 lds\n"
    "v_fma_mix_f32 v31, v19, v47, v47 op_sel:[1,0,1] op_sel_hi:[1,1,1]\n"
    "v_cvt_pk_bf16_f32 v32, v24, v25\n"
    "v_mfma_f32_32x32x16_bf16 v[224:239], a[88:91], v[36:39], v[224:239]\n"
    "s_waitcnt lgkmcnt(5)\n"
    "ds_read_b64_tr_b16 a[72:73], v78 offset:49152\n"
    "ds_read_b64_tr_b16 a[74:75], v78 offset:51200\n"
    "ds_read_b128 v[60:63], v84 offset:976\n"
    "v_cvt_pk_bf16_f32 v33, v26, v27\n"
    "v_cvt_pk_bf16_f32 v34, v28, v29\n"
    "v_cvt_pk_bf16_f32 v35, v30, v31\n"
    "v_mfma_f32_32x32x16_bf16 v[240:255], a[92:95], v[36:39], v[240:255]\n"
    "s_waitcnt lgkmcnt(6)\n"
    "ds_read_b64_tr_b16 a[76:77], v79 offset:49152\n"
    "ds_read_b64_tr_b16 a[78:79], v79 offset:51200\n"
    "buffer_load_dwordx4 v82, s[40:43], s64 offen offset:2048 lds\n"
    "v_mfma_f32_32x32x16_bf16 v[128:143], a[64:67], v[32:35], v[128:143]\n"
    "s_waitcnt lgkmcnt(5)\n"
    "ds_read_b64_tr_b16 a[80:81], v76 offset:49408\n"
    "ds_read_b64_tr_b16 a[82:83], v76 offset:51456\n"
    "v_mfma_f32_32x32x16_bf16 v[144:159], a[68:71], v[32:35], v[144:159]\n"
    "s_waitcnt vmcnt(17) lgkmcnt(5)\n"
    "ds_read_b64_tr_b16 a[84:85], v77 offset:49408\n"
    "ds_read_b64_tr_b16 a[86:87], v77 offset:51456\n"
    "buffer_load_dwordx4 v83, s[40:43], s64 offen offset:3072 lds\n"
    "v_accvgpr_read_b32 v20, a188\n"
    "v_accvgpr_read_b32 v21, a189\n"
    "v_mfma_f32_32x32x16_bf16 v[160:175], a[72:75], v[32:35], v[160:175]\n"
    "s_waitcnt lgkmcnt(4)\n"
    "s_add_u32 m0, s65, 0xc000\n"
    "ds_read_b64_tr_b16 a[88:89], v78 offset:49408\n"
    "ds_read_b64_tr_b16 a[90:91], v78 offset:51456\n"
    "v_accvgpr_read_b32 v22, a190\n"
    "v_accvgpr_read_b32 v23, a191\n"
    "v_fma_mix_f32 v24, v20, v56, v56 op_sel:[0,0,1] op_sel_hi:[1,1,1]\n"
    "v_mfma_f32_32x32x16_bf16 v[176:191], a[76:79], v[32:35], v[176:191]\n"
    "s_waitcnt lgkmcnt(4)\n"
    "s_add_u32 s64, s66, 0x2c0000\n"
    "ds_read_b64_tr_b16 a[92:93], v79 offset:49408\n"
    "ds_read_b64_tr_b16 a[94:95], v79 offset:51456\n"
    "v_fma_mix_f32 v25, v20, v57, v57 op_sel:[1,0,1] op_sel_hi:[1,1,1]\n"
    "v_fma_mix_f32 v26, v21, v58, v58 op_sel:[0,0,1] op_sel_hi:[1,1,1]\n"
    "v_fma_mix_f32 v27, v21, v59, v59 op_sel:[1,0,1] op_sel_hi:[1,1,1]\n"
    "v_mfma_f32_32x32x16_bf16 v[192:207], a[80:83], v[32:35], v[192:207]\n"
    "s_waitcnt lgkmcnt(4)\n"
    "ds_read_b64_tr_b16 a[64:65], v76 offset:57344\n"
    "ds_read_b64_tr_b16 a[66:67], v76 offset:59392\n"
    "buffer_load_dwordx4 v80, s[40:43], s64 offen offset:0 lds\n"
    "ds_read_b128 v[40:43], v84 offset:1024\n"
    "v_fma_mix_f32 v28, v22, v60, v60 op_sel:[0,0,1] op_sel_hi:[1,1,1]\n"
    "v_mfma_f32_32x32x16_bf16 v[208:223], a[84:87], v[32:35], v[208:223]\n"
    "s_waitcnt lgkmcnt(5)\n"
    "ds_read_b64_tr_b16 a[68:69], v77 offset:57344\n"
    "ds_read_b64_tr_b16 a[70:71], v77 offset:59392\n"
    "v_fma_mix_f32 v29, v22, v61, v61 op_sel:[1,0,1] op_sel_hi:[1,1,1]\n"
    "v_fma_mix_f32 v30, v23, v62, v62 op_sel:[0,0,1] op_sel_hi:[1,1,1]\n"
    "v_fma_mix_f32 v31, v23, v63, v63 op_sel:[1,0,1] op_sel_hi:[1,1,1]\n"
    "v_cvt_pk_bf16_f32 v36, v24, v25\n"
    "v_mfma_f32_32x32x16_bf16 v[224:239], a[88:91], v[32:35], v[224:239]\n"
    "s_waitcnt lgkmcnt(5)\n"
    "ds_read_b64_tr_b16 a[72:73], v78 offset:57344\n"
    "ds_read_b64_tr_b16 a[74:75], v78 offset:59392\n"
    "buffer_load_dwordx4 v81, s[40:43], s64 offen offset:1024 lds\n"
    "ds_read_b128 v[44:47], v84 offset:1040\n"
    "v_cvt_pk_bf16_f32 v37, v26, v27\n"
    "v_cvt_pk_bf16_f32 v38, v28, v29\n"
    "v_cvt_pk_bf16_f32 v39, v30, v31\n"
    "v_mfma_f32_32x32x16_bf16 v[240:255], a[92:95], v[32:35], v[240:255]\n"
    "s_waitcnt lgkmcnt(6)\n"
    "ds_read_b64_tr_b16 a[76:77], v79 offset:57344\n"
    "ds_read_b64_tr_b16 a[78:79], v79 offset:59392\n"
    "v_mfma_f32_32x32x16_bf16 v[128:143], a[64:67], v[36:39], v[128:143]\n"
    "s_waitcnt lgkmcnt(5)\n"
    "ds_read_b64_tr_b16 a[80:81], v76 offset:57600\n"
    "ds_read_b64_tr_b16 a[82:83], v76 offset:59648\n"
    "buffer_load_dwordx4 v82, s[40:43], s64 offen offset:2048 lds\n"
    "v_mfma_f32_32x32x16_bf16 v[144:159], a[68:71], v[36:39], v[144:159]\n"
    "s_waitcnt vmcnt(20) lgkmcnt(5)\n"
    "ds_read_b64_tr_b16 a[84:85], v77 offset:57600\n"
    "ds_read_b64_tr_b16 a[86:87], v77 offset:59648\n"
    "v_accvgpr_read_b32 v16, a192\n"
    "v_accvgpr_read_b32 v17, a193\n"
    "v_accvgpr_read_b32 v18, a194\n"
    "v_accvgpr_read_b32 v19, a195\n"
    "v_mfma_f32_32x32x16_bf16 v[160:175], a[72:75], v[36:39], v[160:175]\n"
    "s_waitcnt lgkmcnt(4)\n"
    "ds_read_b64_tr_b16 a[88:89], v78 offset:57600\n"
    "ds_read_b64_tr_b16 a[90:91], v78 offset:59648\n"
    "buffer_load_dwordx4 v83, s[40:43], s64 offen offset:3072 lds\n"
    "v_fma_mix_f32 v24, v16, v40, v40 op_sel:[0,0,1] op_sel_hi:[1,1,1]\n"
    "v_fma_mix_f32 v25, v16, v41, v41 op_sel:[1,0,1] op_sel_hi:[1,1,1]\n"
    "v_mfma_f32_32x32x16_bf16 v[176:191], a[76:79], v[36:39], v[176:191]\n"
    "s_waitcnt lgkmcnt(4)\n"
    "ds_read_b64_tr_b16 a[92:93], v79 offset:57600\n"
    "ds_read_b64_tr_b16 a[94:95], v79 offset:59648\n"
    "v_fma_mix_f32 v26, v17, v42, v42 op_sel:[0,0,1] op_sel_hi:[1,1,1]\n"
    "v_fma_mix_f32 v27, v17, v43, v43 op_sel:[1,0,1] op_sel_hi:[1,1,1]\n"
    "v_fma_mix_f32 v28, v18, v44, v44 op_sel:[0,0,1] op_sel_hi:[1,1,1]\n"
    "v_fma_mix_f32 v29, v18, v45, v45 op_sel:[1,0,1] op_sel_hi:[1,1,1]\n"
    "v_mfma_f32_32x32x16_bf16 v[192:207], a[80:83], v[36:39], v[192:207]\n"
    "s_waitcnt vmcnt(13) lgkmcnt(4)\n"
    "s_barrier\n"
    "s_mov_b32 s77, 0x5400\n"
    "ds_read_b64_tr_b16 a[64:65], v72 offset:0\n"
    "ds_read_b64_tr_b16 a[66:67], v72 offset:2048\n"
    "buffer_load_dwordx4 a[212:215], v0, s[60:63], s77 offen offset:0\n"
    "ds_read_b128 v[56:59], v84 offset:1088\n"
    "v_mfma_f32_32x32x16_bf16 v[208:223], a[84:87], v[36:39], v[208:223]\n"
    "s_waitcnt lgkmcnt(5)\n"
    "ds_read_b64_tr_b16 a[68:69], v73 offset:0\n"
    "ds_read_b64_tr_b16 a[70:71], v73 offset:2048\n"
    "v_fma_mix_f32 v30, v19, v46, v46 op_sel:[0,0,1] op_sel_hi:[1,1,1]\n"
    "v_fma_mix_f32 v31, v19, v47, v47 op_sel:[1,0,1] op_sel_hi:[1,1,1]\n"
    "v_cvt_pk_bf16_f32 v32, v24, v25\n"
    "v_cvt_pk_bf16_f32 v33, v26, v27\n"
    "v_mfma_f32_32x32x16_bf16 v[224:239], a[88:91], v[36:39], v[224:239]\n"
    "s_waitcnt lgkmcnt(5)\n"
    "s_mov_b32 s77, 0x5800\n"
    "ds_read_b64_tr_b16 a[72:73], v74 offset:0\n"
    "ds_read_b64_tr_b16 a[74:75], v74 offset:2048\n"
    "buffer_load_dwordx4 a[216:219], v0, s[60:63], s77 offen offset:0\n"
    "ds_read_b128 v[60:63], v84 offset:1104\n"
    "v_cvt_pk_bf16_f32 v34, v28, v29\n"
    "v_cvt_pk_bf16_f32 v35, v30, v31\n"
    "v_mfma_f32_32x32x16_bf16 v[240:255], a[92:95], v[36:39], v[240:255]\n"
    "s_waitcnt lgkmcnt(6)\n"
    "ds_read_b64_tr_b16 a[76:77], v75 offset:0\n"
    "ds_read_b64_tr_b16 a[78:79], v75 offset:2048\n"
    "v_mfma_f32_32x32x16_bf16 v[128:143], a[64:67], v[32:35], v[128:143]\n"
    "s_waitcnt lgkmcnt(5)\n"
    "s_mov_b32 s77, 0x5c00\n"
    "ds_read_b64_tr_b16 a[80:81], v72 offset:256\n"
    "ds_read_b64_tr_b16 a[82:83], v72 offset:2304\n"
    "buffer_load_dwordx4 a[220:223], v0, s[60:63], s77 offen offset:0\n"
    "v_mfma_f32_32x32x16_bf16 v[144:159], a[68:71], v[32:35], v[144:159]\n"
    "s_waitcnt vmcnt(14) lgkmcnt(5)\n"
    "ds_read_b64_tr_b16 a[84:85], v73 offset:256\n"
    "ds_read_b64_tr_b16 a[86:87], v73 offset:2304\n"
    "v_accvgpr_read_b32 v20, a196\n"
    "v_accvgpr_read_b32 v21, a197\n"
    "v_accvgpr_read_b32 v22, a198\n"
    "v_accvgpr_read_b32 v23, a199\n"
    "v_mfma_f32_32x32x16_bf16 v[160:175], a[72:75], v[32:35], v[160:175]\n"
    "s_waitcnt lgkmcnt(4)\n"
    "s_mov_b32 s77, 0x6000\n"
    "ds_read_b64_tr_b16 a[88:89], v74 offset:256\n"
    "ds_read_b64_tr_b16 a[90:91], v74 offset:2304\n"
    "buffer_load_dwordx4 a[224:227], v0, s[60:63], s77 offen offset:0\n"
    "v_fma_mix_f32 v24, v20, v56, v56 op_sel:[0,0,1] op_sel_hi:[1,1,1]\n"
    "v_mfma_f32_32x32x16_bf16 v[176:191], a[76:79], v[32:35], v[176:191]\n"
    "s_waitcnt lgkmcnt(4)\n"
    "s_add_u32 m0, s65, 0x10000\n"
    "ds_read_b64_tr_b16 a[92:93], v75 offset:256\n"
    "ds_read_b64_tr_b16 a[94:95], v75 offset:2304\n"
    "v_fma_mix_f32 v25, v20, v57, v57 op_sel:[1,0,1] op_sel_hi:[1,1,1]\n"
    "v_fma_mix_f32 v26, v21, v58, v58 op_sel:[0,0,1] op_sel_hi:[1,1,1]\n"
    "v_fma_mix_f32 v27, v21, v59, v59 op_sel:[1,0,1] op_sel_hi:[1,1,1]\n"
    "v_mfma_f32_32x32x16_bf16 v[192:207], a[80:83], v[32:35], v[192:207]\n"
    "s_waitcnt lgkmcnt(4)\n"
    "s_add_u32 s64, s66, 0x300000\n"
    "ds_read_b64_tr_b16 a[64:65], v72 offset:8192\n"
    "ds_read_b64_tr_b16 a[66:67], v72 offset:10240\n"
    "ds_read_b128 v[40:43], v84 offset:1152\n"
    "v_fma_mix_f32 v28, v22, v60, v60 op_sel:[0,0,1] op_sel_hi:[1,1,1]\n"
    "v_fma_mix_f32 v29, v22, v61, v61 op_sel:[1,0,1] op_sel_hi:[1,1,1]\n"
    "v_mfma_f32_32x32x16_bf16 v[208:223], a[84:87], v[32:35], v[208:223]\n"
    "s_waitcnt lgkmcnt(5)\n"
    "ds_read_b64_tr_b16 a[68:69], v73 offset:8192\n"
    "ds_read_b64_tr_b16 a[70:71], v73 offset:10240\n"
    "buffer_load_dwordx4 v80, s[40:43], s64 offen offset:0 lds\n"
    "v_fma_mix_f32 v30, v23, v62, v62 op_sel:[0,0,1] op_sel_hi:[1,1,1]\n"
    "v_fma_mix_f32 v31, v23, v63, v63 op_sel:[1,0,1] op_sel_hi:[1,1,1]\n"
    "v_mfma_f32_32x32x16_bf16 v[224:239], a[88:91], v[32:35], v[224:239]\n"
    "s_waitcnt lgkmcnt(5)\n"
    "ds_read_b64_tr_b16 a[72:73], v74 offset:8192\n"
    "ds_read_b64_tr_b16 a[74:75], v74 offset:10240\n"
    "ds_read_b128 v[44:47], v84 offset:1168\n"
    "v_cvt_pk_bf16_f32 v36, v24, v25\n"
    "v_cvt_pk_bf16_f32 v37, v26, v27\n"
    "v_cvt_pk_bf16_f32 v38, v28, v29\n"
    "v_cvt_pk_bf16_f32 v39, v30, v31\n"
    "v_mfma_f32_32x32x16_bf16 v[240:255], a[92:95], v[32:35], v[240:255]\n"
    "s_waitcnt lgkmcnt(6)\n"
    "ds_read_b64_tr_b16 a[76:77], v75 offset:8192\n"
    "ds_read_b64_tr_b16 a[78:79], v75 offset:10240\n"
    "buffer_load_dwordx4 v81, s[40:43], s64 offen offset:1024 lds\n"
    "v_mfma_f32_32x32x16_bf16 v[128:143], a[64:67], v[36:39], v[128:143]\n"
    "s_waitcnt lgkmcnt(5)\n"
    "ds_read_b64_tr_b16 a[80:81], v72 offset:8448\n"
    "ds_read_b64_tr_b16 a[82:83], v72 offset:10496\n"
    "v_mfma_f32_32x32x16_bf16 v[144:159], a[68:71], v[36:39], v[144:159]\n"
    "s_waitcnt vmcnt(16) lgkmcnt(5)\n"
    "ds_read_b64_tr_b16 a[84:85], v73 offset:8448\n"
    "ds_read_b64_tr_b16 a[86:87], v73 offset:10496\n"
    "buffer_load_dwordx4 v82, s[40:43], s64 offen offset:2048 lds\n"
    "v_accvgpr_read_b32 v16, a200\n"
    "v_accvgpr_read_b32 v17, a201\n"
    "v_mfma_f32_32x32x16_bf16 v[160:175], a[72:75], v[36:39], v[160:175]\n"
    "s_waitcnt lgkmcnt(4)\n"
    "ds_read_b64_tr_b16 a[88:89], v74 offset:8448\n"
    "ds_read_b64_tr_b16 a[90:91], v74 offset:10496\n"
    "v_accvgpr_read_b32 v18, a202\n"
    "v_accvgpr_read_b32 v19, a203\n"
    "v_fma_mix_f32 v24, v16, v40, v40 op_sel:[0,0,1] op_sel_hi:[1,1,1]\n"
    "v_fma_mix_f32 v25, v16, v41, v41 op_sel:[1,0,1] op_sel_hi:[1,1,1]\n"
    "v_mfma_f32_32x32x16_bf16 v[176:191], a[76:79], v[36:39], v[176:191]\n"
    "s_waitcnt lgkmcnt(4)\n"
    "ds_read_b64_tr_b16 a[92:93], v75 offset:8448\n"
    "ds_read_b64_tr_b16 a[94:95], v75 offset:10496\n"
    "buffer_load_dwordx4 v83, s[40:43], s64 offen offset:3072 lds\n"
    "v_fma_mix_f32 v26, v17, v42, v42 op_sel:[0,0,1] op_sel_hi:[1,1,1]\n"
    "v_fma_mix_f32 v27, v17, v43, v43 op_sel:[1,0,1] op_sel_hi:[1,1,1]\n"
    "v_mfma_f32_32x32x16_bf16 v[192:207], a[80:83], v[36:39], v[192:207]\n"
    "s_waitcnt lgkmcnt(4)\n"
    "s_add_u32 m0, s65, 0x14000\n"
    "ds_read_b64_tr_b16 a[64:65], v72 offset:16384\n"
    "ds_read_b64_tr_b16 a[66:67], v72 offset:18432\n"
    "ds_read_b128 v[56:59], v84 offset:1216\n"
    "v_fma_mix_f32 v28, v18, v44, v44 op_sel:[0,0,1] op_sel_hi:[1,1,1]\n"
    "v_fma_mix_f32 v29, v18, v45, v45 op_sel:[1,0,1] op_sel_hi:[1,1,1]\n"
    "v_mfma_f32_32x32x16_bf16 v[208:223], a[84:87], v[36:39], v[208:223]\n"
    "s_waitcnt lgkmcnt(5)\n"
    "s_add_u32 s64, s66, 0x340000\n"
    "ds_read_b64_tr_b16 a[68:69], v73 offset:16384\n"
    "ds_read_b64_tr_b16 a[70:71], v73 offset:18432\n"
    "v_fma_mix_f32 v30, v19, v46, v46 op_sel:[0,0,1] op_sel_hi:[1,1,1]\n"
    "v_fma_mix_f32 v31, v19, v47, v47 op_sel:[1,0,1] op_sel_hi:[1,1,1]\n"
    "v_cvt_pk_bf16_f32 v32, v24, v25\n"
    "v_mfma_f32_32x32x16_bf16 v[224:239], a[88:91], v[36:39], v[224:239]\n"
    "s_waitcnt lgkmcnt(5)\n"
    "ds_read_b64_tr_b16 a[72:73], v74 offset:16384\n"
    "ds_read_b64_tr_b16 a[74:75], v74 offset:18432\n"
    "buffer_load_dwordx4 v80, s[40:43], s64 offen offset:0 lds\n"
    "ds_read_b128 v[60:63], v84 offset:1232\n"
    "v_cvt_pk_bf16_f32 v33, v26, v27\n"
    "v_cvt_pk_bf16_f32 v34, v28, v29\n"
    "v_cvt_pk_bf16_f32 v35, v30, v31\n"
    "v_mfma_f32_32x32x16_bf16 v[240:255], a[92:95], v[36:39], v[240:255]\n"
    "s_waitcnt lgkmcnt(6)\n"
    "ds_read_b64_tr_b16 a[76:77], v75 offset:16384\n"
    "ds_read_b64_tr_b16 a[78:79], v75 offset:18432\n"
    "v_mfma_f32_32x32x16_bf16 v[128:143], a[64:67], v[32:35], v[128:143]\n"
    "s_waitcnt lgkmcnt(5)\n"
    "ds_read_b64_tr_b16 a[80:81], v72 offset:16640\n"
    "ds_read_b64_tr_b16 a[82:83], v72 offset:18688\n"
    "buffer_load_dwordx4 v81, s[40:43], s64 offen offset:1024 lds\n"
    "v_mfma_f32_32x32x16_bf16 v[144:159], a[68:71], v[32:35], v[144:159]\n"
    "s_waitcnt vmcnt(19) lgkmcnt(5)\n"
    "ds_read_b64_tr_b16 a[84:85], v73 offset:16640\n"
    "ds_read_b64_tr_b16 a[86:87], v73 offset:18688\n"
    "v_accvgpr_read_b32 v20, a204\n"
    "v_accvgpr_read_b32 v21, a205\n"
    "v_accvgpr_read_b32 v22, a206\n"
    "v_accvgpr_read_b32 v23, a207\n"
    "v_mfma_f32_32x32x16_bf16 v[160:175], a[72:75], v[32:35], v[160:175]\n"
    "s_waitcnt lgkmcnt(4)\n"
    "ds_read_b64_tr_b16 a[88:89], v74 offset:16640\n"
    "ds_read_b64_tr_b16 a[90:91], v74 offset:18688\n"
    "buffer_load_dwordx4 v82, s[40:43], s64 offen offset:2048 lds\n"
    "v_fma_mix_f32 v24, v20, v56, v56 op_sel:[0,0,1] op_sel_hi:[1,1,1]\n"
    "v_fma_mix_f32 v25, v20, v57, v57 op_sel:[1,0,1] op_sel_hi:[1,1,1]\n"
    "v_mfma_f32_32x32x16_bf16 v[176:191], a[76:79], v[32:35], v[176:191]\n"
    "s_waitcnt lgkmcnt(4)\n"
    "ds_read_b64_tr_b16 a[92:93], v75 offset:16640\n"
    "ds_read_b64_tr_b16 a[94:95], v75 offset:18688\n"
    "v_fma_mix_f32 v26, v21, v58, v58 op_sel:[0,0,1] op_sel_hi:[1,1,1]\n"
    "v_fma_mix_f32 v27, v21, v59, v59 op_sel:[1,0,1] op_sel_hi:[1,1,1]\n"
    "v_fma_mix_f32 v28, v22, v60, v60 op_sel:[0,0,1] op_sel_hi:[1,1,1]\n"
    "v_fma_mix_f32 v29, v22, v61, v61 op_sel:[1,0,1] op_sel_hi:[1,1,1]\n"
    "v_mfma_f32_32x32x16_bf16 v[192:207], a[80:83], v[32:35], v[192:207]\n"
    "s_waitcnt lgkmcnt(4)\n"
    "ds_read_b64_tr_b16 a[64:65], v72 offset:24576\n"
    "ds_read_b64_tr_b16 a[66:67], v72 offset:26624\n"
    "buffer_load_dwordx4 v83, s[40:43], s64 offen offset:3072 lds\n"
    "ds_read_b128 v[40:43], v84 offset:1280\n"
    "v_fma_mix_f32 v30, v23, v62, v62 op_sel:[0,0,1] op_sel_hi:[1,1,1]\n"
    "v_mfma_f32_32x32x16_bf16 v[208:223], a[84:87], v[32:35], v[208:223]\n"
    "s_waitcnt lgkmcnt(5)\n"
    "ds_read_b64_tr_b16 a[68:69], v73 offset:24576\n"
    "ds_read_b64_tr_b16 a[70:71], v73 offset:26624\n"
    "v_fma_mix_f32 v31, v23, v63, v63 op_sel:[1,0,1] op_sel_hi:[1,1,1]\n"
    "v_cvt_pk_bf16_f32 v36, v24, v25\n"
    "v_cvt_pk_bf16_f32 v37, v26, v27\n"
    "v_cvt_pk_bf16_f32 v38, v28, v29\n"
    "v_mfma_f32_32x32x16_bf16 v[224:239], a[88:91], v[32:35], v[224:239]\n"
    "s_waitcnt lgkmcnt(5)\n"
    "s_mov_b32 s77, 0x6400\n"
    "ds_read_b64_tr_b16 a[72:73], v74 offset:24576\n"
    "ds_read_b64_tr_b16 a[74:75], v74 offset:26624\n"
    "buffer_load_dwordx4 a[228:231], v0, s[60:63], s77 offen offset:0\n"
    "ds_read_b128 v[44:47], v84 offset:1296\n"
    "v_cvt_pk_bf16_f32 v39, v30, v31\n"
    "v_mfma_f32_32x32x16_bf16 v[240:255], a[92:95], v[32:35], v[240:255]\n"
    "s_waitcnt lgkmcnt(6)\n"
    "ds_read_b64_tr_b16 a[76:77], v75 offset:24576\n"
    "ds_read_b64_tr_b16 a[78:79], v75 offset:26624\n"
    "v_mfma_f32_32x32x16_bf16 v[128:143], a[64:67], v[36:39], v[128:143]\n"
    "s_waitcnt lgkmcnt(5)\n"
    "s_mov_b32 s77, 0x6800\n"
    "ds_read_b64_tr_b16 a[80:81], v72 offset:24832\n"
    "ds_read_b64_tr_b16 a[82:83], v72 offset:26880\n"
    "buffer_load_dwordx4 a[232:235], v0, s[60:63], s77 offen offset:0\n"
    "v_mfma_f32_32x32x16_bf16 v[144:159], a[68:71], v[36:39], v[144:159]\n"
    "s_waitcnt vmcnt(22) lgkmcnt(5)\n"
    "ds_read_b64_tr_b16 a[84:85], v73 offset:24832\n"
    "ds_read_b64_tr_b16 a[86:87], v73 offset:26880\n"
    "v_accvgpr_read_b32 v16, a208\n"
    "v_accvgpr_read_b32 v17, a209\n"
    "v_accvgpr_read_b32 v18, a210\n"
    "v_accvgpr_read_b32 v19, a211\n"
    "v_mfma_f32_32x32x16_bf16 v[160:175], a[72:75], v[36:39], v[160:175]\n"
    "s_waitcnt lgkmcnt(4)\n"
    "s_mov_b32 s77, 0x6c00\n"
    "ds_read_b64_tr_b16 a[88:89], v74 offset:24832\n"
    "ds_read_b64_tr_b16 a[90:91], v74 offset:26880\n"
    "buffer_load_dwordx4 a[236:239], v0, s[60:63], s77 offen offset:0\n"
    "v_fma_mix_f32 v24, v16, v40, v40 op_sel:[0,0,1] op_sel_hi:[1,1,1]\n"
    "v_mfma_f32_32x32x16_bf16 v[176:191], a[76:79], v[36:39], v[176:191]\n"
    "s_waitcnt lgkmcnt(4)\n"
    "ds_read_b64_tr_b16 a[92:93], v75 offset:24832\n"
    "ds_read_b64_tr_b16 a[94:95], v75 offset:26880\n"
    "v_fma_mix_f32 v25, v16, v41, v41 op_sel:[1,0,1] op_sel_hi:[1,1,1]\n"
    "v_fma_mix_f32 v26, v17, v42, v42 op_sel:[0,0,1] op_sel_hi:[1,1,1]\n"
    "v_fma_mix_f32 v27, v17, v43, v43 op_sel:[1,0,1] op_sel_hi:[1,1,1]\n"
    "v_fma_mix_f32 v28, v18, v44, v44 op_sel:[0,0,1] op_sel_hi:[1,1,1]\n"
    "v_mfma_f32_32x32x16_bf16 v[192:207], a[80:83], v[36:39], v[192:207]\n"
    "s_waitcnt vmcnt(15) lgkmcnt(4)\n"
    "s_barrier\n"
    "ds_read_b64_tr_b16 a[64:65], v72 offset:32768\n"
    "ds_read_b64_tr_b16 a[66:67], v72 offset:34816\n"
    "ds_read_b128 v[56:59], v84 offset:1344\n"
    "v_fma_mix_f32 v29, v18, v45, v45 op_sel:[1,0,1] op_sel_hi:[1,1,1]\n"
    "v_fma_mix_f32 v30, v19, v46, v46 op_sel:[0,0,1] op_sel_hi:[1,1,1]\n"
    "v_mfma_f32_32x32x16_bf16 v[208:223], a[84:87], v[36:39], v[208:223]\n"
    "s_waitcnt lgkmcnt(5)\n"
    "ds_read_b64_tr_b16 a[68:69], v73 offset:32768\n"
    "ds_read_b64_tr_b16 a[70:71], v73 offset:34816\n"
    "v_fma_mix_f32 v31, v19, v47, v47 op_sel:[1,0,1] op_sel_hi:[1,1,1]\n"
    "v_cvt_pk_bf16_f32 v32, v24, v25\n"
    "v_cvt_pk_bf16_f32 v33, v26, v27\n"
    "v_cvt_pk_bf16_f32 v34, v28, v29\n"
    "v_mfma_f32_32x32x16_bf16 v[224:239], a[88:91], v[36:39], v[224:239]\n"
    "s_waitcnt lgkmcnt(5)\n"
    "ds_read_b64_tr_b16 a[72:73], v74 offset:32768\n"
    "ds_read_b64_tr_b16 a[74:75], v74 offset:34816\n"
    "ds_read_b128 v[60:63], v84 offset:1360\n"
    "v_cvt_pk_bf16_f32 v35, v30, v31\n"
    "v_mfma_f32_32x32x16_bf16 v[240:255], a[92:95], v[36:39], v[240:255]\n"
    "s_waitcnt lgkmcnt(6)\n"
    "ds_read_b64_tr_b16 a[76:77], v75 offset:32768\n"
    "ds_read_b64_tr_b16 a[78:79], v75 offset:34816\n"
    "v_mfma_f32_32x32x16_bf16 v[128:143], a[64:67], v[32:35], v[128:143]\n"
    "s_waitcnt lgkmcnt(5)\n"
    "ds_read_b64_tr_b16 a[80:81], v72 offset:33024\n"
    "ds_read_b64_tr_b16 a[82:83], v72 offset:35072\n"
    "v_mfma_f32_32x32x16_bf16 v[144:159], a[68:71], v[32:35], v[144:159]\n"
    "s_waitcnt vmcnt(14) lgkmcnt(5)\n"
    "s_add_u32 m0, s65, 0x18000\n"
    "ds_read_b64_tr_b16 a[84:85], v73 offset:33024\n"
    "ds_read_b64_tr_b16 a[86:87], v73 offset:35072\n"
    "v_accvgpr_read_b32 v20, a212\n"
    "v_accvgpr_read_b32 v21, a213\n"
    "v_accvgpr_read_b32 v22, a214\n"
    "v_mfma_f32_32x32x16_bf16 v[160:175], a[72:75], v[32:35], v[160:175]\n"
    "s_waitcnt lgkmcnt(4)\n"
    "s_add_u32 s64, s67, 0x0\n"
    "ds_read_b64_tr_b16 a[88:89], v74 offset:33024\n"
    "ds_read_b64_tr_b16 a[90:91], v74 offset:35072\n"
    "v_accvgpr_read_b32 v23, a215\n"
    "v_fma_mix_f32 v24, v20, v56, v56 op_sel:[0,0,1] op_sel_hi:[1,1,1]\n"
    "v_fma_mix_f32 v25, v20, v57, v57 op_sel:[1,0,1] op_sel_hi:[1,1,1]\n"
    "v_mfma_f32_32x32x16_bf16 v[176:191], a[76:79], v[32:35], v[176:191]\n"
    "s_waitcnt lgkmcnt(4)\n"
    "ds_read_b64_tr_b16 a[92:93], v75 offset:33024\n"
    "ds_read_b64_tr_b16 a[94:95], v75 offset:35072\n"
    "buffer_load_dwordx4 v0, s[44:47], s64 offen offset:0 lds\n"
    "v_fma_mix_f32 v26, v21, v58, v58 op_sel:[0,0,1] op_sel_hi:[1,1,1]\n"
    "v_fma_mix_f32 v27, v21, v59, v59 op_sel:[1,0,1] op_sel_hi:[1,1,1]\n"
    "v_mfma_f32_32x32x16_bf16 v[192:207], a[80:83], v[32:35], v[192:207]\n"
    "s_waitcnt lgkmcnt(4)\n"
    "ds_read_b64_tr_b16 a[64:65], v72 offset:40960\n"
    "ds_read_b64_tr_b16 a[66:67], v72 offset:43008\n"
    "ds_read_b128 v[40:43], v84 offset:1408\n"
    "v_fma_mix_f32 v28, v22, v60, v60 op_sel:[0,0,1] op_sel_hi:[1,1,1]\n"
    "v_fma_mix_f32 v29, v22, v61, v61 op_sel:[1,0,1] op_sel_hi:[1,1,1]\n"
    "v_fma_mix_f32 v30, v23, v62, v62 op_sel:[0,0,1] op_sel_hi:[1,1,1]\n"
    "v_mfma_f32_32x32x16_bf16 v[208:223], a[84:87], v[32:35], v[208:223]\n"
    "s_waitcnt lgkmcnt(5)\n"
    "ds_read_b64_tr_b16 a[68:69], v73 offset:40960\n"
    "ds_read_b64_tr_b16 a[70:71], v73 offset:43008\n"
    "buffer_load_dwordx4 v0, s[44:47], s64 offen offset:1024 lds\n"
    "v_fma_mix_f32 v31, v23, v63, v63 op_sel:[1,0,1] op_sel_hi:[1,1,1]\n"
    "v_cvt_pk_bf16_f32 v36, v24, v25\n"
    "v_mfma_f32_32x32x16_bf16 v[224:239], a[88:91], v[32:35], v[224:239]\n"
    "s_waitcnt lgkmcnt(5)\n"
    "ds_read_b64_tr_b16 a[72:73], v74 offset:40960\n"
    "ds_read_b64_tr_b16 a[74:75], v74 offset:43008\n"
    "ds_read_b128 v[44:47], v84 offset:1424\n"
    "v_cvt_pk_bf16_f32 v37, v26, v27\n"
    "v_cvt_pk_bf16_f32 v38, v28, v29\n"
    "v_cvt_pk_bf16_f32 v39, v30, v31\n"
    "v_mfma_f32_32x32x16_bf16 v[240:255], a[92:95], v[32:35], v[240:255]\n"
    "s_waitcnt lgkmcnt(6)\n"
    "ds_read_b64_tr_b16 a[76:77], v75 offset:40960\n"
    "ds_read_b64_tr_b16 a[78:79], v75 offset:43008\n"
    "buffer_load_dwordx4 v0, s[44:47], s64 offen offset:2048 lds\n"
    "v_mfma_f32_32x32x16_bf16 v[128:143], a[64:67], v[36:39], v[128:143]\n"
    "s_waitcnt lgkmcnt(5)\n"
    "ds_read_b64_tr_b16 a[80:81], v72 offset:41216\n"
    "ds_read_b64_tr_b16 a[82:83], v72 offset:43264\n"
    "v_mfma_f32_32x32x16_bf16 v[144:159], a[68:71], v[36:39], v[144:159]\n"
    "s_waitcnt vmcnt(16) lgkmcnt(5)\n"
    "ds_read_b64_tr_b16 a[84:85], v73 offset:41216\n"
    "ds_read_b64_tr_b16 a[86:87], v73 offset:43264\n"
    "buffer_load_dwordx4 v0, s[44:47], s64 offen offset:3072 lds\n"
    "v_accvgpr_read_b32 v16, a216\n"
    "v_accvgpr_read_b32 v17, a217\n"
    "v_mfma_f32_32x32x16_bf16 v[160:175], a[72:75], v[36:39], v[160:175]\n"
    "s_waitcnt lgkmcnt(4)\n"
    "s_add_u32 m0, s65, 0x1c000\n"
    "ds_read_b64_tr_b16 a[88:89], v74 offset:41216\n"
    "ds_read_b64_tr_b16 a[90:91], v74 offset:43264\n"
    "v_accvgpr_read_b32 v18, a218\n"
    "v_accvgpr_read_b32 v19, a219\n"
    "v_fma_mix_f32 v24, v16, v40, v40 op_sel:[0,0,1] op_sel_hi:[1,1,1]\n"
    "v_mfma_f32_32x32x16_bf16 v[176:191], a[76:79], v[36:39], v[176:191]\n"
    "s_waitcnt lgkmcnt(4)\n"
    "s_add_u32 s64, s67, 0x4000\n"
    "ds_read_b64_tr_b16 a[92:93], v75 offset:41216\n"
    "ds_read_b64_tr_b16 a[94:95], v75 offset:43264\n"
    "v_fma_mix_f32 v25, v16, v41, v41 op_sel:[1,0,1] op_sel_hi:[1,1,1]\n"
    "v_fma_mix_f32 v26, v17, v42, v42 op_sel:[0,0,1] op_sel_hi:[1,1,1]\n"
    "v_fma_mix_f32 v27, v17, v43, v43 op_sel:[1,0,1] op_sel_hi:[1,1,1]\n"
    "v_mfma_f32_32x32x16_bf16 v[192:207], a[80:83], v[36:39], v[192:207]\n"
    "s_waitcnt lgkmcnt(4)\n"
    "ds_read_b64_tr_b16 a[64:65], v72 offset:49152\n"
    "ds_read_b64_tr_b16 a[66:67], v72 offset:51200\n"
    "buffer_load_dwordx4 v0, s[44:47], s64 offen offset:0 lds\n"
    "ds_read_b128 v[56:59], v84 offset:1472\n"
    "v_fma_mix_f32 v28, v18, v44, v44 op_sel:[0,0,1] op_sel_hi:[1,1,1]\n"
    "v_mfma_f32_32x32x16_bf16 v[208:223], a[84:87], v[36:39], v[208:223]\n"
    "s_waitcnt lgkmcnt(5)\n"
    "ds_read_b64_tr_b16 a[68:69], v73 offset:49152\n"
    "ds_read_b64_tr_b16 a[70:71], v73 offset:51200\n"
    "v_fma_mix_f32 v29, v18, v45, v45 op_sel:[1,0,1] op_sel_hi:[1,1,1]\n"
    "v_fma_mix_f32 v30, v19, v46, v46 op_sel:[0,0,1] op_sel_hi:[1,1,1]\n"
    "v_fma_mix_f32 v31, v19, v47, v47 op_sel:[1,0,1] op_sel_hi:[1,1,1]\n"
    "v_cvt_pk_bf16_f32 v32, v24, v25\n"
    "v_mfma_f32_32x32x16_bf16 v[224:239], a[88:91], v[36:39], v[224:239]\n"
    "s_waitcnt lgkmcnt(5)\n"
    "ds_read_b64_tr_b16 a[72:73], v74 offset:49152\n"
    "ds_read_b64_tr_b16 a[74:75], v74 offset:51200\n"
    "buffer_load_dwordx4 v0, s[44:47], s64 offen offset:1024 lds\n"
    "ds_read_b128 v[60:63], v84 offset:1488\n"
    "v_cvt_pk_bf16_f32 v33, v26, v27\n"
    "v_cvt_pk_bf16_f32 v34, v28, v29\n"
    "v_cvt_pk_bf16_f32 v35, v30, v31\n"
    "v_mfma_f32_32x32x16_bf16 v[240:255], a[92:95], v[36:39], v[240:255]\n"
    "s_waitcnt lgkmcnt(6)\n"
    "ds_read_b64_tr_b16 a[76:77], v75 offset:49152\n"
    "ds_read_b64_tr_b16 a[78:79], v75 offset:51200\n"
    "v_mfma_f32_32x32x16_bf16 v[128:143], a[64:67], v[32:35], v[128:143]\n"
    "s_waitcnt lgkmcnt(5)\n"
    "ds_read_b64_tr_b16 a[80:81], v72 offset:49408\n"
    "ds_read_b64_tr_b16 a[82:83], v72 offset:51456\n"
    "buffer_load_dwordx4 v0, s[44:47], s64 offen offset:2048 lds\n"
    "v_mfma_f32_32x32x16_bf16 v[144:159], a[68:71], v[32:35], v[144:159]\n"
    "s_waitcnt vmcnt(19) lgkmcnt(5)\n"
    "ds_read_b64_tr_b16 a[84:85], v73 offset:49408\n"
    "ds_read_b64_tr_b16 a[86:87], v73 offset:51456\n"
    "v_accvgpr_read_b32 v20, a220\n"
    "v_accvgpr_read_b32 v21, a221\n"
    "v_accvgpr_read_b32 v22, a222\n"
    "v_accvgpr_read_b32 v23, a223\n"
    "v_mfma_f32_32x32x16_bf16 v[160:175], a[72:75], v[32:35], v[160:175]\n"
    "s_waitcnt lgkmcnt(4)\n"
    "ds_read_b64_tr_b16 a[88:89], v74 offset:49408\n"
    "ds_read_b64_tr_b16 a[90:91], v74 offset:51456\n"
    "buffer_load_dwordx4 v0, s[44:47], s64 offen offset:3072 lds\n"
    "v_fma_mix_f32 v24, v20, v56, v56 op_sel:[0,0,1] op_sel_hi:[1,1,1]\n"
    "v_fma_mix_f32 v25, v20, v57, v57 op_sel:[1,0,1] op_sel_hi:[1,1,1]\n"
    "v_mfma_f32_32x32x16_bf16 v[176:191], a[76:79], v[32:35], v[176:191]\n"
    "s_waitcnt lgkmcnt(4)\n"
    "ds_read_b64_tr_b16 a[92:93], v75 offset:49408\n"
    "ds_read_b64_tr_b16 a[94:95], v75 offset:51456\n"
    "v_fma_mix_f32 v26, v21, v58, v58 op_sel:[0,0,1] op_sel_hi:[1,1,1]\n"
    "v_fma_mix_f32 v27, v21, v59, v59 op_sel:[1,0,1] op_sel_hi:[1,1,1]\n"
    "v_fma_mix_f32 v28, v22, v60, v60 op_sel:[0,0,1] op_sel_hi:[1,1,1]\n"
    "v_fma_mix_f32 v29, v22, v61, v61 op_sel:[1,0,1] op_sel_hi:[1,1,1]\n"
    "v_mfma_f32_32x32x16_bf16 v[192:207], a[80:83], v[32:35], v[192:207]\n"
    "s_waitcnt lgkmcnt(4)\n"
    "ds_read_b64_tr_b16 a[64:65], v72 offset:57344\n"
    "ds_read_b64_tr_b16 a[66:67], v72 offset:59392\n"
    "ds_read_b128 v[40:43], v84 offset:1536\n"
    "v_fma_mix_f32 v30, v23, v62, v62 op_sel:[0,0,1] op_sel_hi:[1,1,1]\n"
    "v_fma_mix_f32 v31, v23, v63, v63 op_sel:[1,0,1] op_sel_hi:[1,1,1]\n"
    "v_cvt_pk_bf16_f32 v36, v24, v25\n"
    "v_mfma_f32_32x32x16_bf16 v[208:223], a[84:87], v[32:35], v[208:223]\n"
    "s_waitcnt lgkmcnt(5)\n"
    "ds_read_b64_tr_b16 a[68:69], v73 offset:57344\n"
    "ds_read_b64_tr_b16 a[70:71], v73 offset:59392\n"
    "v_cvt_pk_bf16_f32 v37, v26, v27\n"
    "v_cvt_pk_bf16_f32 v38, v28, v29\n"
    "v_cvt_pk_bf16_f32 v39, v30, v31\n"
    "v_mfma_f32_32x32x16_bf16 v[224:239], a[88:91], v[32:35], v[224:239]\n"
    "s_waitcnt lgkmcnt(5)\n"
    "ds_read_b64_tr_b16 a[72:73], v74 offset:57344\n"
    "ds_read_b64_tr_b16 a[74:75], v74 offset:59392\n"
    "ds_read_b128 v[44:47], v84 offset:1552\n"
    "v_mfma_f32_32x32x16_bf16 v[240:255], a[92:95], v[32:35], v[240:255]\n"
    "s_waitcnt lgkmcnt(6)\n"
    "ds_read_b64_tr_b16 a[76:77], v75 offset:57344\n"
    "ds_read_b64_tr_b16 a[78:79], v75 offset:59392\n"
    "v_mfma_f32_32x32x16_bf16 v[128:143], a[64:67], v[36:39], v[128:143]\n"
    "s_waitcnt lgkmcnt(5)\n"
    "ds_read_b64_tr_b16 a[80:81], v72 offset:57600\n"
    "ds_read_b64_tr_b16 a[82:83], v72 offset:59648\n"
    "v_mfma_f32_32x32x16_bf16 v[144:159], a[68:71], v[36:39], v[144:159]\n"
    "s_waitcnt vmcnt(19) lgkmcnt(5)\n"
    "ds_read_b64_tr_b16 a[84:85], v73 offset:57600\n"
    "ds_read_b64_tr_b16 a[86:87], v73 offset:59648\n"
    "v_accvgpr_read_b32 v16, a224\n"
    "v_accvgpr_read_b32 v17, a225\n"
    "v_accvgpr_read_b32 v18, a226\n"
    "v_accvgpr_read_b32 v19, a227\n"
    "v_mfma_f32_32x32x16_bf16 v[160:175], a[72:75], v[36:39], v[160:175]\n"
    "s_waitcnt lgkmcnt(4)\n"
    "ds_read_b64_tr_b16 a[88:89], v74 offset:57600\n"
    "ds_read_b64_tr_b16 a[90:91], v74 offset:59648\n"
    "v_fma_mix_f32 v24, v16, v40, v40 op_sel:[0,0,1] op_sel_hi:[1,1,1]\n"
    "v_fma_mix_f32 v25, v16, v41, v41 op_sel:[1,0,1] op_sel_hi:[1,1,1]\n"
    "v_fma_mix_f32 v26, v17, v42, v42 op_sel:[0,0,1] op_sel_hi:[1,1,1]\n"
    "v_fma_mix_f32 v27, v17, v43, v43 op_sel:[1,0,1] op_sel_hi:[1,1,1]\n"
    "v_mfma_f32_32x32x16_bf16 v[176:191], a[76:79], v[36:39], v[176:191]\n"
    "s_waitcnt lgkmcnt(4)\n"
    "ds_read_b64_tr_b16 a[92:93], v75 offset:57600\n"
    "ds_read_b64_tr_b16 a[94:95], v75 offset:59648\n"
    "v_fma_mix_f32 v28, v18, v44, v44 op_sel:[0,0,1] op_sel_hi:[1,1,1]\n"
    "v_fma_mix_f32 v29, v18, v45, v45 op_sel:[1,0,1] op_sel_hi:[1,1,1]\n"
    "v_fma_mix_f32 v30, v19, v46, v46 op_sel:[0,0,1] op_sel_hi:[1,1,1]\n"
    "v_fma_mix_f32 v31, v19, v47, v47 op_sel:[1,0,1] op_sel_hi:[1,1,1]\n"
    "v_mfma_f32_32x32x16_bf16 v[192:207], a[80:83], v[36:39], v[192:207]\n"
    "s_waitcnt vmcnt(11) lgkmcnt(4)\n"
    "s_barrier\n"
    "ds_read_b64_tr_b16 a[64:65], v76 offset:0\n"
    "ds_read_b64_tr_b16 a[66:67], v76 offset:2048\n"
    "ds_read_b128 v[56:59], v84 offset:1600\n"
    "v_cvt_pk_bf16_f32 v32, v24, v25\n"
    "v_cvt_pk_bf16_f32 v33, v26, v27\n"
    "v_mfma_f32_32x32x16_bf16 v[208:223], a[84:87], v[36:39], v[208:223]\n"
    "s_waitcnt lgkmcnt(5)\n"
    "ds_read_b64_tr_b16 a[68:69], v77 offset:0\n"
    "ds_read_b64_tr_b16 a[70:71], v77 offset:2048\n"
    "v_cvt_pk_bf16_f32 v34, v28, v29\n"
    "v_cvt_pk_bf16_f32 v35, v30, v31\n"
    "v_mfma_f32_32x32x16_bf16 v[224:239], a[88:91], v[36:39], v[224:239]\n"
    "s_waitcnt lgkmcnt(5)\n"
    "ds_read_b64_tr_b16 a[72:73], v78 offset:0\n"
    "ds_read_b64_tr_b16 a[74:75], v78 offset:2048\n"
    "ds_read_b128 v[60:63], v84 offset:1616\n"
    "v_mfma_f32_32x32x16_bf16 v[240:255], a[92:95], v[36:39], v[240:255]\n"
    "s_waitcnt lgkmcnt(6)\n"
    "ds_read_b64_tr_b16 a[76:77], v79 offset:0\n"
    "ds_read_b64_tr_b16 a[78:79], v79 offset:2048\n"
    "v_mfma_f32_32x32x16_bf16 v[128:143], a[64:67], v[32:35], v[128:143]\n"
    "s_waitcnt lgkmcnt(5)\n"
    "ds_read_b64_tr_b16 a[80:81], v76 offset:256\n"
    "ds_read_b64_tr_b16 a[82:83], v76 offset:2304\n"
    "v_mfma_f32_32x32x16_bf16 v[144:159], a[68:71], v[32:35], v[144:159]\n"
    "s_waitcnt vmcnt(10) lgkmcnt(5)\n"
    "s_add_u32 m0, s65, 0x0\n"
    "ds_read_b64_tr_b16 a[84:85], v77 offset:256\n"
    "ds_read_b64_tr_b16 a[86:87], v77 offset:2304\n"
    "v_accvgpr_read_b32 v20, a228\n"
    "v_accvgpr_read_b32 v21, a229\n"
    "v_accvgpr_read_b32 v22, a230\n"
    "v_mfma_f32_32x32x16_bf16 v[160:175], a[72:75], v[32:35], v[160:175]\n"
    "s_waitcnt lgkmcnt(4)\n"
    "s_add_u32 s64, s67, 0x8000\n"
    "ds_read_b64_tr_b16 a[88:89], v78 offset:256\n"
    "ds_read_b64_tr_b16 a[90:91], v78 offset:2304\n"
    "v_accvgpr_read_b32 v23, a231\n"
    "v_fma_mix_f32 v24, v20, v56, v56 op_sel:[0,0,1] op_sel_hi:[1,1,1]\n"
    "v_fma_mix_f32 v25, v20, v57, v57 op_sel:[1,0,1] op_sel_hi:[1,1,1]\n"
    "v_mfma_f32_32x32x16_bf16 v[176:191], a[76:79], v[32:35], v[176:191]\n"
    "s_waitcnt lgkmcnt(4)\n"
    "ds_read_b64_tr_b16 a[92:93], v79 offset:256\n"
    "ds_read_b64_tr_b16 a[94:95], v79 offset:2304\n"
    "buffer_load_dwordx4 v0, s[44:47], s64 offen offset:0 lds\n"
    "v_fma_mix_f32 v26, v21, v58, v58 op_sel:[0,0,1] op_sel_hi:[1,1,1]\n"
    "v_fma_mix_f32 v27, v21, v59, v59 op_sel:[1,0,1] op_sel_hi:[1,1,1]\n"
    "v_mfma_f32_32x32x16_bf16 v[192:207], a[80:83], v[32:35], v[192:207]\n"
    "s_waitcnt lgkmcnt(4)\n"
    "ds_read_b64_tr_b16 a[64:65], v76 offset:8192\n"
    "ds_read_b64_tr_b16 a[66:67], v76 offset:10240\n"
    "ds_read_b128 v[40:43], v84 offset:1664\n"
    "v_fma_mix_f32 v28, v22, v60, v60 op_sel:[0,0,1] op_sel_hi:[1,1,1]\n"
    "v_fma_mix_f32 v29, v22, v61, v61 op_sel:[1,0,1] op_sel_hi:[1,1,1]\n"
    "v_fma_mix_f32 v30, v23, v62, v62 op_sel:[0,0,1] op_sel_hi:[1,1,1]\n"
    "v_mfma_f32_32x32x16_bf16 v[208:223], a[84:87], v[32:35], v[208:223]\n"
    "s_waitcnt lgkmcnt(5)\n"
    "ds_read_b64_tr_b16 a[68:69], v77 offset:8192\n"
    "ds_read_b64_tr_b16 a[70:71], v77 offset:10240\n"
    "buffer_load_dwordx4 v0, s[44:47], s64 offen offset:1024 lds\n"
    "v_fma_mix_f32 v31, v23, v63, v63 op_sel:[1,0,1] op_sel_hi:[1,1,1]\n"
    "v_cvt_pk_bf16_f32 v36, v24, v25\n"
    "v_mfma_f32_32x32x16_bf16 v[224:239], a[88:91], v[32:35], v[224:239]\n"
    "s_waitcnt lgkmcnt(5)\n"
    "ds_read_b64_tr_b16 a[72:73], v78 offset:8192\n"
    "ds_read_b64_tr_b16 a[74:75], v78 offset:10240\n"
    "ds_read_b128 v[44:47], v84 offset:1680\n"
    "v_cvt_pk_bf16_f32 v37, v26, v27\n"
    "v_cvt_pk_bf16_f32 v38, v28, v29\n"
    "v_cvt_pk_bf16_f32 v39, v30, v31\n"
    "v_mfma_f32_32x32x16_bf16 v[240:255], a[92:95], v[32:35], v[240:255]\n"
    "s_waitcnt lgkmcnt(6)\n"
    "ds_read_b64_tr_b16 a[76:77], v79 offset:8192\n"
    "ds_read_b64_tr_b16 a[78:79], v79 offset:10240\n"
    "buffer_load_dwordx4 v0, s[44:47], s64 offen offset:2048 lds\n"
    "v_mfma_f32_32x32x16_bf16 v[128:143], a[64:67], v[36:39], v[128:143]\n"
    "s_waitcnt lgkmcnt(5)\n"
    "ds_read_b64_tr_b16 a[80:81], v76 offset:8448\n"
    "ds_read_b64_tr_b16 a[82:83], v76 offset:10496\n"
    "v_mfma_f32_32x32x16_bf16 v[144:159], a[68:71], v[36:39], v[144:159]\n"
    "s_waitcnt vmcnt(12) lgkmcnt(5)\n"
    "ds_read_b64_tr_b16 a[84:85], v77 offset:8448\n"
    "ds_read_b64_tr_b16 a[86:87], v77 offset:10496\n"
    "buffer_load_dwordx4 v0, s[44:47], s64 offen offset:3072 lds\n"
    "v_accvgpr_read_b32 v16, a232\n"
    "v_accvgpr_read_b32 v17, a233\n"
    "v_mfma_f32_32x32x16_bf16 v[160:175], a[72:75], v[36:39], v[160:175]\n"
    "s_waitcnt lgkmcnt(4)\n"
    "s_add_u32 m0, s65, 0x4000\n"
    "ds_read_b64_tr_b16 a[88:89], v78 offset:8448\n"
    "ds_read_b64_tr_b16 a[90:91], v78 offset:10496\n"
    "v_accvgpr_read_b32 v18, a234\n"
    "v_accvgpr_read_b32 v19, a235\n"
    "v_fma_mix_f32 v24, v16, v40, v40 op_sel:[0,0,1] op_sel_hi:[1,1,1]\n"
    "v_mfma_f32_32x32x16_bf16 v[176:191], a[76:79], v[36:39], v[176:191]\n"
    "s_waitcnt lgkmcnt(4)\n"
    "s_add_u32 s64, s67, 0xc000\n"
    "ds_read_b64_tr_b16 a[92:93], v79 offset:8448\n"
    "ds_read_b64_tr_b16 a[94:95], v79 offset:10496\n"
    "v_fma_mix_f32 v25, v16, v41, v41 op_sel:[1,0,1] op_sel_hi:[1,1,1]\n"
    "v_fma_mix_f32 v26, v17, v42, v42 op_sel:[0,0,1] op_sel_hi:[1,1,1]\n"
    "v_fma_mix_f32 v27, v17, v43, v43 op_sel:[1,0,1] op_sel_hi:[1,1,1]\n"
    "v_mfma_f32_32x32x16_bf16 v[192:207], a[80:83], v[36:39], v[192:207]\n"
    "s_waitcnt lgkmcnt(4)\n"
    "ds_read_b64_tr_b16 a[64:65], v76 offset:16384\n"
    "ds_read_b64_tr_b16 a[66:67], v76 offset:18432\n"
    "buffer_load_dwordx4 v0, s[44:47], s64 offen offset:0 lds\n"
    "ds_read_b128 v[56:59], v84 offset:1728\n"
    "v_fma_mix_f32 v28, v18, v44, v44 op_sel:[0,0,1] op_sel_hi:[1,1,1]\n"
    "v_mfma_f32_32x32x16_bf16 v[208:223], a[84:87], v[36:39], v[208:223]\n"
    "s_waitcnt lgkmcnt(5)\n"
    "ds_read_b64_tr_b16 a[68:69], v77 offset:16384\n"
    "ds_read_b64_tr_b16 a[70:71], v77 offset:18432\n"
    "v_fma_mix_f32 v29, v18, v45, v45 op_sel:[1,0,1] op_sel_hi:[1,1,1]\n"
    "v_fma_mix_f32 v30, v19, v46, v46 op_sel:[0,0,1] op_sel_hi:[1,1,1]\n"
    "v_fma_mix_f32 v31, v19, v47, v47 op_sel:[1,0,1] op_sel_hi:[1,1,1]\n"
    "v_cvt_pk_bf16_f32 v32, v24, v25\n"
    "v_mfma_f32_32x32x16_bf16 v[224:239], a[88:91], v[36:39], v[224:239]\n"
    "s_waitcnt lgkmcnt(5)\n"
    "ds_read_b64_tr_b16 a[72:73], v78 offset:16384\n"
    "ds_read_b64_tr_b16 a[74:75], v78 offset:18432\n"
    "buffer_load_dwordx4 v0, s[44:47], s64 offen offset:1024 lds\n"
    "ds_read_b128 v[60:63], v84 offset:1744\n"
    "v_cvt_pk_bf16_f32 v33, v26, v27\n"
    "v_cvt_pk_bf16_f32 v34, v28, v29\n"
    "v_cvt_pk_bf16_f32 v35, v30, v31\n"
    "v_mfma_f32_32x32x16_bf16 v[240:255], a[92:95], v[36:39], v[240:255]\n"
    "s_waitcnt lgkmcnt(6)\n"
    "ds_read_b64_tr_b16 a[76:77], v79 offset:16384\n"
    "ds_read_b64_tr_b16 a[78:79], v79 offset:18432\n"
    "v_mfma_f32_32x32x16_bf16 v[128:143], a[64:67], v[32:35], v[128:143]\n"
    "s_waitcnt lgkmcnt(5)\n"
    "ds_read_b64_tr_b16 a[80:81], v76 offset:16640\n"
    "ds_read_b64_tr_b16 a[82:83], v76 offset:18688\n"
    "buffer_load_dwordx4 v0, s[44:47], s64 offen offset:2048 lds\n"
    "v_mfma_f32_32x32x16_bf16 v[144:159], a[68:71], v[32:35], v[144:159]\n"
    "s_waitcnt vmcnt(15) lgkmcnt(5)\n"
    "ds_read_b64_tr_b16 a[84:85], v77 offset:16640\n"
    "ds_read_b64_tr_b16 a[86:87], v77 offset:18688\n"
    "v_accvgpr_read_b32 v20, a236\n"
    "v_accvgpr_read_b32 v21, a237\n"
    "v_accvgpr_read_b32 v22, a238\n"
    "v_accvgpr_read_b32 v23, a239\n"
    "v_mfma_f32_32x32x16_bf16 v[160:175], a[72:75], v[32:35], v[160:175]\n"
    "s_waitcnt lgkmcnt(4)\n"
    "ds_read_b64_tr_b16 a[88:89], v78 offset:16640\n"
    "ds_read_b64_tr_b16 a[90:91], v78 offset:18688\n"
    "buffer_load_dwordx4 v0, s[44:47], s64 offen offset:3072 lds\n"
    "v_fma_mix_f32 v24, v20, v56, v56 op_sel:[0,0,1] op_sel_hi:[1,1,1]\n"
    "v_fma_mix_f32 v25, v20, v57, v57 op_sel:[1,0,1] op_sel_hi:[1,1,1]\n"
    "v_mfma_f32_32x32x16_bf16 v[176:191], a[76:79], v[32:35], v[176:191]\n"
    "s_waitcnt lgkmcnt(4)\n"
    "ds_read_b64_tr_b16 a[92:93], v79 offset:16640\n"
    "ds_read_b64_tr_b16 a[94:95], v79 offset:18688\n"
    "v_fma_mix_f32 v26, v21, v58, v58 op_sel:[0,0,1] op_sel_hi:[1,1,1]\n"
    "v_fma_mix_f32 v27, v21, v59, v59 op_sel:[1,0,1] op_sel_hi:[1,1,1]\n"
    "v_fma_mix_f32 v28, v22, v60, v60 op_sel:[0,0,1] op_sel_hi:[1,1,1]\n"
    "v_fma_mix_f32 v29, v22, v61, v61 op_sel:[1,0,1] op_sel_hi:[1,1,1]\n"
    "v_mfma_f32_32x32x16_bf16 v[192:207], a[80:83], v[32:35], v[192:207]\n"
    "s_waitcnt lgkmcnt(4)\n"
    "ds_read_b64_tr_b16 a[64:65], v76 offset:24576\n"
    "ds_read_b64_tr_b16 a[66:67], v76 offset:26624\n"
    "v_fma_mix_f32 v30, v23, v62, v62 op_sel:[0,0,1] op_sel_hi:[1,1,1]\n"
    "v_fma_mix_f32 v31, v23, v63, v63 op_sel:[1,0,1] op_sel_hi:[1,1,1]\n"
    "v_cvt_pk_bf16_f32 v36, v24, v25\n"
    "v_cvt_pk_bf16_f32 v37, v26, v27\n"
    "v_mfma_f32_32x32x16_bf16 v[208:223], a[84:87], v[32:35], v[208:223]\n"
    "s_waitcnt lgkmcnt(4)\n"
    "ds_read_b64_tr_b16 a[68:69], v77 offset:24576\n"
    "ds_read_b64_tr_b16 a[70:71], v77 offset:26624\n"
    "v_cvt_pk_bf16_f32 v38, v28, v29\n"
    "v_cvt_pk_bf16_f32 v39, v30, v31\n"
    "v_mfma_f32_32x32x16_bf16 v[224:239], a[88:91], v[32:35], v[224:239]\n"
    "s_waitcnt lgkmcnt(4)\n"
    "ds_read_b64_tr_b16 a[72:73], v78 offset:24576\n"
    "ds_read_b64_tr_b16 a[74:75], v78 offset:26624\n"
    "v_mfma_f32_32x32x16_bf16 v[240:255], a[92:95], v[32:35], v[240:255]\n"
    "s_waitcnt lgkmcnt(4)\n"
    "ds_read_b64_tr_b16 a[76:77], v79 offset:24576\n"
    "ds_read_b64_tr_b16 a[78:79], v79 offset:26624\n"
    "v_mfma_f32_32x32x16_bf16 v[128:143], a[64:67], v[36:39], v[128:143]\n"
    "s_waitcnt lgkmcnt(4)\n"
    "ds_read_b64_tr_b16 a[80:81], v76 offset:24832\n"
    "ds_read_b64_tr_b16 a[82:83], v76 offset:26880\n"
    "v_mfma_f32_32x32x16_bf16 v[144:159], a[68:71], v[36:39], v[144:159]\n"
    "s_waitcnt lgkmcnt(4)\n"
    "ds_read_b64_tr_b16 a[84:85], v77 offset:24832\n"
    "ds_read_b64_tr_b16 a[86:87], v77 offset:26880\n"
    "v_mfma_f32_32x32x16_bf16 v[160:175], a[72:75], v[36:39], v[160:175]\n"
    "s_waitcnt lgkmcnt(4)\n"
    "ds_read_b64_tr_b16 a[88:89], v78 offset:24832\n"
    "ds_read_b64_tr_b16 a[90:91], v78 offset:26880\n"
    "v_mfma_f32_32x32x16_bf16 v[176:191], a[76:79], v[36:39], v[176:191]\n"
    "s_waitcnt vmcnt(8) lgkmcnt(4)\n"
    "s_barrier\n"
    "ds_read_b64_tr_b16 a[92:93], v79 offset:24832\n"
    "ds_read_b64_tr_b16 a[94:95], v79 offset:26880\n"
    "ds_read_b128 a[64:67], v2 offset:32768\n"
    "v_cvt_pk_bf16_f32 v88, v128, v129\n"
    "v_cvt_pk_bf16_f32 v89, v130, v131\n"
    "v_mfma_f32_32x32x16_bf16 v[192:207], a[80:83], v[36:39], v[192:207]\n"
    "s_waitcnt lgkmcnt(5)\n"
    "ds_read_b128 a[68:71], v2 offset:33792\n"
    "v_cvt_pk_bf16_f32 v90, v132, v133\n"
    "v_cvt_pk_bf16_f32 v91, v134, v135\n"
    "v_accvgpr_write_b32 a0, v88\n"
    "v_accvgpr_write_b32 a1, v89\n"
    "v_accvgpr_write_b32 a2, v90\n"
    "v_mfma_f32_32x32x16_bf16 v[208:223], a[84:87], v[36:39], v[208:223]\n"
    "s_waitcnt lgkmcnt(4)\n"
    "ds_read_b128 a[72:75], v2 offset:34816\n"
    "v_accvgpr_write_b32 a3, v91\n"
    "v_cvt_pk_bf16_f32 v92, v136, v137\n"
    "v_cvt_pk_bf16_f32 v93, v138, v139\n"
    "v_cvt_pk_bf16_f32 v94, v140, v141\n"
    "v_cvt_pk_bf16_f32 v95, v142, v143\n"
    "v_mfma_f32_32x32x16_bf16 v[224:239], a[88:91], v[36:39], v[224:239]\n"
    "s_waitcnt lgkmcnt(3)\n"
    "ds_read_b128 a[76:79], v2 offset:35840\n"
    "v_accvgpr_write_b32 a4, v92\n"
    "v_accvgpr_write_b32 a5, v93\n"
    "v_accvgpr_write_b32 a6, v94\n"
    "v_accvgpr_write_b32 a7, v95\n"
    "v_cvt_pk_bf16_f32 v88, v144, v145\n"
    "v_mfma_f32_32x32x16_bf16 v[240:255], a[92:95], v[36:39], v[240:255]\n"
    "s_waitcnt lgkmcnt(3)\n"
    "ds_read_b128 a[80:83], v2 offset:36864\n"
    "v_cvt_pk_bf16_f32 v89, v146, v147\n"
    "v_cvt_pk_bf16_f32 v90, v148, v149\n"
    "v_cvt_pk_bf16_f32 v91, v150, v151\n"
    "v_accvgpr_write_b32 a8, v88\n"
    "v_accvgpr_write_b32 a9, v89\n"
    "v_mfma_f32_32x32x16_bf16 v[16:31], a[64:67], a[0:3], 0\n"
    "s_waitcnt lgkmcnt(3)\n"
    "ds_read_b128 a[84:87], v2 offset:37888\n"
    "v_accvgpr_write_b32 a10, v90\n"
    "v_accvgpr_write_b32 a11, v91\n"
    "v_cvt_pk_bf16_f32 v92, v152, v153\n"
    "v_cvt_pk_bf16_f32 v93, v154, v155\n"
    "v_cvt_pk_bf16_f32 v94, v156, v157\n"
    "v_mfma_f32_32x32x16_bf16 v[16:31], a[68:71], a[4:7], v[16:31]\n"
    "s_waitcnt lgkmcnt(3)\n"
    "s_add_u32 m0, s65, 0x8000\n"
    "ds_read_b128 a[88:91], v2 offset:38912\n"
    "v_cvt_pk_bf16_f32 v95, v158, v159\n"
    "v_accvgpr_write_b32 a12, v92\n"
    "v_accvgpr_write_b32 a13, v93\n"
    "v_accvgpr_write_b32 a14, v94\n"
    "v_accvgpr_write_b32 a15, v95\n"
    "v_mfma_f32_32x32x16_bf16 v[16:31], a[72:75], a[8:11], v[16:31]\n"
    "s_waitcnt lgkmcnt(3)\n"
    "s_add_u32 s64, s67, 0x10000\n"
    "ds_read_b128 a[92:95], v2 offset:39936\n"
    "v_cvt_pk_bf16_f32 v88, v160, v161\n"
    "v_cvt_pk_bf16_f32 v89, v162, v163\n"
    "v_cvt_pk_bf16_f32 v90, v164, v165\n"
    "v_cvt_pk_bf16_f32 v91, v166, v167\n"
    "v_accvgpr_write_b32 a16, v88\n"
    "v_accvgpr_write_b32 a17, v89\n"
    "v_accvgpr_write_b32 a18, v90\n"
    "v_accvgpr_write_b32 a19, v91\n"
    "v_mfma_f32_32x32x16_bf16 v[16:31], a[76:79], a[12:15], v[16:31]\n"
    "s_waitcnt lgkmcnt(3)\n"
    "ds_read_b128 a[64:67], v2 offset:40960\n"
    "buffer_load_dwordx4 v0, s[44:47], s64 offen offset:0 lds\n"
    "v_cvt_pk_bf16_f32 v92, v168, v169\n"
    "v_cvt_pk_bf16_f32 v93, v170, v171\n"
    "v_cvt_pk_bf16_f32 v94, v172, v173\n"
    "v_cvt_pk_bf16_f32 v95, v174, v175\n"
    "v_accvgpr_write_b32 a20, v92\n"
    "v_accvgpr_write_b32 a21, v93\n"
    "v_accvgpr_write_b32 a22, v94\n"
    "v_accvgpr_write_b32 a23, v95\n"
    "v_mfma_f32_32x32x16_bf16 v[16:31], a[80:83], a[16:19], v[16:31]\n"
    "s_waitcnt lgkmcnt(3)\n"
    "ds_read_b128 a[68:71], v2 offset:41984\n"
    "v_accvgpr_read_b32 v15, a116\n"
    "v_cvt_pk_bf16_f32 v88, v176, v177\n"
    "v_cvt_pk_bf16_f32 v89, v178, v179\n"
    "v_cvt_pk_bf16_f32 v90, v180, v181\n"
    "v_cvt_pk_bf16_f32 v91, v182, v183\n"
    "v_accvgpr_write_b32 a24, v88\n"
    "v_accvgpr_write_b32 a25, v89\n"
    "v_accvgpr_write_b32 a26, v90\n"
    "v_accvgpr_write_b32 a27, v91\n"
    "v_mfma_f32_32x32x16_bf16 v[16:31], a[84:87], a[20:23], v[16:31]\n"
    "s_waitcnt lgkmcnt(3)\n"
    "ds_read_b128 a[72:75], v2 offset:43008\n"
    "buffer_load_dwordx4 v0, s[44:47], s64 offen offset:1024 lds\n"
    "buffer_load_dword a126, v15, s[48:51], s99 offen offset:0\n"
    "v_cvt_pk_bf16_f32 v92, v184, v185\n"
    "v_cvt_pk_bf16_f32 v93, v186, v187\n"
    "v_cvt_pk_bf16_f32 v94, v188, v189\n"
    "v_cvt_pk_bf16_f32 v95, v190, v191\n"
    "v_accvgpr_write_b32 a28, v92\n"
    "v_accvgpr_write_b32 a29, v93\n"
    "v_accvgpr_write_b32 a30, v94\n"
    "v_accvgpr_write_b32 a31, v95\n"
    "v_mfma_f32_32x32x16_bf16 v[16:31], a[88:91], a[24:27], v[16:31]\n"
    "s_waitcnt lgkmcnt(3)\n"
    "ds_read_b128 a[76:79], v2 offset:44032\n"
    "v_cvt_pk_bf16_f32 v88, v192, v193\n"
    "v_cvt_pk_bf16_f32 v89, v194, v195\n"
    "v_cvt_pk_bf16_f32 v90, v196, v197\n"
    "v_cvt_pk_bf16_f32 v91, v198, v199\n"
    "v_accvgpr_write_b32 a32, v88\n"
    "v_accvgpr_write_b32 a33, v89\n"
    "v_accvgpr_write_b32 a34, v90\n"
    "v_accvgpr_write_b32 a35, v91\n"
    "v_mfma_f32_32x32x16_bf16 v[16:31], a[92:95], a[28:31], v[16:31]\n"
    "s_waitcnt lgkmcnt(3)\n"
    "ds_read_b128 a[80:83], v2 offset:45056\n"
    "buffer_load_dwordx4 v0, s[44:47], s64 offen offset:2048 lds\n"
    "v_cvt_pk_bf16_f32 v92, v200, v201\n"
    "v_cvt_pk_bf16_f32 v93, v202, v203\n"
    "v_cvt_pk_bf16_f32 v94, v204, v205\n"
    "v_cvt_pk_bf16_f32 v95, v206, v207\n"
    "v_accvgpr_write_b32 a36, v92\n"
    "v_accvgpr_write_b32 a37, v93\n"
    "v_accvgpr_write_b32 a38, v94\n"
    "v_accvgpr_write_b32 a39, v95\n"
    "v_mfma_f32_32x32x16_bf16 v[16:31], a[64:67], a[32:35], v[16:31]\n"
    "s_waitcnt lgkmcnt(3)\n"
    "ds_read_b128 a[84:87], v2 offset:46080\n"
    "v_cvt_pk_bf16_f32 v88, v208, v209\n"
    "v_cvt_pk_bf16_f32 v89, v210, v211\n"
    "v_cvt_pk_bf16_f32 v90, v212, v213\n"
    "v_cvt_pk_bf16_f32 v91, v214, v215\n"
    "v_accvgpr_write_b32 a40, v88\n"
    "v_accvgpr_write_b32 a41, v89\n"
    "v_accvgpr_write_b32 a42, v90\n"
    "v_accvgpr_write_b32 a43, v91\n"
    "v_mfma_f32_32x32x16_bf16 v[16:31], a[68:71], a[36:39], v[16:31]\n"
    "s_waitcnt lgkmcnt(3)\n"
    "ds_read_b128 a[88:91], v2 offset:47104\n"
    "buffer_load_dwordx4 v0, s[44:47], s64 offen offset:3072 lds\n"
    "v_cvt_pk_bf16_f32 v92, v216, v217\n"
    "v_cvt_pk_bf16_f32 v93, v218, v219\n"
    "v_cvt_pk_bf16_f32 v94, v220, v221\n"
    "v_cvt_pk_bf16_f32 v95, v222, v223\n"
    "v_accvgpr_write_b32 a44, v92\n"
    "v_accvgpr_write_b32 a45, v93\n"
    "v_accvgpr_write_b32 a46, v94\n"
    "v_accvgpr_write_b32 a47, v95\n"
    "v_mfma_f32_32x32x16_bf16 v[16:31], a[72:75], a[40:43], v[16:31]\n"
    "s_waitcnt lgkmcnt(3)\n"
    "s_add_u32 m0, s65, 0xc000\n"
    "ds_read_b128 a[92:95], v2 offset:48128\n"
    "v_cvt_pk_bf16_f32 v88, v224, v225\n"
    "v_cvt_pk_bf16_f32 v89, v226, v227\n"
    "v_cvt_pk_bf16_f32 v90, v228, v229\n"
    "v_cvt_pk_bf16_f32 v91, v230, v231\n"
    "v_accvgpr_write_b32 a48, v88\n"
    "v_accvgpr_write_b32 a49, v89\n"
    "v_accvgpr_write_b32 a50, v90\n"
    "v_accvgpr_write_b32 a51, v91\n"
    "v_mfma_f32_32x32x16_bf16 v[16:31], a[76:79], a[44:47], v[16:31]\n"
    "s_waitcnt lgkmcnt(3)\n"
    "s_add_u32 s64, s67, 0x14000\n"
    "ds_read_b128 a[64:67], v2 offset:49152\n"
    "v_cvt_pk_bf16_f32 v92, v232, v233\n"
    "v_cvt_pk_bf16_f32 v93, v234, v235\n"
    "v_cvt_pk_bf16_f32 v94, v236, v237\n"
    "v_cvt_pk_bf16_f32 v95, v238, v239\n"
    "v_accvgpr_write_b32 a52, v92\n"
    "v_accvgpr_write_b32 a53, v93\n"
    "v_accvgpr_write_b32 a54, v94\n"
    "v_accvgpr_write_b32 a55, v95\n"
    "v_mfma_f32_32x32x16_bf16 v[16:31], a[80:83], a[48:51], v[16:31]\n"
    "s_waitcnt lgkmcnt(3)\n"
    "ds_read_b128 a[68:71], v2 offset:50176\n"
    "buffer_load_dwordx4 v0, s[44:47], s64 offen offset:0 lds\n"
    "v_cvt_pk_bf16_f32 v88, v240, v241\n"
    "v_cvt_pk_bf16_f32 v89, v242, v243\n"
    "v_cvt_pk_bf16_f32 v90, v244, v245\n"
    "v_cvt_pk_bf16_f32 v91, v246, v247\n"
    "v_accvgpr_write_b32 a56, v88\n"
    "v_accvgpr_write_b32 a57, v89\n"
    "v_accvgpr_write_b32 a58, v90\n"
    "v_accvgpr_write_b32 a59, v91\n"
    "v_mfma_f32_32x32x16_bf16 v[16:31], a[84:87], a[52:55], v[16:31]\n"
    "s_waitcnt lgkmcnt(3)\n"
    "ds_read_b128 a[72:75], v2 offset:51200\n"
    "v_cvt_pk_bf16_f32 v92, v248, v249\n"
    "v_cvt_pk_bf16_f32 v93, v250, v251\n"
    "v_cvt_pk_bf16_f32 v94, v252, v253\n"
    "v_cvt_pk_bf16_f32 v95, v254, v255\n"
    "v_accvgpr_write_b32 a60, v92\n"
    "v_accvgpr_write_b32 a61, v93\n"
    "v_accvgpr_write_b32 a62, v94\n"
    "v_accvgpr_write_b32 a63, v95\n"
    "v_mfma_f32_32x32x16_bf16 v[16:31], a[88:91], a[56:59], v[16:31]\n"
    "s_waitcnt lgkmcnt(3)\n"
    "ds_read_b128 a[76:79], v2 offset:52224\n"
    "buffer_load_dwordx4 v0, s[44:47], s64 offen offset:1024 lds\n"
    "v_mfma_f32_32x32x16_bf16 v[16:31], a[92:95], a[60:63], v[16:31]\n"
    "s_waitcnt lgkmcnt(3)\n"
    "ds_read_b128 a[80:83], v2 offset:53248\n"
    "v_mfma_f32_32x32x16_bf16 v[32:47], a[64:67], a[0:3], 0\n"
    "s_waitcnt lgkmcnt(3)\n"
    "ds_read_b128 a[84:87], v2 offset:54272\n"
    "buffer_load_dwordx4 v0, s[44:47], s64 offen offset:2048 lds\n"
    "v_mfma_f32_32x32x16_bf16 v[32:47], a[68:71], a[4:7], v[32:47]\n"
    "s_waitcnt lgkmcnt(3)\n"
    "ds_read_b128 a[88:91], v2 offset:55296\n"
    "v_mfma_f32_32x32x16_bf16 v[32:47], a[72:75], a[8:11], v[32:47]\n"
    "s_waitcnt lgkmcnt(3)\n"
    "ds_read_b128 a[92:95], v2 offset:56320\n"
    "buffer_load_dwordx4 v0, s[44:47], s64 offen offset:3072 lds\n"
    "v_max_f32 v3, v16, v17\n"
    "v_max_f32 v4, v18, v19\n"
    "v_max3_f32 v3, v20, v21, v3\n"
    "v_mfma_f32_32x32x16_bf16 v[32:47], a[76:79], a[12:15], v[32:47]\n"
    "s_waitcnt lgkmcnt(3)\n"
    "s_mov_b32 m0, s84\n"
    "ds_read_b128 a[64:67], v2 offset:57344\n"
    "v_accvgpr_read_b32 v15, a118\n"
    "v_accvgpr_read_b32 v10, a119\n"
    "v_max3_f32 v4, v22, v23, v4\n"
    "v_max3_f32 v3, v24, v25, v3\n"
    "v_mfma_f32_32x32x16_bf16 v[32:47], a[80:83], a[16:19], v[32:47]\n"
    "s_waitcnt lgkmcnt(3)\n"
    "s_mov_b32 s85, 0x0\n"
    "ds_read_b128 a[68:71], v2 offset:58368\n"
    "buffer_load_dwordx4 v15, s[24:27], s85 offen offset:0 lds\n"
    "v_max3_f32 v4, v26, v27, v4\n"
    "v_max3_f32 v3, v28, v29, v3\n"
    "v_mfma_f32_32x32x16_bf16 v[32:47], a[84:87], a[20:23], v[32:47]\n"
    "s_waitcnt lgkmcnt(3)\n"
    "s_mov_b32 s85, 0x7c00\n"
    "ds_read_b128 a[72:75], v2 offset:59392\n"
    "buffer_load_dwordx4 v10, s[24:27], s85 offen offset:1024 lds\n"
    "v_max3_f32 v4, v30, v31, v4\n"
    "v_mfma_f32_32x32x16_bf16 v[32:47], a[88:91], a[24:27], v[32:47]\n"
    "s_waitcnt lgkmcnt(3)\n"
    "s_mov_b32 s85, 0xf800\n"
    "ds_read_b128 a[76:79], v2 offset:60416\n"
    "buffer_load_dwordx4 v15, s[24:27], s85 offen offset:2048 lds\n"
    "v_mfma_f32_32x32x16_bf16 v[32:47], a[92:95], a[28:31], v[32:47]\n"
    "s_waitcnt lgkmcnt(3)\n"
    "s_mov_b32 s85, 0x17400\n"
    "ds_read_b128 a[80:83], v2 offset:61440\n"
    "buffer_load_dwordx4 v10, s[24:27], s85 offen offset:3072 lds\n"
    "v_mfma_f32_32x32x16_bf16 v[32:47], a[64:67], a[32:35], v[32:47]\n"
    "s_waitcnt lgkmcnt(3)\n"
    "ds_read_b128 a[84:87], v2 offset:62464\n"
    "v_mfma_f32_32x32x16_bf16 v[32:47], a[68:71], a[36:39], v[32:47]\n"
    "s_waitcnt lgkmcnt(3)\n"
    "ds_read_b128 a[88:91], v2 offset:63488\n"
    "v_mfma_f32_32x32x16_bf16 v[32:47], a[72:75], a[40:43], v[32:47]\n"
    "s_waitcnt lgkmcnt(3)\n"
    "ds_read_b128 a[92:95], v2 offset:64512\n"
    "v_mfma_f32_32x32x16_bf16 v[32:47], a[76:79], a[44:47], v[32:47]\n"
    "s_waitcnt vmcnt(13) lgkmcnt(3)\n"
    "s_barrier\n"
    "ds_read_b128 a[64:67], v1 offset:0\n"
    "v_mfma_f32_32x32x16_bf16 v[32:47], a[80:83], a[48:51], v[32:47]\n"
    "s_waitcnt lgkmcnt(3)\n"
    "ds_read_b128 a[68:71], v1 offset:1024\n"
    "v_mfma_f32_32x32x16_bf16 v[32:47], a[84:87], a[52:55], v[32:47]\n"
    "s_waitcnt lgkmcnt(3)\n"
    "ds_read_b128 a[72:75], v1 offset:2048\n"
    "v_mfma_f32_32x32x16_bf16 v[32:47], a[88:91], a[56:59], v[32:47]\n"
    "s_waitcnt lgkmcnt(3)\n"
    "ds_read_b128 a[76:79], v1 offset:3072\n"
    "v_mfma_f32_32x32x16_bf16 v[32:47], a[92:95], a[60:63], v[32:47]\n"
    "s_waitcnt lgkmcnt(3)\n"
    "ds_read_b128 a[80:83], v1 offset:4096\n"
    "v_mfma_f32_32x32x16_bf16 v[48:63], a[64:67], a[0:3], 0\n"
    "s_waitcnt lgkmcnt(3)\n"
    "ds_read_b128 a[84:87], v1 offset:5120\n"
    "v_mfma_f32_32x32x16_bf16 v[48:63], a[68:71], a[4:7], v[48:63]\n"
    "s_waitcnt lgkmcnt(3)\n"
    "s_add_u32 m0, s65, 0x10000\n"
    "ds_read_b128 a[88:91], v1 offset:6144\n"
    "v_mfma_f32_32x32x16_bf16 v[48:63], a[72:75], a[8:11], v[48:63]\n"
    "s_waitcnt lgkmcnt(3)\n"
    "s_add_u32 s64, s67, 0x18000\n"
    "ds_read_b128 a[92:95], v1 offset:7168\n"
    "v_max3_f32 v3, v32, v33, v3\n"
    "v_max3_f32 v4, v34, v35, v4\n"
    "v_max3_f32 v3, v36, v37, v3\n"
    "v_max3_f32 v4, v38, v39, v4\n"
    "v_mfma_f32_32x32x16_bf16 v[48:63], a[76:79], a[12:15], v[48:63]\n"
    "s_waitcnt lgkmcnt(3)\n"
    "ds_read_b128 a[64:67], v1 offset:8192\n"
    "buffer_load_dwordx4 v0, s[44:47], s64 offen offset:0 lds\n"
    "v_max3_f32 v3, v40, v41, v3\n"
    "v_max3_f32 v4, v42, v43, v4\n"
    "v_max3_f32 v3, v44, v45, v3\n"
    "v_mfma_f32_32x32x16_bf16 v[48:63], a[80:83], a[16:19], v[48:63]\n"
    "s_waitcnt lgkmcnt(3)\n"
    "ds_read_b128 a[68:71], v1 offset:9216\n"
    "v_max3_f32 v4, v46, v47, v4\n"
    "v_mfma_f32_32x32x16_bf16 v[48:63], a[84:87], a[20:23], v[48:63]\n"
    "s_waitcnt lgkmcnt(3)\n"
    "ds_read_b128 a[72:75], v1 offset:10240\n"
    "buffer_load_dwordx4 v0, s[44:47], s64 offen offset:1024 lds\n"
    "v_mfma_f32_32x32x16_bf16 v[48:63], a[88:91], a[24:27], v[48:63]\n"
    "s_waitcnt lgkmcnt(3)\n"
    "ds_read_b128 a[76:79], v1 offset:11264\n"
    "v_mfma_f32_32x32x16_bf16 v[48:63], a[92:95], a[28:31], v[48:63]\n"
    "s_waitcnt lgkmcnt(3)\n"
    "ds_read_b128 a[80:83], v1 offset:12288\n"
    "buffer_load_dwordx4 v0, s[44:47], s64 offen offset:2048 lds\n"
    "v_mfma_f32_32x32x16_bf16 v[48:63], a[64:67], a[32:35], v[48:63]\n"
    "s_waitcnt lgkmcnt(3)\n"
    "ds_read_b128 a[84:87], v1 offset:13312\n"
    "v_mfma_f32_32x32x16_bf16 v[48:63], a[68:71], a[36:39], v[48:63]\n"
    "s_waitcnt lgkmcnt(3)\n"
    "ds_read_b128 a[88:91], v1 offset:14336\n"
    "buffer_load_dwordx4 v0, s[44:47], s64 offen offset:3072 lds\n"
    "v_mfma_f32_32x32x16_bf16 v[48:63], a[72:75], a[40:43], v[48:63]\n"
    "s_waitcnt lgkmcnt(3)\n"
    "s_add_u32 m0, s65, 0x14000\n"
    "ds_read_b128 a[92:95], v1 offset:15360\n"
    "v_mfma_f32_32x32x16_bf16 v[48:63], a[76:79], a[44:47], v[48:63]\n"
    "s_waitcnt lgkmcnt(3)\n"
    "s_add_u32 s64, s67, 0x1c000\n"
    "ds_read_b128 a[64:67], v1 offset:16384\n"
    "v_mfma_f32_32x32x16_bf16 v[48:63], a[80:83], a[48:51], v[48:63]\n"
    "s_waitcnt lgkmcnt(3)\n"
    "ds_read_b128 a[68:71], v1 offset:17408\n"
    "buffer_load_dwordx4 v0, s[44:47], s64 offen offset:0 lds\n"
    "v_mfma_f32_32x32x16_bf16 v[48:63], a[84:87], a[52:55], v[48:63]\n"
    "s_waitcnt lgkmcnt(3)\n"
    "ds_read_b128 a[72:75], v1 offset:18432\n"
    "v_mfma_f32_32x32x16_bf16 v[48:63], a[88:91], a[56:59], v[48:63]\n"
    "s_waitcnt lgkmcnt(3)\n"
    "ds_read_b128 a[76:79], v1 offset:19456\n"
    "buffer_load_dwordx4 v0, s[44:47], s64 offen offset:1024 lds\n"
    "v_mfma_f32_32x32x16_bf16 v[48:63], a[92:95], a[60:63], v[48:63]\n"
    "s_waitcnt lgkmcnt(3)\n"
    "ds_read_b128 a[80:83], v1 offset:20480\n"
    "v_mfma_f32_32x32x16_bf16 v[64:79], a[64:67], a[0:3], 0\n"
    "s_waitcnt lgkmcnt(3)\n"
    "ds_read_b128 a[84:87], v1 offset:21504\n"
    "buffer_load_dwordx4 v0, s[44:47], s64 offen offset:2048 lds\n"
    "v_mfma_f32_32x32x16_bf16 v[64:79], a[68:71], a[4:7], v[64:79]\n"
    "s_waitcnt lgkmcnt(3)\n"
    "ds_read_b128 a[88:91], v1 offset:22528\n"
    "v_mfma_f32_32x32x16_bf16 v[64:79], a[72:75], a[8:11], v[64:79]\n"
    "s_waitcnt lgkmcnt(3)\n"
    "ds_read_b128 a[92:95], v1 offset:23552\n"
    "buffer_load_dwordx4 v0, s[44:47], s64 offen offset:3072 lds\n"
    "v_max3_f32 v3, v48, v49, v3\n"
    "v_max3_f32 v4, v50, v51, v4\n"
    "v_max3_f32 v3, v52, v53, v3\n"
    "v_mfma_f32_32x32x16_bf16 v[64:79], a[76:79], a[12:15], v[64:79]\n"
    "s_waitcnt lgkmcnt(3)\n"
    "ds_read_b128 a[64:67], v1 offset:24576\n"
    "v_max3_f32 v4, v54, v55, v4\n"
    "v_max3_f32 v3, v56, v57, v3\n"
    "v_max3_f32 v4, v58, v59, v4\n"
    "v_max3_f32 v3, v60, v61, v3\n"
    "v_max3_f32 v4, v62, v63, v4\n"
    "v_mfma_f32_32x32x16_bf16 v[64:79], a[80:83], a[16:19], v[64:79]\n"
    "s_waitcnt lgkmcnt(3)\n"
    "ds_read_b128 a[68:71], v1 offset:25600\n"
    "v_mfma_f32_32x32x16_bf16 v[64:79], a[84:87], a[20:23], v[64:79]\n"
    "s_waitcnt lgkmcnt(3)\n"
    "ds_read_b128 a[72:75], v1 offset:26624\n"
    "v_mfma_f32_32x32x16_bf16 v[64:79], a[88:91], a[24:27], v[64:79]\n"
    "s_waitcnt lgkmcnt(3)\n"
    "ds_read_b128 a[76:79], v1 offset:27648\n"
    "v_mfma_f32_32x32x16_bf16 v[64:79], a[92:95], a[28:31], v[64:79]\n"
    "s_waitcnt lgkmcnt(3)\n"
    "ds_read_b128 a[80:83], v1 offset:28672\n"
    "v_mfma_f32_32x32x16_bf16 v[64:79], a[64:67], a[32:35], v[64:79]\n"
    "s_waitcnt lgkmcnt(3)\n"
    "ds_read_b128 a[84:87], v1 offset:29696\n"
    "v_mfma_f32_32x32x16_bf16 v[64:79], a[68:71], a[36:39], v[64:79]\n"
    "s_waitcnt lgkmcnt(3)\n"
    "ds_read_b128 a[88:91], v1 offset:30720\n"
    "v_mfma_f32_32x32x16_bf16 v[64:79], a[72:75], a[40:43], v[64:79]\n"
    "s_waitcnt lgkmcnt(3)\n"
    "ds_read_b128 a[92:95], v1 offset:31744\n"
    "v_mfma_f32_32x32x16_bf16 v[64:79], a[76:79], a[44:47], v[64:79]\n"
    "s_waitcnt vmcnt(12) lgkmcnt(3)\n"
    "s_barrier\n"
    "ds_read_b128 a[64:67], v1 offset:32768\n"
    "v_mfma_f32_32x32x16_bf16 v[64:79], a[80:83], a[48:51], v[64:79]\n"
    "s_waitcnt lgkmcnt(3)\n"
    "ds_read_b128 a[68:71], v1 offset:33792\n"
    "v_mfma_f32_32x32x16_bf16 v[64:79], a[84:87], a[52:55], v[64:79]\n"
    "s_waitcnt lgkmcnt(3)\n"
    "ds_read_b128 a[72:75], v1 offset:34816\n"
    "v_mfma_f32_32x32x16_bf16 v[64:79], a[88:91], a[56:59], v[64:79]\n"
    "s_waitcnt lgkmcnt(3)\n"
    "ds_read_b128 a[76:79], v1 offset:35840\n"
    "v_mfma_f32_32x32x16_bf16 v[64:79], a[92:95], a[60:63], v[64:79]\n"
    "s_waitcnt lgkmcnt(3)\n"
    "ds_read_b128 a[80:83], v1 offset:36864\n"
    "v_mfma_f32_32x32x16_bf16 v[80:95], a[64:67], a[0:3], 0\n"
    "s_waitcnt lgkmcnt(3)\n"
    "ds_read_b128 a[84:87], v1 offset:37888\n"
    "v_mfma_f32_32x32x16_bf16 v[80:95], a[68:71], a[4:7], v[80:95]\n"
    "s_waitcnt lgkmcnt(3)\n"
    "s_add_u32 m0, s65, 0x18000\n"
    "ds_read_b128 a[88:91], v1 offset:38912\n"
    "v_mfma_f32_32x32x16_bf16 v[80:95], a[72:75], a[8:11], v[80:95]\n"
    "s_waitcnt lgkmcnt(3)\n"
    "s_add_u32 s64, s67, 0x20000\n"
    "ds_read_b128 a[92:95], v1 offset:39936\n"
    "v_max3_f32 v3, v64, v65, v3\n"
    "v_max3_f32 v4, v66, v67, v4\n"
    "v_max3_f32 v3, v68, v69, v3\n"
    "v_max3_f32 v4, v70, v71, v4\n"
    "v_mfma_f32_32x32x16_bf16 v[80:95], a[76:79], a[12:15], v[80:95]\n"
    "s_waitcnt lgkmcnt(3)\n"
    "ds_read_b128 a[64:67], v1 offset:40960\n"
    "buffer_load_dwordx4 v0, s[44:47], s64 offen offset:0 lds\n"
    "v_max3_f32 v3, v72, v73, v3\n"
    "v_max3_f32 v4, v74, v75, v4\n"
    "v_max3_f32 v3, v76, v77, v3\n"
    "v_mfma_f32_32x32x16_bf16 v[80:95], a[80:83], a[16:19], v[80:95]\n"
    "s_waitcnt lgkmcnt(3)\n"
    "ds_read_b128 a[68:71], v1 offset:41984\n"
    "v_max3_f32 v4, v78, v79, v4\n"
    "v_mfma_f32_32x32x16_bf16 v[80:95], a[84:87], a[20:23], v[80:95]\n"
    "s_waitcnt lgkmcnt(3)\n"
    "ds_read_b128 a[72:75], v1 offset:43008\n"
    "buffer_load_dwordx4 v0, s[44:47], s64 offen offset:1024 lds\n"
    "v_mfma_f32_32x32x16_bf16 v[80:95], a[88:91], a[24:27], v[80:95]\n"
    "s_waitcnt lgkmcnt(3)\n"
    "ds_read_b128 a[76:79], v1 offset:44032\n"
    "v_mfma_f32_32x32x16_bf16 v[80:95], a[92:95], a[28:31], v[80:95]\n"
    "s_waitcnt lgkmcnt(3)\n"
    "ds_read_b128 a[80:83], v1 offset:45056\n"
    "buffer_load_dwordx4 v0, s[44:47], s64 offen offset:2048 lds\n"
    "v_mfma_f32_32x32x16_bf16 v[80:95], a[64:67], a[32:35], v[80:95]\n"
    "s_waitcnt lgkmcnt(3)\n"
    "ds_read_b128 a[84:87], v1 offset:46080\n"
    "v_mfma_f32_32x32x16_bf16 v[80:95], a[68:71], a[36:39], v[80:95]\n"
    "s_waitcnt lgkmcnt(3)\n"
    "ds_read_b128 a[88:91], v1 offset:47104\n"
    "buffer_load_dwordx4 v0, s[44:47], s64 offen offset:3072 lds\n"
    "v_mfma_f32_32x32x16_bf16 v[80:95], a[72:75], a[40:43], v[80:95]\n"
    "s_waitcnt lgkmcnt(3)\n"
    "s_add_u32 m0, s65, 0x1c000\n"
    "ds_read_b128 a[92:95], v1 offset:48128\n"
    "v_mfma_f32_32x32x16_bf16 v[80:95], a[76:79], a[44:47], v[80:95]\n"
    "s_waitcnt lgkmcnt(3)\n"
    "s_add_u32 s64, s67, 0x24000\n"
    "ds_read_b128 a[64:67], v1 offset:49152\n"
    "v_mfma_f32_32x32x16_bf16 v[80:95], a[80:83], a[48:51], v[80:95]\n"
    "s_waitcnt lgkmcnt(3)\n"
    "ds_read_b128 a[68:71], v1 offset:50176\n"
    "buffer_load_dwordx4 v0, s[44:47], s64 offen offset:0 lds\n"
    "v_mfma_f32_32x32x16_bf16 v[80:95], a[84:87], a[52:55], v[80:95]\n"
    "s_waitcnt lgkmcnt(3)\n"
    "ds_read_b128 a[72:75], v1 offset:51200\n"
    "v_mfma_f32_32x32x16_bf16 v[80:95], a[88:91], a[56:59], v[80:95]\n"
    "s_waitcnt lgkmcnt(3)\n"
    "ds_read_b128 a[76:79], v1 offset:52224\n"
    "buffer_load_dwordx4 v0, s[44:47], s64 offen offset:1024 lds\n"
    "v_mfma_f32_32x32x16_bf16 v[80:95], a[92:95], a[60:63], v[80:95]\n"
    "s_waitcnt lgkmcnt(3)\n"
    "ds_read_b128 a[80:83], v1 offset:53248\n"
    "v_mfma_f32_32x32x16_bf16 v[96:111], a[64:67], a[0:3], 0\n"
    "s_waitcnt lgkmcnt(3)\n"
    "ds_read_b128 a[84:87], v1 offset:54272\n"
    "buffer_load_dwordx4 v0, s[44:47], s64 offen offset:2048 lds\n"
    "v_mfma_f32_32x32x16_bf16 v[96:111], a[68:71], a[4:7], v[96:111]\n"
    "s_waitcnt lgkmcnt(3)\n"
    "ds_read_b128 a[88:91], v1 offset:55296\n"
    "v_mfma_f32_32x32x16_bf16 v[96:111], a[72:75], a[8:11], v[96:111]\n"
    "s_waitcnt lgkmcnt(3)\n"
    "ds_read_b128 a[92:95], v1 offset:56320\n"
    "buffer_load_dwordx4 v0, s[44:47], s64 offen offset:3072 lds\n"
    "v_max3_f32 v3, v80, v81, v3\n"
    "v_max3_f32 v4, v82, v83, v4\n"
    "v_max3_f32 v3, v84, v85, v3\n"
    "v_mfma_f32_32x32x16_bf16 v[96:111], a[76:79], a[12:15], v[96:111]\n"
    "s_waitcnt lgkmcnt(3)\n"
    "ds_read_b128 a[64:67], v1 offset:57344\n"
    "v_max3_f32 v4, v86, v87, v4\n"
    "v_max3_f32 v3, v88, v89, v3\n"
    "v_max3_f32 v4, v90, v91, v4\n"
    "v_max3_f32 v3, v92, v93, v3\n"
    "v_max3_f32 v4, v94, v95, v4\n"
    "v_mfma_f32_32x32x16_bf16 v[96:111], a[80:83], a[16:19], v[96:111]\n"
    "s_waitcnt lgkmcnt(3)\n"
    "ds_read_b128 a[68:71], v1 offset:58368\n"
    "v_mfma_f32_32x32x16_bf16 v[96:111], a[84:87], a[20:23], v[96:111]\n"
    "s_waitcnt vmcnt(16) lgkmcnt(3)\n"
    "ds_read_b128 a[72:75], v1 offset:59392\n"
    "v_accvgpr_read_b32 v15, a120\n"
    "v_mfma_f32_32x32x16_bf16 v[96:111], a[88:91], a[24:27], v[96:111]\n"
    "s_waitcnt lgkmcnt(3)\n"
    "ds_read_b128 a[76:79], v1 offset:60416\n"
    "ds_read_b128 a[240:243], v15 offset:0\n"
    "v_mfma_f32_32x32x16_bf16 v[96:111], a[92:95], a[28:31], v[96:111]\n"
    "s_waitcnt lgkmcnt(4)\n"
    "ds_read_b128 a[80:83], v1 offset:61440\n"
    "ds_read_b128 a[244:247], v15 offset:2048\n"
    "v_mfma_f32_32x32x16_bf16 v[96:111], a[64:67], a[32:35], v[96:111]\n"
    "s_waitcnt lgkmcnt(5)\n"
    "ds_read_b128 a[84:87], v1 offset:62464\n"
    "v_mfma_f32_32x32x16_bf16 v[96:111], a[68:71], a[36:39], v[96:111]\n"
    "s_waitcnt lgkmcnt(5)\n"
    "ds_read_b128 a[88:91], v1 offset:63488\n"
    "v_mfma_f32_32x32x16_bf16 v[96:111], a[72:75], a[40:43], v[96:111]\n"
    "s_waitcnt lgkmcnt(5)\n"
    "ds_read_b128 a[92:95], v1 offset:64512\n"
    "v_mfma_f32_32x32x16_bf16 v[96:111], a[76:79], a[44:47], v[96:111]\n"
    "s_waitcnt vmcnt(8) lgkmcnt(4)\n"
    "s_barrier\n"
    "ds_read_b128 a[64:67], v2 offset:0\n"
    "buffer_store_dwordx4 a[240:243], v0, s[88:91], s86 offen offset:0 sc1\n"
    "v_mfma_f32_32x32x16_bf16 v[96:111], a[80:83], a[48:51], v[96:111]\n"
    "s_waitcnt lgkmcnt(3)\n"
    "ds_read_b128 a[68:71], v2 offset:1024\n"
    "buffer_store_dwordx4 a[244:247], v0, s[88:91], s86 offen offset:1024 sc1\n"
    "v_mfma_f32_32x32x16_bf16 v[96:111], a[84:87], a[52:55], v[96:111]\n"
    "s_waitcnt lgkmcnt(3)\n"
    "ds_read_b128 a[72:75], v2 offset:2048\n"
    "v_accvgpr_read_b32 v15, a121\n"
    "v_mfma_f32_32x32x16_bf16 v[96:111], a[88:91], a[56:59], v[96:111]\n"
    "s_waitcnt lgkmcnt(3)\n"
    "ds_read_b128 a[76:79], v2 offset:3072\n"
    "ds_read_b128 a[248:251], v15 offset:0\n"
    "v_mfma_f32_32x32x16_bf16 v[96:111], a[92:95], a[60:63], v[96:111]\n"
    "s_waitcnt lgkmcnt(4)\n"
    "ds_read_b128 a[80:83], v2 offset:4096\n"
    "ds_read_b128 a[252:255], v15 offset:2048\n"
    "v_mfma_f32_32x32x16_bf16 v[112:127], a[64:67], a[0:3], 0\n"
    "s_waitcnt lgkmcnt(5)\n"
    "ds_read_b128 a[84:87], v2 offset:5120\n"
    "v_mfma_f32_32x32x16_bf16 v[112:127], a[68:71], a[4:7], v[112:127]\n"
    "s_waitcnt lgkmcnt(5)\n"
    "s_add_u32 m0, s65, 0x0\n"
    "ds_read_b128 a[88:91], v2 offset:6144\n"
    "v_mfma_f32_32x32x16_bf16 v[112:127], a[72:75], a[8:11], v[112:127]\n"
    "s_waitcnt lgkmcnt(5)\n"
    "s_add_u32 s64, s67, 0x28000\n"
    "ds_read_b128 a[92:95], v2 offset:7168\n"
    "v_max3_f32 v3, v96, v97, v3\n"
    "v_max3_f32 v4, v98, v99, v4\n"
    "v_max3_f32 v3, v100, v101, v3\n"
    "v_max3_f32 v4, v102, v103, v4\n"
    "v_mfma_f32_32x32x16_bf16 v[112:127], a[76:79], a[12:15], v[112:127]\n"
    "s_waitcnt lgkmcnt(4)\n"
    "ds_read_b128 a[64:67], v2 offset:8192\n"
    "buffer_load_dwordx4 v0, s[44:47], s64 offen offset:0 lds\n"
    "buffer_store_dwordx4 a[248:251], v0, s[88:91], s87 offen offset:0 sc1\n"
    "v_mfma_f32_32x32x16_bf16 v[112:127], a[80:83], a[16:19], v[112:127]\n"
    "s_waitcnt lgkmcnt(3)\n"
    "ds_read_b128 a[68:71], v2 offset:9216\n"
    "buffer_store_dwordx4 a[252:255], v0, s[88:91], s87 offen offset:1024 sc1\n"
    "v_mfma_f32_32x32x16_bf16 v[112:127], a[84:87], a[20:23], v[112:127]\n"
    "s_waitcnt lgkmcnt(3)\n"
    "ds_read_b128 a[72:75], v2 offset:10240\n"
    "buffer_load_dwordx4 v0, s[44:47], s64 offen offset:1024 lds\n"
    "v_max3_f32 v3, v104, v105, v3\n"
    "v_max3_f32 v4, v106, v107, v4\n"
    "v_max3_f32 v3, v108, v109, v3\n"
    "v_mfma_f32_32x32x16_bf16 v[112:127], a[88:91], a[24:27], v[112:127]\n"
    "s_waitcnt lgkmcnt(3)\n"
    "ds_read_b128 a[76:79], v2 offset:11264\n"
    "v_max3_f32 v4, v110, v111, v4\n"
    "v_mfma_f32_32x32x16_bf16 v[112:127], a[92:95], a[28:31], v[112:127]\n"
    "s_waitcnt lgkmcnt(3)\n"
    "ds_read_b128 a[80:83], v2 offset:12288\n"
    "buffer_load_dwordx4 v0, s[44:47], s64 offen offset:2048 lds\n"
    "v_mfma_f32_32x32x16_bf16 v[112:127], a[64:67], a[32:35], v[112:127]\n"
    "s_waitcnt lgkmcnt(3)\n"
    "ds_read_b128 a[84:87], v2 offset:13312\n"
    "v_mfma_f32_32x32x16_bf16 v[112:127], a[68:71], a[36:39], v[112:127]\n"
    "s_waitcnt lgkmcnt(3)\n"
    "ds_read_b128 a[88:91], v2 offset:14336\n"
    "buffer_load_dwordx4 v0, s[44:47], s64 offen offset:3072 lds\n"
    "v_mfma_f32_32x32x16_bf16 v[112:127], a[72:75], a[40:43], v[112:127]\n"
    "s_waitcnt lgkmcnt(3)\n"
    "s_add_u32 m0, s65, 0x4000\n"
    "ds_read_b128 a[92:95], v2 offset:15360\n"
    "v_mfma_f32_32x32x16_bf16 v[112:127], a[76:79], a[44:47], v[112:127]\n"
    "s_waitcnt lgkmcnt(3)\n"
    "s_add_u32 s64, s67, 0x2c000\n"
    "ds_read_b128 a[64:67], v2 offset:16384\n"
    "v_mfma_f32_32x32x16_bf16 v[112:127], a[80:83], a[48:51], v[112:127]\n"
    "s_waitcnt lgkmcnt(3)\n"
    "ds_read_b128 a[68:71], v2 offset:17408\n"
    "buffer_load_dwordx4 v0, s[44:47], s64 offen offset:0 lds\n"
    "v_mfma_f32_32x32x16_bf16 v[112:127], a[84:87], a[52:55], v[112:127]\n"
    "s_waitcnt lgkmcnt(3)\n"
    "ds_read_b128 a[72:75], v2 offset:18432\n"
    "v_mfma_f32_32x32x16_bf16 v[112:127], a[88:91], a[56:59], v[112:127]\n"
    "s_waitcnt lgkmcnt(3)\n"
    "ds_read_b128 a[76:79], v2 offset:19456\n"
    "buffer_load_dwordx4 v0, s[44:47], s64 offen offset:1024 lds\n"
    "v_mfma_f32_32x32x16_bf16 v[112:127], a[92:95], a[60:63], v[112:127]\n"
    "s_waitcnt lgkmcnt(3)\n"
    "ds_read_b128 a[80:83], v2 offset:20480\n"
    "v_mfma_f32_32x32x16_bf16 v[128:143], a[64:67], a[0:3], 0\n"
    "s_waitcnt lgkmcnt(3)\n"
    "ds_read_b128 a[84:87], v2 offset:21504\n"
    "buffer_load_dwordx4 v0, s[44:47], s64 offen offset:2048 lds\n"
    "v_mfma_f32_32x32x16_bf16 v[128:143], a[68:71], a[4:7], v[128:143]\n"
    "s_waitcnt lgkmcnt(3)\n"
    "ds_read_b128 a[88:91], v2 offset:22528\n"
    "v_mfma_f32_32x32x16_bf16 v[128:143], a[72:75], a[8:11], v[128:143]\n"
    "s_waitcnt lgkmcnt(3)\n"
    "ds_read_b128 a[92:95], v2 offset:23552\n"
    "buffer_load_dwordx4 v0, s[44:47], s64 offen offset:3072 lds\n"
    "v_max3_f32 v3, v112, v113, v3\n"
    "v_max3_f32 v4, v114, v115, v4\n"
    "v_max3_f32 v3, v116, v117, v3\n"
    "v_mfma_f32_32x32x16_bf16 v[128:143], a[76:79], a[12:15], v[128:143]\n"
    "s_waitcnt lgkmcnt(3)\n"
    "s_mov_b32 m0, s84\n"
    "ds_read_b128 a[64:67], v2 offset:24576\n"
    "v_accvgpr_read_b32 v15, a118\n"
    "v_accvgpr_read_b32 v10, a119\n"
    "v_max3_f32 v4, v118, v119, v4\n"
    "v_max3_f32 v3, v120, v121, v3\n"
    "v_mfma_f32_32x32x16_bf16 v[128:143], a[80:83], a[16:19], v[128:143]\n"
    "s_waitcnt lgkmcnt(3)\n"
    "s_mov_b32 s85, 0x20000\n"
    "ds_read_b128 a[68:71], v2 offset:25600\n"
    "buffer_load_dwordx4 v15, s[24:27], s85 offen offset:0 lds\n"
    "v_max3_f32 v4, v122, v123, v4\n"
    "v_max3_f32 v3, v124, v125, v3\n"
    "v_mfma_f32_32x32x16_bf16 v[128:143], a[84:87], a[20:23], v[128:143]\n"
    "s_waitcnt lgkmcnt(3)\n"
    "s_mov_b32 s85, 0x27c00\n"
    "ds_read_b128 a[72:75], v2 offset:26624\n"
    "buffer_load_dwordx4 v10, s[24:27], s85 offen offset:1024 lds\n"
    "v_max3_f32 v4, v126, v127, v4\n"
    "v_mfma_f32_32x32x16_bf16 v[128:143], a[88:91], a[24:27], v[128:143]\n"
    "s_waitcnt lgkmcnt(3)\n"
    "s_mov_b32 s85, 0x2f800\n"
    "ds_read_b128 a[76:79], v2 offset:27648\n"
    "buffer_load_dwordx4 v15, s[24:27], s85 offen offset:2048 lds\n"
    "v_mfma_f32_32x32x16_bf16 v[128:143], a[92:95], a[28:31], v[128:143]\n"
    "s_waitcnt lgkmcnt(3)\n"
    "s_mov_b32 s85, 0x37400\n"
    "ds_read_b128 a[80:83], v2 offset:28672\n"
    "buffer_load_dwordx4 v10, s[24:27], s85 offen offset:3072 lds\n"
    "v_mfma_f32_32x32x16_bf16 v[128:143], a[64:67], a[32:35], v[128:143]\n"
    "s_waitcnt lgkmcnt(3)\n"
    "ds_read_b128 a[84:87], v2 offset:29696\n"
    "v_mfma_f32_32x32x16_bf16 v[128:143], a[68:71], a[36:39], v[128:143]\n"
    "s_waitcnt lgkmcnt(3)\n"
    "ds_read_b128 a[88:91], v2 offset:30720\n"
    "v_mfma_f32_32x32x16_bf16 v[128:143], a[72:75], a[40:43], v[128:143]\n"
    "s_waitcnt lgkmcnt(3)\n"
    "ds_read_b128 a[92:95], v2 offset:31744\n"
    "v_mfma_f32_32x32x16_bf16 v[128:143], a[76:79], a[44:47], v[128:143]\n"
    "s_waitcnt vmcnt(16) lgkmcnt(3)\n"
    "s_barrier\n"
    "ds_read_b128 a[64:67], v2 offset:32768\n"
    "v_mfma_f32_32x32x16_bf16 v[128:143], a[80:83], a[48:51], v[128:143]\n"
    "s_waitcnt lgkmcnt(3)\n"
    "ds_read_b128 a[68:71], v2 offset:33792\n"
    "v_mfma_f32_32x32x16_bf16 v[128:143], a[84:87], a[52:55], v[128:143]\n"
    "s_waitcnt lgkmcnt(3)\n"
    "ds_read_b128 a[72:75], v2 offset:34816\n"
    "v_mfma_f32_32x32x16_bf16 v[128:143], a[88:91], a[56:59], v[128:143]\n"
    "s_waitcnt lgkmcnt(3)\n"
    "ds_read_b128 a[76:79], v2 offset:35840\n"
    "v_mfma_f32_32x32x16_bf16 v[128:143], a[92:95], a[60:63], v[128:143]\n"
    "s_waitcnt lgkmcnt(3)\n"
    "ds_read_b128 a[80:83], v2 offset:36864\n"
    "v_mfma_f32_32x32x16_bf16 a[96:111], a[64:67], a[0:3], 0\n"
    "s_waitcnt lgkmcnt(3)\n"
    "ds_read_b128 a[84:87], v2 offset:37888\n"
    "v_mfma_f32_32x32x16_bf16 a[96:111], a[68:71], a[4:7], a[96:111]\n"
    "s_waitcnt lgkmcnt(3)\n"
    "s_add_u32 m0, s65, 0x8000\n"
    "ds_read_b128 a[88:91], v2 offset:38912\n"
    "v_mfma_f32_32x32x16_bf16 a[96:111], a[72:75], a[8:11], a[96:111]\n"
    "s_waitcnt lgkmcnt(3)\n"
    "s_add_u32 s64, s67, 0x30000\n"
    "ds_read_b128 a[92:95], v2 offset:39936\n"
    "v_max3_f32 v3, v128, v129, v3\n"
    "v_max3_f32 v4, v130, v131, v4\n"
    "v_max3_f32 v3, v132, v133, v3\n"
    "v_max3_f32 v4, v134, v135, v4\n"
    "v_mfma_f32_32x32x16_bf16 a[96:111], a[76:79], a[12:15], a[96:111]\n"
    "s_waitcnt lgkmcnt(3)\n"
    "ds_read_b128 a[64:67], v2 offset:40960\n"
    "buffer_load_dwordx4 v0, s[44:47], s64 offen offset:0 lds\n"
    "v_max3_f32 v3, v136, v137, v3\n"
    "v_max3_f32 v4, v138, v139, v4\n"
    "v_max3_f32 v3, v140, v141, v3\n"
    "v_mfma_f32_32x32x16_bf16 a[96:111], a[80:83], a[16:19], a[96:111]\n"
    "s_waitcnt lgkmcnt(3)\n"
    "ds_read_b128 a[68:71], v2 offset:41984\n"
    "v_max3_f32 v4, v142, v143, v4\n"
    "v_mfma_f32_32x32x16_bf16 a[96:111], a[84:87], a[20:23], a[96:111]\n"
    "s_waitcnt lgkmcnt(3)\n"
    "ds_read_b128 a[72:75], v2 offset:43008\n"
    "buffer_load_dwordx4 v0, s[44:47], s64 offen offset:1024 lds\n"
    "v_mfma_f32_32x32x16_bf16 a[96:111], a[88:91], a[24:27], a[96:111]\n"
    "s_waitcnt lgkmcnt(3)\n"
    "ds_read_b128 a[76:79], v2 offset:44032\n"
    "v_mfma_f32_32x32x16_bf16 a[96:111], a[92:95], a[28:31], a[96:111]\n"
    "s_waitcnt lgkmcnt(3)\n"
    "ds_read_b128 a[80:83], v2 offset:45056\n"
    "buffer_load_dwordx4 v0, s[44:47], s64 offen offset:2048 lds\n"
    "v_mfma_f32_32x32x16_bf16 a[96:111], a[64:67], a[32:35], a[96:111]\n"
    "s_waitcnt lgkmcnt(3)\n"
    "ds_read_b128 a[84:87], v2 offset:46080\n"
    "v_mfma_f32_32x32x16_bf16 a[96:111], a[68:71], a[36:39], a[96:111]\n"
    "s_waitcnt lgkmcnt(3)\n"
    "ds_read_b128 a[88:91], v2 offset:47104\n"
    "buffer_load_dwordx4 v0, s[44:47], s64 offen offset:3072 lds\n"
    "v_max_f32 v3, v3, v4\n"
    "v_mov_b32 v4, v3\n"
    "v_mfma_f32_32x32x16_bf16 a[96:111], a[72:75], a[40:43], a[96:111]\n"
    "s_waitcnt lgkmcnt(3)\n"
    "s_add_u32 m0, s65, 0xc000\n"
    "ds_read_b128 a[92:95], v2 offset:48128\n"
    "v_permlane32_swap_b32 v3, v4\n"
    "v_max_f32 v5, v3, v4\n"
    "v_add_f32 v5, 0x42a00000, v5\n"
    "v_mfma_f32_32x32x16_bf16 a[96:111], a[76:79], a[44:47], a[96:111]\n"
    "s_waitcnt lgkmcnt(3)\n"
    "s_add_u32 s64, s67, 0x34000\n"
    "ds_read_b128 a[64:67], v2 offset:49152\n"
    "v_mfma_f32_32x32x16_bf16 a[96:111], a[80:83], a[48:51], a[96:111]\n"
    "s_waitcnt lgkmcnt(3)\n"
    "ds_read_b128 a[68:71], v2 offset:50176\n"
    "buffer_load_dwordx4 v0, s[44:47], s64 offen offset:0 lds\n"
    "v_mfma_f32_32x32x16_bf16 a[96:111], a[84:87], a[52:55], a[96:111]\n"
    "s_waitcnt lgkmcnt(3)\n"
    "ds_read_b128 a[72:75], v2 offset:51200\n"
    "v_mfma_f32_32x32x16_bf16 a[96:111], a[88:91], a[56:59], a[96:111]\n"
    "s_waitcnt lgkmcnt(3)\n"
    "ds_read_b128 a[76:79], v2 offset:52224\n"
    "buffer_load_dwordx4 v0, s[44:47], s64 offen offset:1024 lds\n"
    "v_mfma_f32_32x32x16_bf16 a[96:111], a[92:95], a[60:63], a[96:111]\n"
    "s_waitcnt lgkmcnt(3)\n"
    "ds_read_b128 a[80:83], v2 offset:53248\n"
    "v_sub_f32 v16, v16, v5\n"
    "v_sub_f32 v17, v17, v5\n"
    "v_exp_f32 v16, v16\n"
    "v_sub_f32 v18, v18, v5\n"
    "v_mfma_f32_32x32x16_bf16 v[144:159], a[64:67], a[0:3], 0\n"
    "s_waitcnt lgkmcnt(3)\n"
    "ds_read_b128 a[84:87], v2 offset:54272\n"
    "buffer_load_dwordx4 v0, s[44:47], s64 offen offset:2048 lds\n"
    "v_exp_f32 v17, v17\n"
    "v_sub_f32 v19, v19, v5\n"
    "v_mfma_f32_32x32x16_bf16 v[144:159], a[68:71], a[4:7], v[144:159]\n"
    "s_waitcnt lgkmcnt(3)\n"
    "ds_read_b128 a[88:91], v2 offset:55296\n"
    "v_exp_f32 v18, v18\n"
    "v_mov_b32 v6, v16\n"
    "v_sub_f32 v20, v20, v5\n"
    "v_mfma_f32_32x32x16_bf16 v[144:159], a[72:75], a[8:11], v[144:159]\n"
    "s_waitcnt lgkmcnt(3)\n"
    "ds_read_b128 a[92:95], v2 offset:56320\n"
    "buffer_load_dwordx4 v0, s[44:47], s64 offen offset:3072 lds\n"
    "v_accvgpr_read_b32 v9, a96\n"
    "v_accvgpr_read_b32 v10, a97\n"
    "v_max_f32 v3, v9, v10\n"
    "v_mfma_f32_32x32x16_bf16 v[144:159], a[76:79], a[12:15], v[144:159]\n"
    "s_waitcnt lgkmcnt(3)\n"
    "ds_read_b128 a[64:67], v2 offset:57344\n"
    "v_accvgpr_read_b32 v9, a98\n"
    "v_accvgpr_read_b32 v10, a99\n"
    "v_max_f32 v4, v9, v10\n"
    "v_accvgpr_read_b32 v9, a100\n"
    "v_accvgpr_read_b32 v10, a101\n"
    "v_mfma_f32_32x32x16_bf16 v[144:159], a[80:83], a[16:19], v[144:159]\n"
    "s_waitcnt lgkmcnt(3)\n"
    "ds_read_b128 a[68:71], v2 offset:58368\n"
    "v_max3_f32 v3, v9, v10, v3\n"
    "v_accvgpr_read_b32 v9, a102\n"
    "v_accvgpr_read_b32 v10, a103\n"
    "v_max3_f32 v4, v9, v10, v4\n"
    "v_accvgpr_read_b32 v9, a104\n"
    "v_mfma_f32_32x32x16_bf16 v[144:159], a[84:87], a[20:23], v[144:159]\n"
    "s_waitcnt lgkmcnt(3)\n"
    "ds_read_b128 a[72:75], v2 offset:59392\n"
    "v_accvgpr_read_b32 v10, a105\n"
    "v_max3_f32 v3, v9, v10, v3\n"
    "v_accvgpr_read_b32 v9, a106\n"
    "v_accvgpr_read_b32 v10, a107\n"
    "v_max3_f32 v4, v9, v10, v4\n"
    "v_mfma_f32_32x32x16_bf16 v[144:159], a[88:91], a[24:27], v[144:159]\n"
    "s_waitcnt lgkmcnt(3)\n"
    "ds_read_b128 a[76:79], v2 offset:60416\n"
    "v_accvgpr_read_b32 v9, a108\n"
    "v_accvgpr_read_b32 v10, a109\n"
    "v_max3_f32 v3, v9, v10, v3\n"
    "v_accvgpr_read_b32 v9, a110\n"
    "v_accvgpr_read_b32 v10, a111\n"
    "v_mfma_f32_32x32x16_bf16 v[144:159], a[92:95], a[28:31], v[144:159]\n"
    "s_waitcnt lgkmcnt(3)\n"
    "ds_read_b128 a[80:83], v2 offset:61440\n"
    "v_max3_f32 v4, v9, v10, v4\n"
    "v_exp_f32 v19, v19\n"
    "v_add_f32 v6, v6, v17\n"
    "v_sub_f32 v21, v21, v5\n"
    "v_mfma_f32_32x32x16_bf16 v[144:159], a[64:67], a[32:35], v[144:159]\n"
    "s_waitcnt lgkmcnt(3)\n"
    "ds_read_b128 a[84:87], v2 offset:62464\n"
    "v_exp_f32 v20, v20\n"
    "v_add_f32 v6, v6, v18\n"
    "v_sub_f32 v22, v22, v5\n"
    "v_mfma_f32_32x32x16_bf16 v[144:159], a[68:71], a[36:39], v[144:159]\n"
    "s_waitcnt lgkmcnt(3)\n"
    "ds_read_b128 a[88:91], v2 offset:63488\n"
    "v_exp_f32 v21, v21\n"
    "v_add_f32 v6, v6, v19\n"
    "v_sub_f32 v23, v23, v5\n"
    "v_mfma_f32_32x32x16_bf16 v[144:159], a[72:75], a[40:43], v[144:159]\n"
    "s_waitcnt lgkmcnt(3)\n"
    "ds_read_b128 a[92:95], v2 offset:64512\n"
    "v_exp_f32 v22, v22\n"
    "v_add_f32 v6, v6, v20\n"
    "v_exp_f32 v23, v23\n"
    "v_mfma_f32_32x32x16_bf16 v[144:159], a[76:79], a[44:47], v[144:159]\n"
    "s_waitcnt vmcnt(12) lgkmcnt(3)\n"
    "s_barrier\n"
    "ds_read_b128 a[64:67], v1 offset:0\n"
    "v_add_f32 v6, v6, v21\n"
    "v_add_f32 v6, v6, v22\n"
    "v_add_f32 v6, v6, v23\n"
    "v_sub_f32 v24, v24, v5\n"
    "v_mfma_f32_32x32x16_bf16 v[144:159], a[80:83], a[48:51], v[144:159]\n"
    "s_waitcnt lgkmcnt(3)\n"
    "ds_read_b128 a[68:71], v1 offset:1024\n"
    "v_sub_f32 v25, v25, v5\n"
    "v_exp_f32 v24, v24\n"
    "v_sub_f32 v26, v26, v5\n"
    "v_mfma_f32_32x32x16_bf16 v[144:159], a[84:87], a[52:55], v[144:159]\n"
    "s_waitcnt lgkmcnt(3)\n"
    "ds_read_b128 a[72:75], v1 offset:2048\n"
    "v_exp_f32 v25, v25\n"
    "v_sub_f32 v27, v27, v5\n"
    "v_exp_f32 v26, v26\n"
    "v_mfma_f32_32x32x16_bf16 v[144:159], a[88:91], a[56:59], v[144:159]\n"
    "s_waitcnt lgkmcnt(3)\n"
    "ds_read_b128 a[76:79], v1 offset:3072\n"
    "v_add_f32 v6, v6, v24\n"
    "v_sub_f32 v28, v28, v5\n"
    "v_exp_f32 v27, v27\n"
    "v_add_f32 v6, v6, v25\n"
    "v_mfma_f32_32x32x16_bf16 v[144:159], a[92:95], a[60:63], v[144:159]\n"
    "s_waitcnt lgkmcnt(3)\n"
    "ds_read_b128 a[80:83], v1 offset:4096\n"
    "v_sub_f32 v29, v29, v5\n"
    "v_exp_f32 v28, v28\n"
    "v_add_f32 v6, v6, v26\n"
    "v_sub_f32 v30, v30, v5\n"
    "v_mfma_f32_32x32x16_bf16 v[160:175], a[64:67], a[0:3], 0\n"
    "s_waitcnt lgkmcnt(3)\n"
    "ds_read_b128 a[84:87], v1 offset:5120\n"
    "v_exp_f32 v29, v29\n"
    "v_add_f32 v6, v6, v27\n"
    "v_sub_f32 v31, v31, v5\n"
    "v_mfma_f32_32x32x16_bf16 v[160:175], a[68:71], a[4:7], v[160:175]\n"
    "s_waitcnt lgkmcnt(3)\n"
    "s_add_u32 m0, s65, 0x10000\n"
    "ds_read_b128 a[88:91], v1 offset:6144\n"
    "v_exp_f32 v30, v30\n"
    "v_add_f32 v6, v6, v28\n"
    "v_mfma_f32_32x32x16_bf16 v[160:175], a[72:75], a[8:11], v[160:175]\n"
    "s_waitcnt lgkmcnt(3)\n"
    "s_add_u32 s64, s67, 0x38000\n"
    "ds_read_b128 a[92:95], v1 offset:7168\n"
    "v_max3_f32 v3, v144, v145, v3\n"
    "v_max3_f32 v4, v146, v147, v4\n"
    "v_max3_f32 v3, v148, v149, v3\n"
    "v_max3_f32 v4, v150, v151, v4\n"
    "v_mfma_f32_32x32x16_bf16 v[160:175], a[76:79], a[12:15], v[160:175]\n"
    "s_waitcnt lgkmcnt(3)\n"
    "ds_read_b128 a[64:67], v1 offset:8192\n"
    "buffer_load_dwordx4 v0, s[44:47], s64 offen offset:0 lds\n"
    "v_max3_f32 v3, v152, v153, v3\n"
    "v_max3_f32 v4, v154, v155, v4\n"
    "v_max3_f32 v3, v156, v157, v3\n"
    "v_mfma_f32_32x32x16_bf16 v[160:175], a[80:83], a[16:19], v[160:175]\n"
    "s_waitcnt lgkmcnt(3)\n"
    "ds_read_b128 a[68:71], v1 offset:9216\n"
    "v_max3_f32 v4, v158, v159, v4\n"
    "v_exp_f32 v31, v31\n"
    "v_add_f32 v6, v6, v29\n"
    "v_add_f32 v6, v6, v30\n"
    "v_mfma_f32_32x32x16_bf16 v[160:175], a[84:87], a[20:23], v[160:175]\n"
    "s_waitcnt lgkmcnt(3)\n"
    "ds_read_b128 a[72:75], v1 offset:10240\n"
    "buffer_load_dwordx4 v0, s[44:47], s64 offen offset:1024 lds\n"
    "v_add_f32 v6, v6, v31\n"
    "v_sub_f32 v32, v32, v5\n"
    "v_sub_f32 v33, v33, v5\n"
    "v_mfma_f32_32x32x16_bf16 v[160:175], a[88:91], a[24:27], v[160:175]\n"
    "s_waitcnt lgkmcnt(3)\n"
    "ds_read_b128 a[76:79], v1 offset:11264\n"
    "v_exp_f32 v32, v32\n"
    "v_sub_f32 v34, v34, v5\n"
    "v_exp_f32 v33, v33\n"
    "v_mfma_f32_32x32x16_bf16 v[160:175], a[92:95], a[28:31], v[160:175]\n"
    "s_waitcnt lgkmcnt(3)\n"
    "ds_read_b128 a[80:83], v1 offset:12288\n"
    "buffer_load_dwordx4 v0, s[44:47], s64 offen offset:2048 lds\n"
    "v_sub_f32 v35, v35, v5\n"
    "v_exp_f32 v34, v34\n"
    "v_mfma_f32_32x32x16_bf16 v[160:175], a[64:67], a[32:35], v[160:175]\n"
    "s_waitcnt lgkmcnt(3)\n"
    "ds_read_b128 a[84:87], v1 offset:13312\n"
    "v_add_f32 v6, v6, v32\n"
    "v_sub_f32 v36, v36, v5\n"
    "v_exp_f32 v35, v35\n"
    "v_add_f32 v6, v6, v33\n"
    "v_mfma_f32_32x32x16_bf16 v[160:175], a[68:71], a[36:39], v[160:175]\n"
    "s_waitcnt lgkmcnt(3)\n"
    "ds_read_b128 a[88:91], v1 offset:14336\n"
    "buffer_load_dwordx4 v0, s[44:47], s64 offen offset:3072 lds\n"
    "v_sub_f32 v37, v37, v5\n"
    "v_exp_f32 v36, v36\n"
    "v_mfma_f32_32x32x16_bf16 v[160:175], a[72:75], a[40:43], v[160:175]\n"
    "s_waitcnt lgkmcnt(3)\n"
    "s_add_u32 m0, s65, 0x14000\n"
    "ds_read_b128 a[92:95], v1 offset:15360\n"
    "v_add_f32 v6, v6, v34\n"
    "v_sub_f32 v38, v38, v5\n"
    "v_exp_f32 v37, v37\n"
    "v_mfma_f32_32x32x16_bf16 v[160:175], a[76:79], a[44:47], v[160:175]\n"
    "s_waitcnt lgkmcnt(3)\n"
    "s_add_u32 s64, s67, 0x3c000\n"
    "ds_read_b128 a[64:67], v1 offset:16384\n"
    "v_add_f32 v6, v6, v35\n"
    "v_sub_f32 v39, v39, v5\n"
    "v_exp_f32 v38, v38\n"
    "v_mfma_f32_32x32x16_bf16 v[160:175], a[80:83], a[48:51], v[160:175]\n"
    "s_waitcnt lgkmcnt(3)\n"
    "ds_read_b128 a[68:71], v1 offset:17408\n"
    "buffer_load_dwordx4 v0, s[44:47], s64 offen offset:0 lds\n"
    "v_add_f32 v6, v6, v36\n"
    "v_exp_f32 v39, v39\n"
    "v_mfma_f32_32x32x16_bf16 v[160:175], a[84:87], a[52:55], v[160:175]\n"
    "s_waitcnt lgkmcnt(3)\n"
    "ds_read_b128 a[72:75], v1 offset:18432\n"
    "v_add_f32 v6, v6, v37\n"
    "v_add_f32 v6, v6, v38\n"
    "v_add_f32 v6, v6, v39\n"
    "v_sub_f32 v40, v40, v5\n"
    "v_sub_f32 v41, v41, v5\n"
    "v_mfma_f32_32x32x16_bf16 v[160:175], a[88:91], a[56:59], v[160:175]\n"
    "s_waitcnt lgkmcnt(3)\n"
    "ds_read_b128 a[76:79], v1 offset:19456\n"
    "buffer_load_dwordx4 v0, s[44:47], s64 offen offset:1024 lds\n"
    "v_exp_f32 v40, v40\n"
    "v_sub_f32 v42, v42, v5\n"
    "v_mfma_f32_32x32x16_bf16 v[160:175], a[92:95], a[60:63], v[160:175]\n"
    "s_waitcnt lgkmcnt(3)\n"
    "ds_read_b128 a[80:83], v1 offset:20480\n"
    "v_exp_f32 v41, v41\n"
    "v_sub_f32 v43, v43, v5\n"
    "v_exp_f32 v42, v42\n"
    "v_mfma_f32_32x32x16_bf16 v[176:191], a[64:67], a[0:3], 0\n"
    "s_waitcnt lgkmcnt(3)\n"
    "ds_read_b128 a[84:87], v1 offset:21504\n"
    "buffer_load_dwordx4 v0, s[44:47], s64 offen offset:2048 lds\n"
    "v_add_f32 v6, v6, v40\n"
    "v_sub_f32 v44, v44, v5\n"
    "v_mfma_f32_32x32x16_bf16 v[176:191], a[68:71], a[4:7], v[176:191]\n"
    "s_waitcnt lgkmcnt(3)\n"
    "ds_read_b128 a[88:91], v1 offset:22528\n"
    "v_exp_f32 v43, v43\n"
    "v_add_f32 v6, v6, v41\n"
    "v_sub_f32 v45, v45, v5\n"
    "v_mfma_f32_32x32x16_bf16 v[176:191], a[72:75], a[8:11], v[176:191]\n"
    "s_waitcnt lgkmcnt(3)\n"
    "ds_read_b128 a[92:95], v1 offset:23552\n"
    "buffer_load_dwordx4 v0, s[44:47], s64 offen offset:3072 lds\n"
    "v_max3_f32 v3, v160, v161, v3\n"
    "v_max3_f32 v4, v162, v163, v4\n"
    "v_max3_f32 v3, v164, v165, v3\n"
    "v_mfma_f32_32x32x16_bf16 v[176:191], a[76:79], a[12:15], v[176:191]\n"
    "s_waitcnt lgkmcnt(3)\n"
    "ds_read_b128 a[64:67], v1 offset:24576\n"
    "v_max3_f32 v4, v166, v167, v4\n"
    "v_max3_f32 v3, v168, v169, v3\n"
    "v_max3_f32 v4, v170, v171, v4\n"
    "v_max3_f32 v3, v172, v173, v3\n"
    "v_max3_f32 v4, v174, v175, v4\n"
    "v_mfma_f32_32x32x16_bf16 v[176:191], a[80:83], a[16:19], v[176:191]\n"
    "s_waitcnt lgkmcnt(3)\n"
    "ds_read_b128 a[68:71], v1 offset:25600\n"
    "v_exp_f32 v44, v44\n"
    "v_add_f32 v6, v6, v42\n"
    "v_sub_f32 v46, v46, v5\n"
    "v_mfma_f32_32x32x16_bf16 v[176:191], a[84:87], a[20:23], v[176:191]\n"
    "s_waitcnt vmcnt(16) lgkmcnt(3)\n"
    "ds_read_b128 a[72:75], v1 offset:26624\n"
    "v_accvgpr_read_b32 v15, a120\n"
    "v_exp_f32 v45, v45\n"
    "v_add_f32 v6, v6, v43\n"
    "v_sub_f32 v47, v47, v5\n"
    "v_mfma_f32_32x32x16_bf16 v[176:191], a[88:91], a[24:27], v[176:191]\n"
    "s_waitcnt lgkmcnt(3)\n"
    "ds_read_b128 a[76:79], v1 offset:27648\n"
    "ds_read_b128 a[240:243], v15 offset:0\n"
    "v_exp_f32 v46, v46\n"
    "v_add_f32 v6, v6, v44\n"
    "v_mfma_f32_32x32x16_bf16 v[176:191], a[92:95], a[28:31], v[176:191]\n"
    "s_waitcnt lgkmcnt(4)\n"
    "ds_read_b128 a[80:83], v1 offset:28672\n"
    "ds_read_b128 a[244:247], v15 offset:2048\n"
    "v_exp_f32 v47, v47\n"
    "v_add_f32 v6, v6, v45\n"
    "v_add_f32 v6, v6, v46\n"
    "v_mfma_f32_32x32x16_bf16 v[176:191], a[64:67], a[32:35], v[176:191]\n"
    "s_waitcnt lgkmcnt(5)\n"
    "ds_read_b128 a[84:87], v1 offset:29696\n"
    "v_add_f32 v6, v6, v47\n"
    "v_sub_f32 v48, v48, v5\n"
    "v_sub_f32 v49, v49, v5\n"
    "v_exp_f32 v48, v48\n"
    "v_mfma_f32_32x32x16_bf16 v[176:191], a[68:71], a[36:39], v[176:191]\n"
    "s_waitcnt lgkmcnt(5)\n"
    "ds_read_b128 a[88:91], v1 offset:30720\n"
    "v_sub_f32 v50, v50, v5\n"
    "v_exp_f32 v49, v49\n"
    "v_sub_f32 v51, v51, v5\n"
    "v_mfma_f32_32x32x16_bf16 v[176:191], a[72:75], a[40:43], v[176:191]\n"
    "s_waitcnt lgkmcnt(5)\n"
    "ds_read_b128 a[92:95], v1 offset:31744\n"
    "v_exp_f32 v50, v50\n"
    "v_add_f32 v6, v6, v48\n"
    "v_sub_f32 v52, v52, v5\n"
    "v_mfma_f32_32x32x16_bf16 v[176:191], a[76:79], a[44:47], v[176:191]\n"
    "s_waitcnt vmcnt(8) lgkmcnt(4)\n"
    "s_barrier\n"
    "ds_read_b128 a[64:67], v1 offset:32768\n"
    "buffer_store_dwordx4 a[240:243], v0, s[88:91], s86 offen offset:2048 sc1\n"
    "v_mfma_f32_32x32x16_bf16 v[176:191], a[80:83], a[48:51], v[176:191]\n"
    "s_waitcnt lgkmcnt(3)\n"
    "ds_read_b128 a[68:71], v1 offset:33792\n"
    "buffer_store_dwordx4 a[244:247], v0, s[88:91], s86 offen offset:3072 sc1\n"
    "v_mfma_f32_32x32x16_bf16 v[176:191], a[84:87], a[52:55], v[176:191]\n"
    "s_waitcnt lgkmcnt(3)\n"
    "ds_read_b128 a[72:75], v1 offset:34816\n"
    "v_accvgpr_read_b32 v15, a121\n"
    "v_exp_f32 v51, v51\n"
    "v_add_f32 v6, v6, v49\n"
    "v_sub_f32 v53, v53, v5\n"
    "v_mfma_f32_32x32x16_bf16 v[176:191], a[88:91], a[56:59], v[176:191]\n"
    "s_waitcnt lgkmcnt(3)\n"
    "ds_read_b128 a[76:79], v1 offset:35840\n"
    "ds_read_b128 a[248:251], v15 offset:0\n"
    "v_exp_f32 v52, v52\n"
    "v_add_f32 v6, v6, v50\n"
    "v_sub_f32 v54, v54, v5\n"
    "v_mfma_f32_32x32x16_bf16 v[176:191], a[92:95], a[60:63], v[176:191]\n"
    "s_waitcnt lgkmcnt(4)\n"
    "ds_read_b128 a[80:83], v1 offset:36864\n"
    "ds_read_b128 a[252:255], v15 offset:2048\n"
    "v_exp_f32 v53, v53\n"
    "v_add_f32 v6, v6, v51\n"
    "v_sub_f32 v55, v55, v5\n"
    "v_mfma_f32_32x32x16_bf16 v[192:207], a[64:67], a[0:3], 0\n"
    "s_waitcnt lgkmcnt(5)\n"
    "ds_read_b128 a[84:87], v1 offset:37888\n"
    "v_exp_f32 v54, v54\n"
    "v_add_f32 v6, v6, v52\n"
    "v_exp_f32 v55, v55\n"
    "v_mfma_f32_32x32x16_bf16 v[192:207], a[68:71], a[4:7], v[192:207]\n"
    "s_waitcnt lgkmcnt(5)\n"
    "s_add_u32 m0, s65, 0x18000\n"
    "ds_read_b128 a[88:91], v1 offset:38912\n"
    "v_add_f32 v6, v6, v53\n"
    "v_add_f32 v6, v6, v54\n"
    "v_add_f32 v6, v6, v55\n"
    "v_sub_f32 v56, v56, v5\n"
    "v_mfma_f32_32x32x16_bf16 v[192:207], a[72:75], a[8:11], v[192:207]\n"
    "s_waitcnt lgkmcnt(5)\n"
    "s_add_u32 s64, s67, 0x0\n"
    "ds_read_b128 a[92:95], v1 offset:39936\n"
    "v_max3_f32 v3, v176, v177, v3\n"
    "v_max3_f32 v4, v178, v179, v4\n"
    "v_max3_f32 v3, v180, v181, v3\n"
    "v_max3_f32 v4, v182, v183, v4\n"
    "v_mfma_f32_32x32x16_bf16 v[192:207], a[76:79], a[12:15], v[192:207]\n"
    "s_waitcnt lgkmcnt(4)\n"
    "ds_read_b128 a[64:67], v1 offset:40960\n"
    "buffer_load_dwordx4 v0, s[48:51], s64 offen offset:0 lds\n"
    "buffer_store_dwordx4 a[248:251], v0, s[88:91], s87 offen offset:2048 sc1\n"
    "v_mfma_f32_32x32x16_bf16 v[192:207], a[80:83], a[16:19], v[192:207]\n"
    "s_waitcnt lgkmcnt(3)\n"
    "ds_read_b128 a[68:71], v1 offset:41984\n"
    "buffer_store_dwordx4 a[252:255], v0, s[88:91], s87 offen offset:3072 sc1\n"
    "v_mfma_f32_32x32x16_bf16 v[192:207], a[84:87], a[20:23], v[192:207]\n"
    "s_waitcnt lgkmcnt(3)\n"
    "ds_read_b128 a[72:75], v1 offset:43008\n"
    "buffer_load_dwordx4 v0, s[48:51], s64 offen offset:1024 lds\n"
    "v_max3_f32 v3, v184, v185, v3\n"
    "v_max3_f32 v4, v186, v187, v4\n"
    "v_max3_f32 v3, v188, v189, v3\n"
    "v_mfma_f32_32x32x16_bf16 v[192:207], a[88:91], a[24:27], v[192:207]\n"
    "s_waitcnt lgkmcnt(3)\n"
    "ds_read_b128 a[76:79], v1 offset:44032\n"
    "v_max3_f32 v4, v190, v191, v4\n"
    "v_sub_f32 v57, v57, v5\n"
    "v_exp_f32 v56, v56\n"
    "v_sub_f32 v58, v58, v5\n"
    "v_mfma_f32_32x32x16_bf16 v[192:207], a[92:95], a[28:31], v[192:207]\n"
    "s_waitcnt lgkmcnt(3)\n"
    "ds_read_b128 a[80:83], v1 offset:45056\n"
    "buffer_load_dwordx4 v0, s[48:51], s64 offen offset:2048 lds\n"
    "v_exp_f32 v57, v57\n"
    "v_sub_f32 v59, v59, v5\n"
    "v_mfma_f32_32x32x16_bf16 v[192:207], a[64:67], a[32:35], v[192:207]\n"
    "s_waitcnt lgkmcnt(3)\n"
    "ds_read_b128 a[84:87], v1 offset:46080\n"
    "v_exp_f32 v58, v58\n"
    "v_add_f32 v6, v6, v56\n"
    "v_sub_f32 v60, v60, v5\n"
    "v_mfma_f32_32x32x16_bf16 v[192:207], a[68:71], a[36:39], v[192:207]\n"
    "s_waitcnt lgkmcnt(3)\n"
    "ds_read_b128 a[88:91], v1 offset:47104\n"
    "buffer_load_dwordx4 v0, s[48:51], s64 offen offset:3072 lds\n"
    "v_exp_f32 v59, v59\n"
    "v_add_f32 v6, v6, v57\n"
    "v_mfma_f32_32x32x16_bf16 v[192:207], a[72:75], a[40:43], v[192:207]\n"
    "s_waitcnt lgkmcnt(3)\n"
    "s_add_u32 m0, s65, 0x1c000\n"
    "ds_read_b128 a[92:95], v1 offset:48128\n"
    "v_sub_f32 v61, v61, v5\n"
    "v_exp_f32 v60, v60\n"
    "v_add_f32 v6, v6, v58\n"
    "v_mfma_f32_32x32x16_bf16 v[192:207], a[76:79], a[44:47], v[192:207]\n"
    "s_waitcnt lgkmcnt(3)\n"
    "s_add_u32 s64, s67, 0x4000\n"
    "ds_read_b128 a[64:67], v1 offset:49152\n"
    "v_sub_f32 v62, v62, v5\n"
    "v_exp_f32 v61, v61\n"
    "v_add_f32 v6, v6, v59\n"
    "v_mfma_f32_32x32x16_bf16 v[192:207], a[80:83], a[48:51], v[192:207]\n"
    "s_waitcnt lgkmcnt(3)\n"
    "ds_read_b128 a[68:71], v1 offset:50176\n"
    "buffer_load_dwordx4 v0, s[48:51], s64 offen offset:0 lds\n"
    "v_sub_f32 v63, v63, v5\n"
    "v_exp_f32 v62, v62\n"
    "v_mfma_f32_32x32x16_bf16 v[192:207], a[84:87], a[52:55], v[192:207]\n"
    "s_waitcnt lgkmcnt(3)\n"
    "ds_read_b128 a[72:75], v1 offset:51200\n"
    "v_add_f32 v6, v6, v60\n"
    "v_exp_f32 v63, v63\n"
    "v_add_f32 v6, v6, v61\n"
    "v_add_f32 v6, v6, v62\n"
    "v_mfma_f32_32x32x16_bf16 v[192:207], a[88:91], a[56:59], v[192:207]\n"
    "s_waitcnt lgkmcnt(3)\n"
    "ds_read_b128 a[76:79], v1 offset:52224\n"
    "buffer_load_dwordx4 v0, s[48:51], s64 offen offset:1024 lds\n"
    "v_add_f32 v6, v6, v63\n"
    "v_sub_f32 v64, v64, v5\n"
    "v_sub_f32 v65, v65, v5\n"
    "v_mfma_f32_32x32x16_bf16 v[192:207], a[92:95], a[60:63], v[192:207]\n"
    "s_waitcnt lgkmcnt(3)\n"
    "ds_read_b128 a[80:83], v1 offset:53248\n"
    "v_exp_f32 v64, v64\n"
    "v_sub_f32 v66, v66, v5\n"
    "v_exp_f32 v65, v65\n"
    "v_mfma_f32_32x32x16_bf16 v[240:255], a[64:67], a[0:3], 0\n"
    "s_waitcnt lgkmcnt(3)\n"
    "ds_read_b128 a[84:87], v1 offset:54272\n"
    "buffer_load_dwordx4 v0, s[48:51], s64 offen offset:2048 lds\n"
    "v_sub_f32 v67, v67, v5\n"
    "v_exp_f32 v66, v66\n"
    "v_mfma_f32_32x32x16_bf16 v[240:255], a[68:71], a[4:7], v[240:255]\n"
    "s_waitcnt lgkmcnt(3)\n"
    "ds_read_b128 a[88:91], v1 offset:55296\n"
    "v_add_f32 v6, v6, v64\n"
    "v_sub_f32 v68, v68, v5\n"
    "v_exp_f32 v67, v67\n"
    "v_add_f32 v6, v6, v65\n"
    "v_mfma_f32_32x32x16_bf16 v[240:255], a[72:75], a[8:11], v[240:255]\n"
    "s_waitcnt lgkmcnt(3)\n"
    "ds_read_b128 a[92:95], v1 offset:56320\n"
    "buffer_load_dwordx4 v0, s[48:51], s64 offen offset:3072 lds\n"
    "v_max3_f32 v3, v192, v193, v3\n"
    "v_max3_f32 v4, v194, v195, v4\n"
    "v_max3_f32 v3, v196, v197, v3\n"
    "v_mfma_f32_32x32x16_bf16 v[240:255], a[76:79], a[12:15], v[240:255]\n"
    "s_waitcnt lgkmcnt(3)\n"
    "ds_read_b128 a[64:67], v1 offset:57344\n"
    "v_max3_f32 v4, v198, v199, v4\n"
    "v_max3_f32 v3, v200, v201, v3\n"
    "v_max3_f32 v4, v202, v203, v4\n"
    "v_max3_f32 v3, v204, v205, v3\n"
    "v_max3_f32 v4, v206, v207, v4\n"
    "v_mfma_f32_32x32x16_bf16 v[240:255], a[80:83], a[16:19], v[240:255]\n"
    "s_waitcnt lgkmcnt(3)\n"
    "ds_read_b128 a[68:71], v1 offset:58368\n"
    "v_sub_f32 v69, v69, v5\n"
    "v_exp_f32 v68, v68\n"
    "v_add_f32 v6, v6, v66\n"
    "v_sub_f32 v70, v70, v5\n"
    "v_mfma_f32_32x32x16_bf16 v[240:255], a[84:87], a[20:23], v[240:255]\n"
    "s_waitcnt lgkmcnt(3)\n"
    "ds_read_b128 a[72:75], v1 offset:59392\n"
    "v_exp_f32 v69, v69\n"
    "v_add_f32 v6, v6, v67\n"
    "v_sub_f32 v71, v71, v5\n"
    "v_mfma_f32_32x32x16_bf16 v[240:255], a[88:91], a[24:27], v[240:255]\n"
    "s_waitcnt lgkmcnt(3)\n"
    "ds_read_b128 a[76:79], v1 offset:60416\n"
    "v_exp_f32 v70, v70\n"
    "v_add_f32 v6, v6, v68\n"
    "v_exp_f32 v71, v71\n"
    "v_mfma_f32_32x32x16_bf16 v[240:255], a[92:95], a[28:31], v[240:255]\n"
    "s_waitcnt lgkmcnt(3)\n"
    "ds_read_b128 a[80:83], v1 offset:61440\n"
    "v_add_f32 v6, v6, v69\n"
    "v_add_f32 v6, v6, v70\n"
    "v_add_f32 v6, v6, v71\n"
    "v_sub_f32 v72, v72, v5\n"
    "v_sub_f32 v73, v73, v5\n"
    "v_mfma_f32_32x32x16_bf16 v[240:255], a[64:67], a[32:35], v[240:255]\n"
    "s_waitcnt lgkmcnt(3)\n"
    "ds_read_b128 a[84:87], v1 offset:62464\n"
    "v_exp_f32 v72, v72\n"
    "v_sub_f32 v74, v74, v5\n"
    "v_exp_f32 v73, v73\n"
    "v_mfma_f32_32x32x16_bf16 v[240:255], a[68:71], a[36:39], v[240:255]\n"
    "s_waitcnt lgkmcnt(3)\n"
    "ds_read_b128 a[88:91], v1 offset:63488\n"
    "v_sub_f32 v75, v75, v5\n"
    "v_exp_f32 v74, v74\n"
    "v_add_f32 v6, v6, v72\n"
    "v_sub_f32 v76, v76, v5\n"
    "v_mfma_f32_32x32x16_bf16 v[240:255], a[72:75], a[40:43], v[240:255]\n"
    "s_waitcnt lgkmcnt(3)\n"
    "ds_read_b128 a[92:95], v1 offset:64512\n"
    "v_exp_f32 v75, v75\n"
    "v_add_f32 v6, v6, v73\n"
    "v_sub_f32 v77, v77, v5\n"
    "v_mfma_f32_32x32x16_bf16 v[240:255], a[76:79], a[44:47], v[240:255]\n"
    "s_waitcnt vmcnt(12) lgkmcnt(3)\n"
    "s_barrier\n"
    "ds_read_b128 a[64:67], v2 offset:0\n"
    "v_exp_f32 v76, v76\n"
    "v_add_f32 v6, v6, v74\n"
    "v_sub_f32 v78, v78, v5\n"
    "v_mfma_f32_32x32x16_bf16 v[240:255], a[80:83], a[48:51], v[240:255]\n"
    "s_waitcnt lgkmcnt(3)\n"
    "ds_read_b128 a[68:71], v2 offset:1024\n"
    "v_exp_f32 v77, v77\n"
    "v_add_f32 v6, v6, v75\n"
    "v_sub_f32 v79, v79, v5\n"
    "v_mfma_f32_32x32x16_bf16 v[240:255], a[84:87], a[52:55], v[240:255]\n"
    "s_waitcnt lgkmcnt(3)\n"
    "ds_read_b128 a[72:75], v2 offset:2048\n"
    "v_exp_f32 v78, v78\n"
    "v_add_f32 v6, v6, v76\n"
    "v_exp_f32 v79, v79\n"
    "v_mfma_f32_32x32x16_bf16 v[240:255], a[88:91], a[56:59], v[240:255]\n"
    "s_waitcnt lgkmcnt(3)\n"
    "ds_read_b128 a[76:79], v2 offset:3072\n"
    "v_add_f32 v6, v6, v77\n"
    "v_add_f32 v6, v6, v78\n"
    "v_add_f32 v6, v6, v79\n"
    "v_sub_f32 v80, v80, v5\n"
    "v_sub_f32 v81, v81, v5\n"
    "v_mfma_f32_32x32x16_bf16 v[240:255], a[92:95], a[60:63], v[240:255]\n"
    "s_waitcnt lgkmcnt(3)\n"
    "ds_read_b128 a[80:83], v2 offset:4096\n"
    "v_exp_f32 v80, v80\n"
    "v_sub_f32 v82, v82, v5\n"
    "v_exp_f32 v81, v81\n"
    "v_mfma_f32_32x32x16_bf16 v[208:223], a[64:67], a[0:3], 0\n"
    "s_waitcnt lgkmcnt(3)\n"
    "ds_read_b128 a[84:87], v2 offset:5120\n"
    "v_sub_f32 v83, v83, v5\n"
    "v_exp_f32 v82, v82\n"
    "v_add_f32 v6, v6, v80\n"
    "v_sub_f32 v84, v84, v5\n"
    "v_mfma_f32_32x32x16_bf16 v[208:223], a[68:71], a[4:7], v[208:223]\n"
    "s_waitcnt lgkmcnt(3)\n"
    "s_add_u32 m0, s65, 0x0\n"
    "ds_read_b128 a[88:91], v2 offset:6144\n"
    "v_exp_f32 v83, v83\n"
    "v_add_f32 v6, v6, v81\n"
    "v_sub_f32 v85, v85, v5\n"
    "v_mfma_f32_32x32x16_bf16 v[208:223], a[72:75], a[8:11], v[208:223]\n"
    "s_waitcnt lgkmcnt(3)\n"
    "s_add_u32 s64, s67, 0x8000\n"
    "ds_read_b128 a[92:95], v2 offset:7168\n"
    "v_max3_f32 v3, v240, v241, v3\n"
    "v_max3_f32 v4, v242, v243, v4\n"
    "v_max3_f32 v3, v244, v245, v3\n"
    "v_max3_f32 v4, v246, v247, v4\n"
    "v_mfma_f32_32x32x16_bf16 v[208:223], a[76:79], a[12:15], v[208:223]\n"
    "s_waitcnt lgkmcnt(3)\n"
    "ds_read_b128 a[64:67], v2 offset:8192\n"
    "buffer_load_dwordx4 v0, s[48:51], s64 offen offset:0 lds\n"
    "v_max3_f32 v3, v248, v249, v3\n"
    "v_max3_f32 v4, v250, v251, v4\n"
    "v_max3_f32 v3, v252, v253, v3\n"
    "v_mfma_f32_32x32x16_bf16 v[208:223], a[80:83], a[16:19], v[208:223]\n"
    "s_waitcnt lgkmcnt(3)\n"
    "ds_read_b128 a[68:71], v2 offset:9216\n"
    "v_max3_f32 v4, v254, v255, v4\n"
    "v_exp_f32 v84, v84\n"
    "v_add_f32 v6, v6, v82\n"
    "v_sub_f32 v86, v86, v5\n"
    "v_mfma_f32_32x32x16_bf16 v[208:223], a[84:87], a[20:23], v[208:223]\n"
    "s_waitcnt lgkmcnt(3)\n"
    "ds_read_b128 a[72:75], v2 offset:10240\n"
    "buffer_load_dwordx4 v0, s[48:51], s64 offen offset:1024 lds\n"
    "v_exp_f32 v85, v85\n"
    "v_add_f32 v6, v6, v83\n"
    "v_mfma_f32_32x32x16_bf16 v[208:223], a[88:91], a[24:27], v[208:223]\n"
    "s_waitcnt lgkmcnt(3)\n"
    "ds_read_b128 a[76:79], v2 offset:11264\n"
    "v_sub_f32 v87, v87, v5\n"
    "v_exp_f32 v86, v86\n"
    "v_add_f32 v6, v6, v84\n"
    "v_mfma_f32_32x32x16_bf16 v[208:223], a[92:95], a[28:31], v[208:223]\n"
    "s_waitcnt lgkmcnt(3)\n"
    "ds_read_b128 a[80:83], v2 offset:12288\n"
    "buffer_load_dwordx4 v0, s[48:51], s64 offen offset:2048 lds\n"
    "v_exp_f32 v87, v87\n"
    "v_add_f32 v6, v6, v85\n"
    "v_mfma_f32_32x32x16_bf16 v[208:223], a[64:67], a[32:35], v[208:223]\n"
    "s_waitcnt lgkmcnt(3)\n"
    "ds_read_b128 a[84:87], v2 offset:13312\n"
    "v_add_f32 v6, v6, v86\n"
    "v_add_f32 v6, v6, v87\n"
    "v_sub_f32 v88, v88, v5\n"
    "v_sub_f32 v89, v89, v5\n"
    "v_mfma_f32_32x32x16_bf16 v[208:223], a[68:71], a[36:39], v[208:223]\n"
    "s_waitcnt lgkmcnt(3)\n"
    "ds_read_b128 a[88:91], v2 offset:14336\n"
    "buffer_load_dwordx4 v0, s[48:51], s64 offen offset:3072 lds\n"
    "v_exp_f32 v88, v88\n"
    "v_sub_f32 v90, v90, v5\n"
    "v_mfma_f32_32x32x16_bf16 v[208:223], a[72:75], a[40:43], v[208:223]\n"
    "s_waitcnt lgkmcnt(3)\n"
    "s_add_u32 m0, s65, 0x4000\n"
    "ds_read_b128 a[92:95], v2 offset:15360\n"
    "v_exp_f32 v89, v89\n"
    "v_sub_f32 v91, v91, v5\n"
    "v_mfma_f32_32x32x16_bf16 v[208:223], a[76:79], a[44:47], v[208:223]\n"
    "s_waitcnt lgkmcnt(3)\n"
    "s_add_u32 s64, s67, 0xc000\n"
    "ds_read_b128 a[64:67], v2 offset:16384\n"
    "v_exp_f32 v90, v90\n"
    "v_add_f32 v6, v6, v88\n"
    "v_sub_f32 v92, v92, v5\n"
    "v_mfma_f32_32x32x16_bf16 v[208:223], a[80:83], a[48:51], v[208:223]\n"
    "s_waitcnt lgkmcnt(3)\n"
    "ds_read_b128 a[68:71], v2 offset:17408\n"
    "buffer_load_dwordx4 v0, s[48:51], s64 offen offset:0 lds\n"
    "v_exp_f32 v91, v91\n"
    "v_add_f32 v6, v6, v89\n"
    "v_mfma_f32_32x32x16_bf16 v[208:223], a[84:87], a[52:55], v[208:223]\n"
    "s_waitcnt lgkmcnt(3)\n"
    "ds_read_b128 a[72:75], v2 offset:18432\n"
    "v_sub_f32 v93, v93, v5\n"
    "v_exp_f32 v92, v92\n"
    "v_add_f32 v6, v6, v90\n"
    "v_sub_f32 v94, v94, v5\n"
    "v_mfma_f32_32x32x16_bf16 v[208:223], a[88:91], a[56:59], v[208:223]\n"
    "s_waitcnt lgkmcnt(3)\n"
    "ds_read_b128 a[76:79], v2 offset:19456\n"
    "buffer_load_dwordx4 v0, s[48:51], s64 offen offset:1024 lds\n"
    "v_exp_f32 v93, v93\n"
    "v_add_f32 v6, v6, v91\n"
    "v_mfma_f32_32x32x16_bf16 v[208:223], a[92:95], a[60:63], v[208:223]\n"
    "s_waitcnt lgkmcnt(3)\n"
    "ds_read_b128 a[80:83], v2 offset:20480\n"
    "v_sub_f32 v95, v95, v5\n"
    "v_exp_f32 v94, v94\n"
    "v_add_f32 v6, v6, v92\n"
    "v_mfma_f32_32x32x16_bf16 v[224:239], a[64:67], a[0:3], 0\n"
    "s_waitcnt lgkmcnt(3)\n"
    "ds_read_b128 a[84:87], v2 offset:21504\n"
    "buffer_load_dwordx4 v0, s[48:51], s64 offen offset:2048 lds\n"
    "v_exp_f32 v95, v95\n"
    "v_add_f32 v6, v6, v93\n"
    "v_mfma_f32_32x32x16_bf16 v[224:239], a[68:71], a[4:7], v[224:239]\n"
    "s_waitcnt lgkmcnt(3)\n"
    "ds_read_b128 a[88:91], v2 offset:22528\n"
    "v_add_f32 v6, v6, v94\n"
    "v_add_f32 v6, v6, v95\n"
    "v_sub_f32 v96, v96, v5\n"
    "v_sub_f32 v97, v97, v5\n"
    "v_mfma_f32_32x32x16_bf16 v[224:239], a[72:75], a[8:11], v[224:239]\n"
    "s_waitcnt lgkmcnt(3)\n"
    "ds_read_b128 a[92:95], v2 offset:23552\n"
    "buffer_load_dwordx4 v0, s[48:51], s64 offen offset:3072 lds\n"
    "v_max3_f32 v3, v208, v209, v3\n"
    "v_max3_f32 v4, v210, v211, v4\n"
    "v_max3_f32 v3, v212, v213, v3\n"
    "v_mfma_f32_32x32x16_bf16 v[224:239], a[76:79], a[12:15], v[224:239]\n"
    "s_waitcnt lgkmcnt(3)\n"
    "ds_read_b128 a[64:67], v2 offset:24576\n"
    "v_max3_f32 v4, v214, v215, v4\n"
    "v_max3_f32 v3, v216, v217, v3\n"
    "v_max3_f32 v4, v218, v219, v4\n"
    "v_max3_f32 v3, v220, v221, v3\n"
    "v_max3_f32 v4, v222, v223, v4\n"
    "v_mfma_f32_32x32x16_bf16 v[224:239], a[80:83], a[16:19], v[224:239]\n"
    "s_waitcnt lgkmcnt(3)\n"
    "ds_read_b128 a[68:71], v2 offset:25600\n"
    "v_exp_f32 v96, v96\n"
    "v_sub_f32 v98, v98, v5\n"
    "v_exp_f32 v97, v97\n"
    "v_mfma_f32_32x32x16_bf16 v[224:239], a[84:87], a[20:23], v[224:239]\n"
    "s_waitcnt lgkmcnt(3)\n"
    "ds_read_b128 a[72:75], v2 offset:26624\n"
    "v_sub_f32 v99, v99, v5\n"
    "v_exp_f32 v98, v98\n"
    "v_add_f32 v6, v6, v96\n"
    "v_sub_f32 v100, v100, v5\n"
    "v_mfma_f32_32x32x16_bf16 v[224:239], a[88:91], a[24:27], v[224:239]\n"
    "s_waitcnt lgkmcnt(3)\n"
    "ds_read_b128 a[76:79], v2 offset:27648\n"
    "v_exp_f32 v99, v99\n"
    "v_add_f32 v6, v6, v97\n"
    "v_sub_f32 v101, v101, v5\n"
    "v_mfma_f32_32x32x16_bf16 v[224:239], a[92:95], a[28:31], v[224:239]\n"
    "s_waitcnt lgkmcnt(3)\n"
    "ds_read_b128 a[80:83], v2 offset:28672\n"
    "v_exp_f32 v100, v100\n"
    "v_add_f32 v6, v6, v98\n"
    "v_sub_f32 v102, v102, v5\n"
    "v_mfma_f32_32x32x16_bf16 v[224:239], a[64:67], a[32:35], v[224:239]\n"
    "s_waitcnt lgkmcnt(3)\n"
    "ds_read_b128 a[84:87], v2 offset:29696\n"
    "v_exp_f32 v101, v101\n"
    "v_add_f32 v6, v6, v99\n"
    "v_sub_f32 v103, v103, v5\n"
    "v_mfma_f32_32x32x16_bf16 v[224:239], a[68:71], a[36:39], v[224:239]\n"
    "s_waitcnt lgkmcnt(3)\n"
    "ds_read_b128 a[88:91], v2 offset:30720\n"
    "v_exp_f32 v102, v102\n"
    "v_add_f32 v6, v6, v100\n"
    "v_exp_f32 v103, v103\n"
    "v_mfma_f32_32x32x16_bf16 v[224:239], a[72:75], a[40:43], v[224:239]\n"
    "s_waitcnt lgkmcnt(3)\n"
    "ds_read_b128 a[92:95], v2 offset:31744\n"
    "v_add_f32 v6, v6, v101\n"
    "v_add_f32 v6, v6, v102\n"
    "v_add_f32 v6, v6, v103\n"
    "v_sub_f32 v104, v104, v5\n"
    "v_sub_f32 v105, v105, v5\n"
    "v_mfma_f32_32x32x16_bf16 v[224:239], a[76:79], a[44:47], v[224:239]\n"
    "s_waitcnt vmcnt(8) lgkmcnt(3)\n"
    "s_barrier\n"
    "ds_read_b128 a[64:67], v2 offset:32768\n"
    "v_exp_f32 v104, v104\n"
    "v_sub_f32 v106, v106, v5\n"
    "v_mfma_f32_32x32x16_bf16 v[224:239], a[80:83], a[48:51], v[224:239]\n"
    "s_waitcnt lgkmcnt(3)\n"
    "ds_read_b128 a[68:71], v2 offset:33792\n"
    "v_exp_f32 v105, v105\n"
    "v_sub_f32 v107, v107, v5\n"
    "v_exp_f32 v106, v106\n"
    "v_mfma_f32_32x32x16_bf16 v[224:239], a[84:87], a[52:55], v[224:239]\n"
    "s_waitcnt lgkmcnt(3)\n"
    "ds_read_b128 a[72:75], v2 offset:34816\n"
    "v_add_f32 v6, v6, v104\n"
    "v_sub_f32 v108, v108, v5\n"
    "v_exp_f32 v107, v107\n"
    "v_add_f32 v6, v6, v105\n"
    "v_mfma_f32_32x32x16_bf16 v[224:239], a[88:91], a[56:59], v[224:239]\n"
    "s_waitcnt lgkmcnt(3)\n"
    "ds_read_b128 a[76:79], v2 offset:35840\n"
    "v_sub_f32 v109, v109, v5\n"
    "v_exp_f32 v108, v108\n"
    "v_add_f32 v6, v6, v106\n"
    "v_sub_f32 v110, v110, v5\n"
    "v_mfma_f32_32x32x16_bf16 v[224:239], a[92:95], a[60:63], v[224:239]\n"
    "s_waitcnt lgkmcnt(3)\n"
    "ds_read_b128 a[80:83], v2 offset:36864\n"
    "s_nop 9\n"
    "v_max3_f32 v3, v224, v225, v3\n"
    "v_max3_f32 v4, v226, v227, v4\n"
    "v_max3_f32 v3, v228, v229, v3\n"
    "v_max3_f32 v4, v230, v231, v4\n"
    "v_max3_f32 v3, v232, v233, v3\n"
    "v_max3_f32 v4, v234, v235, v4\n"
    "v_max3_f32 v3, v236, v237, v3\n"
    "v_max3_f32 v4, v238, v239, v4\n"
    "v_max_f32 v3, v3, v4\n"
    "v_mov_b32 v4, v3\n"
    "s_nop 1\n"
    "v_permlane32_swap_b32 v3, v4\n"
    "v_max_f32 v3, v3, v4\n"
    "v_add_f32 v3, 0x42a00000, v3\n"
    "v_max_f32 v8, v3, v5\n"
    "v_sub_f32 v4, v5, v8\n"
    "v_exp_f32 v4, v4\n"
    "s_nop 0\n"
    "v_mul_f32 v16, v16, v4\n"
    "v_mul_f32 v17, v17, v4\n"
    "v_mul_f32 v18, v18, v4\n"
    "v_mul_f32 v19, v19, v4\n"
    "v_mul_f32 v20, v20, v4\n"
    "v_mul_f32 v21, v21, v4\n"
    "v_mul_f32 v22, v22, v4\n"
    "v_mul_f32 v23, v23, v4\n"
    "v_cvt_pk_bf16_f32 v16, v16, v17\n"
    "v_cvt_pk_bf16_f32 v17, v18, v19\n"
    "v_cvt_pk_bf16_f32 v18, v20, v21\n"
    "v_cvt_pk_bf16_f32 v19, v22, v23\n"
    "s_nop 1\n"
    "v_mfma_f32_32x32x16_bf16 a[128:143], a[64:67], v[16:19], 0\n"
    "s_waitcnt lgkmcnt(3)\n"
    "ds_read_b128 a[84:87], v2 offset:37888\n"
    "v_mul_f32 v24, v24, v4\n"
    "v_mul_f32 v25, v25, v4\n"
    "v_mul_f32 v26, v26, v4\n"
    "v_mul_f32 v27, v27, v4\n"
    "v_mul_f32 v28, v28, v4\n"
    "v_mfma_f32_32x32x16_bf16 a[144:159], a[68:71], v[16:19], 0\n"
    "s_waitcnt lgkmcnt(3)\n"
    "s_add_u32 m0, s65, 0x8000\n"
    "ds_read_b128 a[88:91], v2 offset:38912\n"
    "v_mul_f32 v29, v29, v4\n"
    "v_mul_f32 v30, v30, v4\n"
    "v_mul_f32 v31, v31, v4\n"
    "v_cvt_pk_bf16_f32 v24, v24, v25\n"
    "v_mfma_f32_32x32x16_bf16 a[160:175], a[72:75], v[16:19], 0\n"
    "s_waitcnt lgkmcnt(3)\n"
    "s_add_u32 s64, s67, 0x10000\n"
    "ds_read_b128 a[92:95], v2 offset:39936\n"
    "v_cvt_pk_bf16_f32 v25, v26, v27\n"
    "v_cvt_pk_bf16_f32 v26, v28, v29\n"
    "v_cvt_pk_bf16_f32 v27, v30, v31\n"
    "v_mul_f32 v32, v32, v4\n"
    "v_mfma_f32_32x32x16_bf16 a[176:191], a[76:79], v[16:19], 0\n"
    "s_waitcnt lgkmcnt(3)\n"
    "ds_read_b128 a[64:67], v2 offset:40960\n"
    "buffer_load_dwordx4 v0, s[48:51], s64 offen offset:0 lds\n"
    "v_mul_f32 v33, v33, v4\n"
    "v_mul_f32 v34, v34, v4\n"
    "v_mul_f32 v35, v35, v4\n"
    "v_mfma_f32_32x32x16_bf16 a[192:207], a[80:83], v[16:19], 0\n"
    "s_waitcnt lgkmcnt(3)\n"
    "ds_read_b128 a[68:71], v2 offset:41984\n"
    "v_mul_f32 v36, v36, v4\n"
    "v_mul_f32 v37, v37, v4\n"
    "v_mul_f32 v38, v38, v4\n"
    "v_mul_f32 v39, v39, v4\n"
    "v_cvt_pk_bf16_f32 v32, v32, v33\n"
    "v_mfma_f32_32x32x16_bf16 a[208:223], a[84:87], v[16:19], 0\n"
    "s_waitcnt lgkmcnt(3)\n"
    "ds_read_b128 a[72:75], v2 offset:43008\n"
    "buffer_load_dwordx4 v0, s[48:51], s64 offen offset:1024 lds\n"
    "v_cvt_pk_bf16_f32 v33, v34, v35\n"
    "v_cvt_pk_bf16_f32 v34, v36, v37\n"
    "v_cvt_pk_bf16_f32 v35, v38, v39\n"
    "v_mfma_f32_32x32x16_bf16 a[224:239], a[88:91], v[16:19], 0\n"
    "s_waitcnt lgkmcnt(3)\n"
    "ds_read_b128 a[76:79], v2 offset:44032\n"
    "v_mul_f32 v40, v40, v4\n"
    "v_mul_f32 v41, v41, v4\n"
    "v_mul_f32 v42, v42, v4\n"
    "v_mul_f32 v43, v43, v4\n"
    "v_mul_f32 v44, v44, v4\n"
    "v_mfma_f32_32x32x16_bf16 a[240:255], a[92:95], v[16:19], 0\n"
    "s_waitcnt lgkmcnt(3)\n"
    "ds_read_b128 a[80:83], v2 offset:45056\n"
    "buffer_load_dwordx4 v0, s[48:51], s64 offen offset:2048 lds\n"
    "v_mul_f32 v45, v45, v4\n"
    "v_mul_f32 v46, v46, v4\n"
    "v_mul_f32 v47, v47, v4\n"
    "v_mfma_f32_32x32x16_bf16 a[128:143], a[64:67], v[24:27], a[128:143]\n"
    "s_waitcnt lgkmcnt(3)\n"
    "ds_read_b128 a[84:87], v2 offset:46080\n"
    "v_cvt_pk_bf16_f32 v40, v40, v41\n"
    "v_cvt_pk_bf16_f32 v41, v42, v43\n"
    "v_cvt_pk_bf16_f32 v42, v44, v45\n"
    "v_cvt_pk_bf16_f32 v43, v46, v47\n"
    "v_mul_f32 v48, v48, v4\n"
    "v_mfma_f32_32x32x16_bf16 a[144:159], a[68:71], v[24:27], a[144:159]\n"
    "s_waitcnt lgkmcnt(3)\n"
    "ds_read_b128 a[88:91], v2 offset:47104\n"
    "buffer_load_dwordx4 v0, s[48:51], s64 offen offset:3072 lds\n"
    "v_mul_f32 v49, v49, v4\n"
    "v_mul_f32 v50, v50, v4\n"
    "v_mul_f32 v51, v51, v4\n"
    "v_mfma_f32_32x32x16_bf16 a[160:175], a[72:75], v[24:27], a[160:175]\n"
    "s_waitcnt lgkmcnt(3)\n"
    "s_add_u32 m0, s65, 0xc000\n"
    "ds_read_b128 a[92:95], v2 offset:48128\n"
    "v_mul_f32 v52, v52, v4\n"
    "v_mul_f32 v53, v53, v4\n"
    "v_mul_f32 v54, v54, v4\n"
    "v_mul_f32 v55, v55, v4\n"
    "v_mfma_f32_32x32x16_bf16 a[176:191], a[76:79], v[24:27], a[176:191]\n"
    "s_waitcnt lgkmcnt(3)\n"
    "s_add_u32 s64, s67, 0x14000\n"
    "ds_read_b128 a[64:67], v2 offset:49152\n"
    "v_cvt_pk_bf16_f32 v48, v48, v49\n"
    "v_cvt_pk_bf16_f32 v49, v50, v51\n"
    "v_cvt_pk_bf16_f32 v50, v52, v53\n"
    "v_cvt_pk_bf16_f32 v51, v54, v55\n"
    "v_mfma_f32_32x32x16_bf16 a[192:207], a[80:83], v[24:27], a[192:207]\n"
    "s_waitcnt lgkmcnt(3)\n"
    "ds_read_b128 a[68:71], v2 offset:50176\n"
    "buffer_load_dwordx4 v0, s[48:51], s64 offen offset:0 lds\n"
    "v_mul_f32 v56, v56, v4\n"
    "v_mul_f32 v57, v57, v4\n"
    "v_mul_f32 v58, v58, v4\n"
    "v_mfma_f32_32x32x16_bf16 a[208:223], a[84:87], v[24:27], a[208:223]\n"
    "s_waitcnt lgkmcnt(3)\n"
    "ds_read_b128 a[72:75], v2 offset:51200\n"
    "v_mul_f32 v59, v59, v4\n"
    "v_mul_f32 v60, v60, v4\n"
    "v_mul_f32 v61, v61, v4\n"
    "v_mul_f32 v62, v62, v4\n"
    "v_mul_f32 v63, v63, v4\n"
    "v_mfma_f32_32x32x16_bf16 a[224:239], a[88:91], v[24:27], a[224:239]\n"
    "s_waitcnt lgkmcnt(3)\n"
    "ds_read_b128 a[76:79], v2 offset:52224\n"
    "buffer_load_dwordx4 v0, s[48:51], s64 offen offset:1024 lds\n"
    "v_cvt_pk_bf16_f32 v56, v56, v57\n"
    "v_cvt_pk_bf16_f32 v57, v58, v59\n"
    "v_cvt_pk_bf16_f32 v58, v60, v61\n"
    "v_mfma_f32_32x32x16_bf16 a[240:255], a[92:95], v[24:27], a[240:255]\n"
    "s_waitcnt lgkmcnt(3)\n"
    "ds_read_b128 a[80:83], v2 offset:53248\n"
    "v_cvt_pk_bf16_f32 v59, v62, v63\n"
    "v_mul_f32 v64, v64, v4\n"
    "v_mul_f32 v65, v65, v4\n"
    "v_mul_f32 v66, v66, v4\n"
    "v_mul_f32 v67, v67, v4\n"
    "v_mfma_f32_32x32x16_bf16 a[128:143], a[64:67], v[32:35], a[128:143]\n"
    "s_waitcnt lgkmcnt(3)\n"
    "ds_read_b128 a[84:87], v2 offset:54272\n"
    "buffer_load_dwordx4 v0, s[48:51], s64 offen offset:2048 lds\n"
    "v_mul_f32 v68, v68, v4\n"
    "v_mul_f32 v69, v69, v4\n"
    "v_mul_f32 v70, v70, v4\n"
    "v_mfma_f32_32x32x16_bf16 a[144:159], a[68:71], v[32:35], a[144:159]\n"
    "s_waitcnt lgkmcnt(3)\n"
    "ds_read_b128 a[88:91], v2 offset:55296\n"
    "v_mul_f32 v71, v71, v4\n"
    "v_cvt_pk_bf16_f32 v64, v64, v65\n"
    "v_cvt_pk_bf16_f32 v65, v66, v67\n"
    "v_cvt_pk_bf16_f32 v66, v68, v69\n"
    "v_cvt_pk_bf16_f32 v67, v70, v71\n"
    "v_mfma_f32_32x32x16_bf16 a[160:175], a[72:75], v[32:35], a[160:175]\n"
    "s_waitcnt lgkmcnt(3)\n"
    "ds_read_b128 a[92:95], v2 offset:56320\n"
    "buffer_load_dwordx4 v0, s[48:51], s64 offen offset:3072 lds\n"
    "v_mul_f32 v72, v72, v4\n"
    "v_mul_f32 v73, v73, v4\n"
    "v_mul_f32 v74, v74, v4\n"
    "v_mfma_f32_32x32x16_bf16 a[176:191], a[76:79], v[32:35], a[176:191]\n"
    "s_waitcnt lgkmcnt(3)\n"
    "ds_read_b128 a[64:67], v2 offset:57344\n"
    "v_mul_f32 v75, v75, v4\n"
    "v_mul_f32 v76, v76, v4\n"
    "v_mul_f32 v77, v77, v4\n"
    "v_mul_f32 v78, v78, v4\n"
    "v_mul_f32 v79, v79, v4\n"
    "v_mfma_f32_32x32x16_bf16 a[192:207], a[80:83], v[32:35], a[192:207]\n"
    "s_waitcnt lgkmcnt(3)\n"
    "ds_read_b128 a[68:71], v2 offset:58368\n"
    "v_cvt_pk_bf16_f32 v72, v72, v73\n"
    "v_cvt_pk_bf16_f32 v73, v74, v75\n"
    "v_cvt_pk_bf16_f32 v74, v76, v77\n"
    "v_cvt_pk_bf16_f32 v75, v78, v79\n"
    "v_mul_f32 v80, v80, v4\n"
    "v_mfma_f32_32x32x16_bf16 a[208:223], a[84:87], v[32:35], a[208:223]\n"
    "s_waitcnt lgkmcnt(3)\n"
    "ds_read_b128 a[72:75], v2 offset:59392\n"
    "v_mul_f32 v81, v81, v4\n"
    "v_mul_f32 v82, v82, v4\n"
    "v_mul_f32 v83, v83, v4\n"
    "v_mul_f32 v84, v84, v4\n"
    "v_mul_f32 v85, v85, v4\n"
    "v_mfma_f32_32x32x16_bf16 a[224:239], a[88:91], v[32:35], a[224:239]\n"
    "s_waitcnt lgkmcnt(3)\n"
    "ds_read_b128 a[76:79], v2 offset:60416\n"
    "v_mul_f32 v86, v86, v4\n"
    "v_mul_f32 v87, v87, v4\n"
    "v_cvt_pk_bf16_f32 v80, v80, v81\n"
    "v_cvt_pk_bf16_f32 v81, v82, v83\n"
    "v_cvt_pk_bf16_f32 v82, v84, v85\n"
    "v_mfma_f32_32x32x16_bf16 a[240:255], a[92:95], v[32:35], a[240:255]\n"
    "s_waitcnt lgkmcnt(3)\n"
    "ds_read_b128 a[80:83], v2 offset:61440\n"
    "v_cvt_pk_bf16_f32 v83, v86, v87\n"
    "v_mul_f32 v88, v88, v4\n"
    "v_mul_f32 v89, v89, v4\n"
    "v_mul_f32 v90, v90, v4\n"
    "v_mul_f32 v91, v91, v4\n"
    "v_mfma_f32_32x32x16_bf16 a[128:143], a[64:67], v[40:43], a[128:143]\n"
    "s_waitcnt lgkmcnt(3)\n"
    "ds_read_b128 a[84:87], v2 offset:62464\n"
    "v_mul_f32 v92, v92, v4\n"
    "v_mul_f32 v93, v93, v4\n"
    "v_mul_f32 v94, v94, v4\n"
    "v_mul_f32 v95, v95, v4\n"
    "v_cvt_pk_bf16_f32 v88, v88, v89\n"
    "v_mfma_f32_32x32x16_bf16 a[144:159], a[68:71], v[40:43], a[144:159]\n"
    "s_waitcnt lgkmcnt(3)\n"
    "ds_read_b128 a[88:91], v2 offset:63488\n"
    "v_cvt_pk_bf16_f32 v89, v90, v91\n"
    "v_cvt_pk_bf16_f32 v90, v92, v93\n"
    "v_cvt_pk_bf16_f32 v91, v94, v95\n"
    "v_exp_f32 v109, v109\n"
    "v_mfma_f32_32x32x16_bf16 a[160:175], a[72:75], v[40:43], a[160:175]\n"
    "s_waitcnt lgkmcnt(3)\n"
    "ds_read_b128 a[92:95], v2 offset:64512\n"
    "v_add_f32 v6, v6, v107\n"
    "v_sub_f32 v111, v111, v5\n"
    "v_exp_f32 v110, v110\n"
    "v_add_f32 v6, v6, v108\n"
    "v_mfma_f32_32x32x16_bf16 a[176:191], a[76:79], v[40:43], a[176:191]\n"
    "s_waitcnt vmcnt(8) lgkmcnt(3)\n"
    "s_barrier\n"
    "ds_read_b128 a[64:67], v1 offset:0\n"
    "v_exp_f32 v111, v111\n"
    "v_add_f32 v6, v6, v109\n"
    "v_add_f32 v6, v6, v110\n"
    "v_mfma_f32_32x32x16_bf16 a[192:207], a[80:83], v[40:43], a[192:207]\n"
    "s_waitcnt lgkmcnt(3)\n"
    "ds_read_b128 a[68:71], v1 offset:1024\n"
    "v_add_f32 v6, v6, v111\n"
    "v_mul_f32 v96, v96, v4\n"
    "v_mul_f32 v97, v97, v4\n"
    "v_mul_f32 v98, v98, v4\n"
    "v_mul_f32 v99, v99, v4\n"
    "v_mfma_f32_32x32x16_bf16 a[208:223], a[84:87], v[40:43], a[208:223]\n"
    "s_waitcnt lgkmcnt(3)\n"
    "ds_read_b128 a[72:75], v1 offset:2048\n"
    "v_mul_f32 v100, v100, v4\n"
    "v_mul_f32 v101, v101, v4\n"
    "v_mul_f32 v102, v102, v4\n"
    "v_mul_f32 v103, v103, v4\n"
    "v_cvt_pk_bf16_f32 v96, v96, v97\n"
    "v_mfma_f32_32x32x16_bf16 a[224:239], a[88:91], v[40:43], a[224:239]\n"
    "s_waitcnt lgkmcnt(3)\n"
    "ds_read_b128 a[76:79], v1 offset:3072\n"
    "v_cvt_pk_bf16_f32 v97, v98, v99\n"
    "v_cvt_pk_bf16_f32 v98, v100, v101\n"
    "v_cvt_pk_bf16_f32 v99, v102, v103\n"
    "v_sub_f32 v112, v112, v5\n"
    "v_sub_f32 v113, v113, v5\n"
    "v_mfma_f32_32x32x16_bf16 a[240:255], a[92:95], v[40:43], a[240:255]\n"
    "s_waitcnt lgkmcnt(3)\n"
    "ds_read_b128 a[80:83], v1 offset:4096\n"
    "v_exp_f32 v112, v112\n"
    "v_sub_f32 v114, v114, v5\n"
    "v_exp_f32 v113, v113\n"
    "v_mfma_f32_32x32x16_bf16 a[128:143], a[64:67], v[48:51], a[128:143]\n"
    "s_waitcnt lgkmcnt(3)\n"
    "ds_read_b128 a[84:87], v1 offset:5120\n"
    "v_sub_f32 v115, v115, v5\n"
    "v_exp_f32 v114, v114\n"
    "v_add_f32 v6, v6, v112\n"
    "v_sub_f32 v116, v116, v5\n"
    "v_mfma_f32_32x32x16_bf16 a[144:159], a[68:71], v[48:51], a[144:159]\n"
    "s_waitcnt lgkmcnt(3)\n"
    "s_add_u32 m0, s65, 0x10000\n"
    "ds_read_b128 a[88:91], v1 offset:6144\n"
    "v_exp_f32 v115, v115\n"
    "v_add_f32 v6, v6, v113\n"
    "v_sub_f32 v117, v117, v5\n"
    "v_mfma_f32_32x32x16_bf16 a[160:175], a[72:75], v[48:51], a[160:175]\n"
    "s_waitcnt lgkmcnt(3)\n"
    "s_add_u32 s64, s67, 0x18000\n"
    "ds_read_b128 a[92:95], v1 offset:7168\n"
    "v_exp_f32 v116, v116\n"
    "v_add_f32 v6, v6, v114\n"
    "v_sub_f32 v118, v118, v5\n"
    "v_mfma_f32_32x32x16_bf16 a[176:191], a[76:79], v[48:51], a[176:191]\n"
    "s_waitcnt lgkmcnt(3)\n"
    "ds_read_b128 a[64:67], v1 offset:8192\n"
    "buffer_load_dwordx4 v0, s[48:51], s64 offen offset:0 lds\n"
    "v_exp_f32 v117, v117\n"
    "v_add_f32 v6, v6, v115\n"
    "v_mfma_f32_32x32x16_bf16 a[192:207], a[80:83], v[48:51], a[192:207]\n"
    "s_waitcnt lgkmcnt(3)\n"
    "ds_read_b128 a[68:71], v1 offset:9216\n"
    "v_sub_f32 v119, v119, v5\n"
    "v_exp_f32 v118, v118\n"
    "v_add_f32 v6, v6, v116\n"
    "v_mfma_f32_32x32x16_bf16 a[208:223], a[84:87], v[48:51], a[208:223]\n"
    "s_waitcnt lgkmcnt(3)\n"
    "ds_read_b128 a[72:75], v1 offset:10240\n"
    "buffer_load_dwordx4 v0, s[48:51], s64 offen offset:1024 lds\n"
    "v_exp_f32 v119, v119\n"
    "v_add_f32 v6, v6, v117\n"
    "v_mfma_f32_32x32x16_bf16 a[224:239], a[88:91], v[48:51], a[224:239]\n"
    "s_waitcnt lgkmcnt(3)\n"
    "ds_read_b128 a[76:79], v1 offset:11264\n"
    "v_add_f32 v6, v6, v118\n"
    "v_add_f32 v6, v6, v119\n"
    "v_mul_f32 v104, v104, v4\n"
    "v_mul_f32 v105, v105, v4\n"
    "v_mul_f32 v106, v106, v4\n"
    "v_mfma_f32_32x32x16_bf16 a[240:255], a[92:95], v[48:51], a[240:255]\n"
    "s_waitcnt lgkmcnt(3)\n"
    "ds_read_b128 a[80:83], v1 offset:12288\n"
    "buffer_load_dwordx4 v0, s[48:51], s64 offen offset:2048 lds\n"
    "v_mul_f32 v107, v107, v4\n"
    "v_mul_f32 v108, v108, v4\n"
    "v_mul_f32 v109, v109, v4\n"
    "v_mfma_f32_32x32x16_bf16 a[128:143], a[64:67], v[56:59], a[128:143]\n"
    "s_waitcnt lgkmcnt(3)\n"
    "ds_read_b128 a[84:87], v1 offset:13312\n"
    "v_accvgpr_read_b32 v15, a117\n"
    "v_mul_f32 v110, v110, v4\n"
    "v_mul_f32 v111, v111, v4\n"
    "v_cvt_pk_bf16_f32 v104, v104, v105\n"
    "v_cvt_pk_bf16_f32 v105, v106, v107\n"
    "v_mfma_f32_32x32x16_bf16 a[144:159], a[68:71], v[56:59], a[144:159]\n"
    "s_waitcnt lgkmcnt(3)\n"
    "ds_read_b128 a[88:91], v1 offset:14336\n"
    "buffer_load_dwordx4 v0, s[48:51], s64 offen offset:3072 lds\n"
    "buffer_load_dword a127, v15, s[28:31], 0 offen offset:0\n"
    "v_cvt_pk_bf16_f32 v106, v108, v109\n"
    "v_mfma_f32_32x32x16_bf16 a[160:175], a[72:75], v[56:59], a[160:175]\n"
    "s_waitcnt lgkmcnt(3)\n"
    "s_add_u32 m0, s65, 0x14000\n"
    "ds_read_b128 a[92:95], v1 offset:15360\n"
    "v_cvt_pk_bf16_f32 v107, v110, v111\n"
    "v_sub_f32 v120, v120, v5\n"
    "v_sub_f32 v121, v121, v5\n"
    "v_mfma_f32_32x32x16_bf16 a[176:191], a[76:79], v[56:59], a[176:191]\n"
    "s_waitcnt lgkmcnt(3)\n"
    "s_add_u32 s64, s67, 0x1c000\n"
    "ds_read_b128 a[64:67], v1 offset:16384\n"
    "v_exp_f32 v120, v120\n"
    "v_sub_f32 v122, v122, v5\n"
    "v_mfma_f32_32x32x16_bf16 a[192:207], a[80:83], v[56:59], a[192:207]\n"
    "s_waitcnt lgkmcnt(3)\n"
    "ds_read_b128 a[68:71], v1 offset:17408\n"
    "buffer_load_dwordx4 v0, s[48:51], s64 offen offset:0 lds\n"
    "v_exp_f32 v121, v121\n"
    "v_sub_f32 v123, v123, v5\n"
    "v_mfma_f32_32x32x16_bf16 a[208:223], a[84:87], v[56:59], a[208:223]\n"
    "s_waitcnt lgkmcnt(3)\n"
    "ds_read_b128 a[72:75], v1 offset:18432\n"
    "v_exp_f32 v122, v122\n"
    "v_add_f32 v6, v6, v120\n"
    "v_sub_f32 v124, v124, v5\n"
    "v_mfma_f32_32x32x16_bf16 a[224:239], a[88:91], v[56:59], a[224:239]\n"
    "s_waitcnt lgkmcnt(3)\n"
    "ds_read_b128 a[76:79], v1 offset:19456\n"
    "buffer_load_dwordx4 v0, s[48:51], s64 offen offset:1024 lds\n"
    "v_exp_f32 v123, v123\n"
    "v_add_f32 v6, v6, v121\n"
    "v_mfma_f32_32x32x16_bf16 a[240:255], a[92:95], v[56:59], a[240:255]\n"
    "s_waitcnt lgkmcnt(3)\n"
    "ds_read_b128 a[80:83], v1 offset:20480\n"
    "v_sub_f32 v125, v125, v5\n"
    "v_exp_f32 v124, v124\n"
    "v_add_f32 v6, v6, v122\n"
    "v_sub_f32 v126, v126, v5\n"
    "v_mfma_f32_32x32x16_bf16 a[128:143], a[64:67], v[64:67], a[128:143]\n"
    "s_waitcnt lgkmcnt(3)\n"
    "ds_read_b128 a[84:87], v1 offset:21504\n"
    "buffer_load_dwordx4 v0, s[48:51], s64 offen offset:2048 lds\n"
    "v_exp_f32 v125, v125\n"
    "v_add_f32 v6, v6, v123\n"
    "v_mfma_f32_32x32x16_bf16 a[144:159], a[68:71], v[64:67], a[144:159]\n"
    "s_waitcnt lgkmcnt(3)\n"
    "ds_read_b128 a[88:91], v1 offset:22528\n"
    "v_sub_f32 v127, v127, v5\n"
    "v_exp_f32 v126, v126\n"
    "v_add_f32 v6, v6, v124\n"
    "v_mfma_f32_32x32x16_bf16 a[160:175], a[72:75], v[64:67], a[160:175]\n"
    "s_waitcnt lgkmcnt(3)\n"
    "ds_read_b128 a[92:95], v1 offset:23552\n"
    "buffer_load_dwordx4 v0, s[48:51], s64 offen offset:3072 lds\n"
    "v_exp_f32 v127, v127\n"
    "v_add_f32 v6, v6, v125\n"
    "v_mfma_f32_32x32x16_bf16 a[176:191], a[76:79], v[64:67], a[176:191]\n"
    "s_waitcnt lgkmcnt(3)\n"
    "ds_read_b128 a[64:67], v1 offset:24576\n"
    "v_add_f32 v6, v6, v126\n"
    "v_add_f32 v6, v6, v127\n"
    "v_mul_f32 v112, v112, v4\n"
    "v_mul_f32 v113, v113, v4\n"
    "v_mul_f32 v114, v114, v4\n"
    "v_mfma_f32_32x32x16_bf16 a[192:207], a[80:83], v[64:67], a[192:207]\n"
    "s_waitcnt lgkmcnt(3)\n"
    "ds_read_b128 a[68:71], v1 offset:25600\n"
    "v_mul_f32 v115, v115, v4\n"
    "v_mul_f32 v116, v116, v4\n"
    "v_mul_f32 v117, v117, v4\n"
    "v_mul_f32 v118, v118, v4\n"
    "v_mul_f32 v119, v119, v4\n"
    "v_mfma_f32_32x32x16_bf16 a[208:223], a[84:87], v[64:67], a[208:223]\n"
    "s_waitcnt lgkmcnt(3)\n"
    "ds_read_b128 a[72:75], v1 offset:26624\n"
    "v_cvt_pk_bf16_f32 v112, v112, v113\n"
    "v_cvt_pk_bf16_f32 v113, v114, v115\n"
    "v_cvt_pk_bf16_f32 v114, v116, v117\n"
    "v_cvt_pk_bf16_f32 v115, v118, v119\n"
    "v_sub_f32 v128, v128, v5\n"
    "v_mfma_f32_32x32x16_bf16 a[224:239], a[88:91], v[64:67], a[224:239]\n"
    "s_waitcnt lgkmcnt(3)\n"
    "ds_read_b128 a[76:79], v1 offset:27648\n"
    "v_sub_f32 v129, v129, v5\n"
    "v_exp_f32 v128, v128\n"
    "v_sub_f32 v130, v130, v5\n"
    "v_mfma_f32_32x32x16_bf16 a[240:255], a[92:95], v[64:67], a[240:255]\n"
    "s_waitcnt lgkmcnt(3)\n"
    "ds_read_b128 a[80:83], v1 offset:28672\n"
    "v_exp_f32 v129, v129\n"
    "v_sub_f32 v131, v131, v5\n"
    "v_exp_f32 v130, v130\n"
    "v_mfma_f32_32x32x16_bf16 a[128:143], a[64:67], v[72:75], a[128:143]\n"
    "s_waitcnt lgkmcnt(3)\n"
    "ds_read_b128 a[84:87], v1 offset:29696\n"
    "v_add_f32 v6, v6, v128\n"
    "v_sub_f32 v132, v132, v5\n"
    "v_exp_f32 v131, v131\n"
    "v_add_f32 v6, v6, v129\n"
    "v_mfma_f32_32x32x16_bf16 a[144:159], a[68:71], v[72:75], a[144:159]\n"
    "s_waitcnt lgkmcnt(3)\n"
    "ds_read_b128 a[88:91], v1 offset:30720\n"
    "v_sub_f32 v133, v133, v5\n"
    "v_exp_f32 v132, v132\n"
    "v_add_f32 v6, v6, v130\n"
    "v_sub_f32 v134, v134, v5\n"
    "v_mfma_f32_32x32x16_bf16 a[160:175], a[72:75], v[72:75], a[160:175]\n"
    "s_waitcnt lgkmcnt(3)\n"
    "ds_read_b128 a[92:95], v1 offset:31744\n"
    "v_exp_f32 v133, v133\n"
    "v_add_f32 v6, v6, v131\n"
    "v_sub_f32 v135, v135, v5\n"
    "v_mfma_f32_32x32x16_bf16 a[176:191], a[76:79], v[72:75], a[176:191]\n"
    "s_waitcnt vmcnt(9) lgkmcnt(3)\n"
    "s_barrier\n"
    "ds_read_b128 a[64:67], v1 offset:32768\n"
    "v_exp_f32 v134, v134\n"
    "v_add_f32 v6, v6, v132\n"
    "v_mfma_f32_32x32x16_bf16 a[192:207], a[80:83], v[72:75], a[192:207]\n"
    "s_waitcnt lgkmcnt(3)\n"
    "ds_read_b128 a[68:71], v1 offset:33792\n"
    "v_exp_f32 v135, v135\n"
    "v_add_f32 v6, v6, v133\n"
    "v_add_f32 v6, v6, v134\n"
    "v_add_f32 v6, v6, v135\n"
    "v_mfma_f32_32x32x16_bf16 a[208:223], a[84:87], v[72:75], a[208:223]\n"
    "s_waitcnt lgkmcnt(3)\n"
    "ds_read_b128 a[72:75], v1 offset:34816\n"
    "v_mul_f32 v120, v120, v4\n"
    "v_mul_f32 v121, v121, v4\n"
    "v_mul_f32 v122, v122, v4\n"
    "v_mul_f32 v123, v123, v4\n"
    "v_mul_f32 v124, v124, v4\n"
    "v_mfma_f32_32x32x16_bf16 a[224:239], a[88:91], v[72:75], a[224:239]\n"
    "s_waitcnt lgkmcnt(3)\n"
    "ds_read_b128 a[76:79], v1 offset:35840\n"
    "v_mul_f32 v125, v125, v4\n"
    "v_mul_f32 v126, v126, v4\n"
    "v_mul_f32 v127, v127, v4\n"
    "v_cvt_pk_bf16_f32 v120, v120, v121\n"
    "v_cvt_pk_bf16_f32 v121, v122, v123\n"
    "v_mfma_f32_32x32x16_bf16 a[240:255], a[92:95], v[72:75], a[240:255]\n"
    "s_waitcnt lgkmcnt(3)\n"
    "ds_read_b128 a[80:83], v1 offset:36864\n"
    "v_cvt_pk_bf16_f32 v122, v124, v125\n"
    "v_cvt_pk_bf16_f32 v123, v126, v127\n"
    "v_sub_f32 v136, v136, v5\n"
    "v_sub_f32 v137, v137, v5\n"
    "v_mfma_f32_32x32x16_bf16 a[128:143], a[64:67], v[80:83], a[128:143]\n"
    "s_waitcnt lgkmcnt(3)\n"
    "ds_read_b128 a[84:87], v1 offset:37888\n"
    "v_exp_f32 v136, v136\n"
    "v_sub_f32 v138, v138, v5\n"
    "v_exp_f32 v137, v137\n"
    "v_mfma_f32_32x32x16_bf16 a[144:159], a[68:71], v[80:83], a[144:159]\n"
    "s_waitcnt lgkmcnt(3)\n"
    "s_add_u32 m0, s65, 0x18000\n"
    "ds_read_b128 a[88:91], v1 offset:38912\n"
    "v_sub_f32 v139, v139, v5\n"
    "v_exp_f32 v138, v138\n"
    "v_add_f32 v6, v6, v136\n"
    "v_mfma_f32_32x32x16_bf16 a[160:175], a[72:75], v[80:83], a[160:175]\n"
    "s_waitcnt lgkmcnt(3)\n"
    "s_add_u32 s64, s67, 0x20000\n"
    "ds_read_b128 a[92:95], v1 offset:39936\n"
    "v_sub_f32 v140, v140, v5\n"
    "v_exp_f32 v139, v139\n"
    "v_add_f32 v6, v6, v137\n"
    "v_mfma_f32_32x32x16_bf16 a[176:191], a[76:79], v[80:83], a[176:191]\n"
    "s_waitcnt lgkmcnt(3)\n"
    "ds_read_b128 a[64:67], v1 offset:40960\n"
    "buffer_load_dwordx4 v0, s[48:51], s64 offen offset:0 lds\n"
    "v_sub_f32 v141, v141, v5\n"
    "v_exp_f32 v140, v140\n"
    "v_mfma_f32_32x32x16_bf16 a[192:207], a[80:83], v[80:83], a[192:207]\n"
    "s_waitcnt lgkmcnt(3)\n"
    "ds_read_b128 a[68:71], v1 offset:41984\n"
    "v_add_f32 v6, v6, v138\n"
    "v_sub_f32 v142, v142, v5\n"
    "v_exp_f32 v141, v141\n"
    "v_add_f32 v6, v6, v139\n"
    "v_mfma_f32_32x32x16_bf16 a[208:223], a[84:87], v[80:83], a[208:223]\n"
    "s_waitcnt lgkmcnt(3)\n"
    "ds_read_b128 a[72:75], v1 offset:43008\n"
    "buffer_load_dwordx4 v0, s[48:51], s64 offen offset:1024 lds\n"
    "v_sub_f32 v143, v143, v5\n"
    "v_exp_f32 v142, v142\n"
    "v_mfma_f32_32x32x16_bf16 a[224:239], a[88:91], v[80:83], a[224:239]\n"
    "s_waitcnt lgkmcnt(3)\n"
    "ds_read_b128 a[76:79], v1 offset:44032\n"
    "v_add_f32 v6, v6, v140\n"
    "v_exp_f32 v143, v143\n"
    "v_add_f32 v6, v6, v141\n"
    "v_add_f32 v6, v6, v142\n"
    "v_mfma_f32_32x32x16_bf16 a[240:255], a[92:95], v[80:83], a[240:255]\n"
    "s_waitcnt lgkmcnt(3)\n"
    "ds_read_b128 a[80:83], v1 offset:45056\n"
    "buffer_load_dwordx4 v0, s[48:51], s64 offen offset:2048 lds\n"
    "v_add_f32 v6, v6, v143\n"
    "v_mul_f32 v128, v128, v4\n"
    "v_mul_f32 v129, v129, v4\n"
    "v_mfma_f32_32x32x16_bf16 a[128:143], a[64:67], v[88:91], a[128:143]\n"
    "s_waitcnt lgkmcnt(3)\n"
    "ds_read_b128 a[84:87], v1 offset:46080\n"
    "v_mul_f32 v130, v130, v4\n"
    "v_mul_f32 v131, v131, v4\n"
    "v_mul_f32 v132, v132, v4\n"
    "v_mul_f32 v133, v133, v4\n"
    "v_mul_f32 v134, v134, v4\n"
    "v_mfma_f32_32x32x16_bf16 a[144:159], a[68:71], v[88:91], a[144:159]\n"
    "s_waitcnt lgkmcnt(3)\n"
    "ds_read_b128 a[88:91], v1 offset:47104\n"
    "buffer_load_dwordx4 v0, s[48:51], s64 offen offset:3072 lds\n"
    "v_mul_f32 v135, v135, v4\n"
    "v_cvt_pk_bf16_f32 v128, v128, v129\n"
    "v_cvt_pk_bf16_f32 v129, v130, v131\n"
    "v_mfma_f32_32x32x16_bf16 a[160:175], a[72:75], v[88:91], a[160:175]\n"
    "s_waitcnt lgkmcnt(3)\n"
    "s_add_u32 m0, s65, 0x1c000\n"
    "ds_read_b128 a[92:95], v1 offset:48128\n"
    "v_cvt_pk_bf16_f32 v130, v132, v133\n"
    "v_cvt_pk_bf16_f32 v131, v134, v135\n"
    "v_accvgpr_read_b32 v16, a96\n"
    "v_accvgpr_read_b32 v17, a97\n"
    "v_mfma_f32_32x32x16_bf16 a[176:191], a[76:79], v[88:91], a[176:191]\n"
    "s_waitcnt lgkmcnt(3)\n"
    "s_add_u32 s64, s67, 0x24000\n"
    "ds_read_b128 a[64:67], v1 offset:49152\n"
    "v_accvgpr_read_b32 v18, a98\n"
    "v_accvgpr_read_b32 v19, a99\n"
    "v_accvgpr_read_b32 v20, a100\n"
    "v_accvgpr_read_b32 v21, a101\n"
    "v_mfma_f32_32x32x16_bf16 a[192:207], a[80:83], v[88:91], a[192:207]\n"
    "s_waitcnt lgkmcnt(3)\n"
    "ds_read_b128 a[68:71], v1 offset:50176\n"
    "buffer_load_dwordx4 v0, s[48:51], s64 offen offset:0 lds\n"
    "v_accvgpr_read_b32 v22, a102\n"
    "v_accvgpr_read_b32 v23, a103\n"
    "v_sub_f32 v16, v16, v8\n"
    "v_mfma_f32_32x32x16_bf16 a[208:223], a[84:87], v[88:91], a[208:223]\n"
    "s_waitcnt lgkmcnt(3)\n"
    "ds_read_b128 a[72:75], v1 offset:51200\n"
    "v_sub_f32 v17, v17, v8\n"
    "v_exp_f32 v16, v16\n"
    "v_sub_f32 v18, v18, v8\n"
    "v_mfma_f32_32x32x16_bf16 a[224:239], a[88:91], v[88:91], a[224:239]\n"
    "s_waitcnt lgkmcnt(3)\n"
    "ds_read_b128 a[76:79], v1 offset:52224\n"
    "buffer_load_dwordx4 v0, s[48:51], s64 offen offset:1024 lds\n"
    "v_exp_f32 v17, v17\n"
    "v_sub_f32 v19, v19, v8\n"
    "v_mfma_f32_32x32x16_bf16 a[240:255], a[92:95], v[88:91], a[240:255]\n"
    "s_waitcnt lgkmcnt(3)\n"
    "ds_read_b128 a[80:83], v1 offset:53248\n"
    "v_exp_f32 v18, v18\n"
    "v_mov_b32 v7, v16\n"
    "v_sub_f32 v20, v20, v8\n"
    "v_mfma_f32_32x32x16_bf16 a[128:143], a[64:67], v[96:99], a[128:143]\n"
    "s_waitcnt lgkmcnt(3)\n"
    "ds_read_b128 a[84:87], v1 offset:54272\n"
    "buffer_load_dwordx4 v0, s[48:51], s64 offen offset:2048 lds\n"
    "v_exp_f32 v19, v19\n"
    "v_add_f32 v7, v7, v17\n"
    "v_mfma_f32_32x32x16_bf16 a[144:159], a[68:71], v[96:99], a[144:159]\n"
    "s_waitcnt lgkmcnt(3)\n"
    "ds_read_b128 a[88:91], v1 offset:55296\n"
    "v_cvt_pk_bf16_f32 v16, v16, v17\n"
    "v_sub_f32 v21, v21, v8\n"
    "v_exp_f32 v20, v20\n"
    "v_add_f32 v7, v7, v18\n"
    "v_mfma_f32_32x32x16_bf16 a[160:175], a[72:75], v[96:99], a[160:175]\n"
    "s_waitcnt lgkmcnt(3)\n"
    "ds_read_b128 a[92:95], v1 offset:56320\n"
    "buffer_load_dwordx4 v0, s[48:51], s64 offen offset:3072 lds\n"
    "v_sub_f32 v22, v22, v8\n"
    "v_exp_f32 v21, v21\n"
    "v_mfma_f32_32x32x16_bf16 a[176:191], a[76:79], v[96:99], a[176:191]\n"
    "s_waitcnt lgkmcnt(3)\n"
    "ds_read_b128 a[64:67], v1 offset:57344\n"
    "v_add_f32 v7, v7, v19\n"
    "v_cvt_pk_bf16_f32 v17, v18, v19\n"
    "v_sub_f32 v23, v23, v8\n"
    "v_exp_f32 v22, v22\n"
    "v_mfma_f32_32x32x16_bf16 a[192:207], a[80:83], v[96:99], a[192:207]\n"
    "s_waitcnt lgkmcnt(3)\n"
    "ds_read_b128 a[68:71], v1 offset:58368\n"
    "v_add_f32 v7, v7, v20\n"
    "v_exp_f32 v23, v23\n"
    "v_add_f32 v7, v7, v21\n"
    "v_cvt_pk_bf16_f32 v18, v20, v21\n"
    "v_mfma_f32_32x32x16_bf16 a[208:223], a[84:87], v[96:99], a[208:223]\n"
    "s_waitcnt lgkmcnt(3)\n"
    "ds_read_b128 a[72:75], v1 offset:59392\n"
    "v_add_f32 v7, v7, v22\n"
    "v_add_f32 v7, v7, v23\n"
    "v_cvt_pk_bf16_f32 v19, v22, v23\n"
    "v_mul_f32 v136, v136, v4\n"
    "v_mul_f32 v137, v137, v4\n"
    "v_mfma_f32_32x32x16_bf16 a[224:239], a[88:91], v[96:99], a[224:239]\n"
    "s_waitcnt lgkmcnt(3)\n"
    "ds_read_b128 a[76:79], v1 offset:60416\n"
    "v_mul_f32 v138, v138, v4\n"
    "v_mul_f32 v139, v139, v4\n"
    "v_mul_f32 v140, v140, v4\n"
    "v_mul_f32 v141, v141, v4\n"
    "v_mul_f32 v142, v142, v4\n"
    "v_mfma_f32_32x32x16_bf16 a[240:255], a[92:95], v[96:99], a[240:255]\n"
    "s_waitcnt lgkmcnt(3)\n"
    "ds_read_b128 a[80:83], v1 offset:61440\n"
    "v_mul_f32 v143, v143, v4\n"
    "v_cvt_pk_bf16_f32 v136, v136, v137\n"
    "v_cvt_pk_bf16_f32 v137, v138, v139\n"
    "v_cvt_pk_bf16_f32 v138, v140, v141\n"
    "v_cvt_pk_bf16_f32 v139, v142, v143\n"
    "v_mfma_f32_32x32x16_bf16 a[128:143], a[64:67], v[104:107], a[128:143]\n"
    "s_waitcnt lgkmcnt(3)\n"
    "ds_read_b128 a[84:87], v1 offset:62464\n"
    "v_accvgpr_read_b32 v24, a104\n"
    "v_accvgpr_read_b32 v25, a105\n"
    "v_accvgpr_read_b32 v26, a106\n"
    "v_accvgpr_read_b32 v27, a107\n"
    "v_accvgpr_read_b32 v28, a108\n"
    "v_mfma_f32_32x32x16_bf16 a[144:159], a[68:71], v[104:107], a[144:159]\n"
    "s_waitcnt lgkmcnt(3)\n"
    "ds_read_b128 a[88:91], v1 offset:63488\n"
    "v_accvgpr_read_b32 v29, a109\n"
    "v_accvgpr_read_b32 v30, a110\n"
    "v_accvgpr_read_b32 v31, a111\n"
    "v_sub_f32 v24, v24, v8\n"
    "v_sub_f32 v25, v25, v8\n"
    "v_mfma_f32_32x32x16_bf16 a[160:175], a[72:75], v[104:107], a[160:175]\n"
    "s_waitcnt lgkmcnt(3)\n"
    "ds_read_b128 a[92:95], v1 offset:64512\n"
    "v_exp_f32 v24, v24\n"
    "v_sub_f32 v26, v26, v8\n"
    "v_exp_f32 v25, v25\n"
    "v_mfma_f32_32x32x16_bf16 a[176:191], a[76:79], v[104:107], a[176:191]\n"
    "s_waitcnt vmcnt(8) lgkmcnt(3)\n"
    "s_barrier\n"
    "ds_read_b128 a[64:67], v2 offset:0\n"
    "v_sub_f32 v27, v27, v8\n"
    "v_exp_f32 v26, v26\n"
    "v_add_f32 v7, v7, v24\n"
    "v_mfma_f32_32x32x16_bf16 a[192:207], a[80:83], v[104:107], a[192:207]\n"
    "s_waitcnt lgkmcnt(3)\n"
    "ds_read_b128 a[68:71], v2 offset:1024\n"
    "v_sub_f32 v28, v28, v8\n"
    "v_exp_f32 v27, v27\n"
    "v_add_f32 v7, v7, v25\n"
    "v_cvt_pk_bf16_f32 v24, v24, v25\n"
    "v_mfma_f32_32x32x16_bf16 a[208:223], a[84:87], v[104:107], a[208:223]\n"
    "s_waitcnt lgkmcnt(3)\n"
    "ds_read_b128 a[72:75], v2 offset:2048\n"
    "v_sub_f32 v29, v29, v8\n"
    "v_exp_f32 v28, v28\n"
    "v_add_f32 v7, v7, v26\n"
    "v_sub_f32 v30, v30, v8\n"
    "v_mfma_f32_32x32x16_bf16 a[224:239], a[88:91], v[104:107], a[224:239]\n"
    "s_waitcnt lgkmcnt(3)\n"
    "ds_read_b128 a[76:79], v2 offset:3072\n"
    "v_exp_f32 v29, v29\n"
    "v_add_f32 v7, v7, v27\n"
    "v_cvt_pk_bf16_f32 v25, v26, v27\n"
    "v_sub_f32 v31, v31, v8\n"
    "v_mfma_f32_32x32x16_bf16 a[240:255], a[92:95], v[104:107], a[240:255]\n"
    "s_waitcnt lgkmcnt(3)\n"
    "ds_read_b128 a[80:83], v2 offset:4096\n"
    "v_exp_f32 v30, v30\n"
    "v_add_f32 v7, v7, v28\n"
    "v_exp_f32 v31, v31\n"
    "v_mfma_f32_32x32x16_bf16 a[128:143], a[64:67], v[112:115], a[128:143]\n"
    "s_waitcnt lgkmcnt(3)\n"
    "ds_read_b128 a[84:87], v2 offset:5120\n"
    "v_add_f32 v7, v7, v29\n"
    "v_cvt_pk_bf16_f32 v26, v28, v29\n"
    "v_add_f32 v7, v7, v30\n"
    "v_add_f32 v7, v7, v31\n"
    "v_cvt_pk_bf16_f32 v27, v30, v31\n"
    "v_mfma_f32_32x32x16_bf16 a[144:159], a[68:71], v[112:115], a[144:159]\n"
    "s_waitcnt lgkmcnt(3)\n"
    "s_add_u32 m0, s65, 0x0\n"
    "ds_read_b128 a[88:91], v2 offset:6144\n"
    "v_sub_f32 v144, v144, v8\n"
    "v_sub_f32 v145, v145, v8\n"
    "v_exp_f32 v144, v144\n"
    "v_mfma_f32_32x32x16_bf16 a[160:175], a[72:75], v[112:115], a[160:175]\n"
    "s_waitcnt lgkmcnt(3)\n"
    "s_add_u32 s64, s67, 0x28000\n"
    "ds_read_b128 a[92:95], v2 offset:7168\n"
    "v_sub_f32 v146, v146, v8\n"
    "v_exp_f32 v145, v145\n"
    "v_sub_f32 v147, v147, v8\n"
    "v_mfma_f32_32x32x16_bf16 a[176:191], a[76:79], v[112:115], a[176:191]\n"
    "s_waitcnt lgkmcnt(3)\n"
    "ds_read_b128 a[64:67], v2 offset:8192\n"
    "buffer_load_dwordx4 v0, s[48:51], s64 offen offset:0 lds\n"
    "v_exp_f32 v146, v146\n"
    "v_add_f32 v7, v7, v144\n"
    "v_mfma_f32_32x32x16_bf16 a[192:207], a[80:83], v[112:115], a[192:207]\n"
    "s_waitcnt lgkmcnt(3)\n"
    "ds_read_b128 a[68:71], v2 offset:9216\n"
    "v_sub_f32 v148, v148, v8\n"
    "v_exp_f32 v147, v147\n"
    "v_add_f32 v7, v7, v145\n"
    "v_cvt_pk_bf16_f32 v144, v144, v145\n"
    "v_mfma_f32_32x32x16_bf16 a[208:223], a[84:87], v[112:115], a[208:223]\n"
    "s_waitcnt lgkmcnt(3)\n"
    "ds_read_b128 a[72:75], v2 offset:10240\n"
    "buffer_load_dwordx4 v0, s[48:51], s64 offen offset:1024 lds\n"
    "v_sub_f32 v149, v149, v8\n"
    "v_exp_f32 v148, v148\n"
    "v_mfma_f32_32x32x16_bf16 a[224:239], a[88:91], v[112:115], a[224:239]\n"
    "s_waitcnt lgkmcnt(3)\n"
    "ds_read_b128 a[76:79], v2 offset:11264\n"
    "v_add_f32 v7, v7, v146\n"
    "v_sub_f32 v150, v150, v8\n"
    "v_exp_f32 v149, v149\n"
    "v_add_f32 v7, v7, v147\n"
    "v_mfma_f32_32x32x16_bf16 a[240:255], a[92:95], v[112:115], a[240:255]\n"
    "s_waitcnt lgkmcnt(3)\n"
    "ds_read_b128 a[80:83], v2 offset:12288\n"
    "buffer_load_dwordx4 v0, s[48:51], s64 offen offset:2048 lds\n"
    "v_cvt_pk_bf16_f32 v145, v146, v147\n"
    "v_sub_f32 v151, v151, v8\n"
    "v_mfma_f32_32x32x16_bf16 a[128:143], a[64:67], v[120:123], a[128:143]\n"
    "s_waitcnt lgkmcnt(3)\n"
    "ds_read_b128 a[84:87], v2 offset:13312\n"
    "v_exp_f32 v150, v150\n"
    "v_add_f32 v7, v7, v148\n"
    "v_exp_f32 v151, v151\n"
    "v_mfma_f32_32x32x16_bf16 a[144:159], a[68:71], v[120:123], a[144:159]\n"
    "s_waitcnt lgkmcnt(3)\n"
    "ds_read_b128 a[88:91], v2 offset:14336\n"
    "buffer_load_dwordx4 v0, s[48:51], s64 offen offset:3072 lds\n"
    "v_add_f32 v7, v7, v149\n"
    "v_cvt_pk_bf16_f32 v146, v148, v149\n"
    "v_add_f32 v7, v7, v150\n"
    "v_mfma_f32_32x32x16_bf16 a[160:175], a[72:75], v[120:123], a[160:175]\n"
    "s_waitcnt lgkmcnt(3)\n"
    "s_add_u32 m0, s65, 0x4000\n"
    "ds_read_b128 a[92:95], v2 offset:15360\n"
    "v_add_f32 v7, v7, v151\n"
    "v_cvt_pk_bf16_f32 v147, v150, v151\n"
    "v_sub_f32 v152, v152, v8\n"
    "v_sub_f32 v153, v153, v8\n"
    "v_mfma_f32_32x32x16_bf16 a[176:191], a[76:79], v[120:123], a[176:191]\n"
    "s_waitcnt lgkmcnt(3)\n"
    "s_add_u32 s64, s67, 0x2c000\n"
    "ds_read_b128 a[64:67], v2 offset:16384\n"
    "v_exp_f32 v152, v152\n"
    "v_sub_f32 v154, v154, v8\n"
    "v_mfma_f32_32x32x16_bf16 a[192:207], a[80:83], v[120:123], a[192:207]\n"
    "s_waitcnt lgkmcnt(3)\n"
    "ds_read_b128 a[68:71], v2 offset:17408\n"
    "buffer_load_dwordx4 v0, s[48:51], s64 offen offset:0 lds\n"
    "v_exp_f32 v153, v153\n"
    "v_sub_f32 v155, v155, v8\n"
    "v_mfma_f32_32x32x16_bf16 a[208:223], a[84:87], v[120:123], a[208:223]\n"
    "s_waitcnt lgkmcnt(3)\n"
    "ds_read_b128 a[72:75], v2 offset:18432\n"
    "v_exp_f32 v154, v154\n"
    "v_add_f32 v7, v7, v152\n"
    "v_sub_f32 v156, v156, v8\n"
    "v_mfma_f32_32x32x16_bf16 a[224:239], a[88:91], v[120:123], a[224:239]\n"
    "s_waitcnt lgkmcnt(3)\n"
    "ds_read_b128 a[76:79], v2 offset:19456\n"
    "buffer_load_dwordx4 v0, s[48:51], s64 offen offset:1024 lds\n"
    "v_exp_f32 v155, v155\n"
    "v_add_f32 v7, v7, v153\n"
    "v_mfma_f32_32x32x16_bf16 a[240:255], a[92:95], v[120:123], a[240:255]\n"
    "s_waitcnt lgkmcnt(3)\n"
    "ds_read_b128 a[80:83], v2 offset:20480\n"
    "v_cvt_pk_bf16_f32 v152, v152, v153\n"
    "v_sub_f32 v157, v157, v8\n"
    "v_exp_f32 v156, v156\n"
    "v_add_f32 v7, v7, v154\n"
    "v_mfma_f32_32x32x16_bf16 a[128:143], a[64:67], v[128:131], a[128:143]\n"
    "s_waitcnt lgkmcnt(3)\n"
    "ds_read_b128 a[84:87], v2 offset:21504\n"
    "buffer_load_dwordx4 v0, s[48:51], s64 offen offset:2048 lds\n"
    "v_sub_f32 v158, v158, v8\n"
    "v_exp_f32 v157, v157\n"
    "v_mfma_f32_32x32x16_bf16 a[144:159], a[68:71], v[128:131], a[144:159]\n"
    "s_waitcnt lgkmcnt(3)\n"
    "ds_read_b128 a[88:91], v2 offset:22528\n"
    "v_add_f32 v7, v7, v155\n"
    "v_cvt_pk_bf16_f32 v153, v154, v155\n"
    "v_sub_f32 v159, v159, v8\n"
    "v_exp_f32 v158, v158\n"
    "v_mfma_f32_32x32x16_bf16 a[160:175], a[72:75], v[128:131], a[160:175]\n"
    "s_waitcnt lgkmcnt(3)\n"
    "ds_read_b128 a[92:95], v2 offset:23552\n"
    "buffer_load_dwordx4 v0, s[48:51], s64 offen offset:3072 lds\n"
    "v_add_f32 v7, v7, v156\n"
    "v_exp_f32 v159, v159\n"
    "v_mfma_f32_32x32x16_bf16 a[176:191], a[76:79], v[128:131], a[176:191]\n"
    "s_waitcnt lgkmcnt(3)\n"
    "ds_read_b128 a[64:67], v2 offset:24576\n"
    "v_add_f32 v7, v7, v157\n"
    "v_cvt_pk_bf16_f32 v154, v156, v157\n"
    "v_add_f32 v7, v7, v158\n"
    "v_add_f32 v7, v7, v159\n"
    "v_cvt_pk_bf16_f32 v155, v158, v159\n"
    "v_mfma_f32_32x32x16_bf16 a[192:207], a[80:83], v[128:131], a[192:207]\n"
    "s_waitcnt lgkmcnt(3)\n"
    "ds_read_b128 a[68:71], v2 offset:25600\n"
    "v_sub_f32 v160, v160, v8\n"
    "v_sub_f32 v161, v161, v8\n"
    "v_exp_f32 v160, v160\n"
    "v_sub_f32 v162, v162, v8\n"
    "v_mfma_f32_32x32x16_bf16 a[208:223], a[84:87], v[128:131], a[208:223]\n"
    "s_waitcnt lgkmcnt(3)\n"
    "ds_read_b128 a[72:75], v2 offset:26624\n"
    "v_exp_f32 v161, v161\n"
    "v_sub_f32 v163, v163, v8\n"
    "v_exp_f32 v162, v162\n"
    "v_mfma_f32_32x32x16_bf16 a[224:239], a[88:91], v[128:131], a[224:239]\n"
    "s_waitcnt lgkmcnt(3)\n"
    "ds_read_b128 a[76:79], v2 offset:27648\n"
    "v_add_f32 v7, v7, v160\n"
    "v_sub_f32 v164, v164, v8\n"
    "v_exp_f32 v163, v163\n"
    "v_add_f32 v7, v7, v161\n"
    "v_mfma_f32_32x32x16_bf16 a[240:255], a[92:95], v[128:131], a[240:255]\n"
    "s_waitcnt lgkmcnt(3)\n"
    "ds_read_b128 a[80:83], v2 offset:28672\n"
    "v_cvt_pk_bf16_f32 v160, v160, v161\n"
    "v_sub_f32 v165, v165, v8\n"
    "v_exp_f32 v164, v164\n"
    "v_add_f32 v7, v7, v162\n"
    "v_mfma_f32_32x32x16_bf16 a[128:143], a[64:67], v[136:139], a[128:143]\n"
    "s_waitcnt lgkmcnt(3)\n"
    "ds_read_b128 a[84:87], v2 offset:29696\n"
    "v_sub_f32 v166, v166, v8\n"
    "v_exp_f32 v165, v165\n"
    "v_add_f32 v7, v7, v163\n"
    "v_cvt_pk_bf16_f32 v161, v162, v163\n"
    "v_mfma_f32_32x32x16_bf16 a[144:159], a[68:71], v[136:139], a[144:159]\n"
    "s_waitcnt lgkmcnt(3)\n"
    "ds_read_b128 a[88:91], v2 offset:30720\n"
    "v_sub_f32 v167, v167, v8\n"
    "v_exp_f32 v166, v166\n"
    "v_add_f32 v7, v7, v164\n"
    "v_mfma_f32_32x32x16_bf16 a[160:175], a[72:75], v[136:139], a[160:175]\n"
    "s_waitcnt lgkmcnt(3)\n"
    "ds_read_b128 a[92:95], v2 offset:31744\n"
    "v_exp_f32 v167, v167\n"
    "v_add_f32 v7, v7, v165\n"
    "v_cvt_pk_bf16_f32 v162, v164, v165\n"
    "v_add_f32 v7, v7, v166\n"
    "v_mfma_f32_32x32x16_bf16 a[176:191], a[76:79], v[136:139], a[176:191]\n"
    "s_waitcnt vmcnt(8) lgkmcnt(3)\n"
    "s_barrier\n"
    "ds_read_b128 a[64:67], v2 offset:32768\n"
    "v_add_f32 v7, v7, v167\n"
    "v_cvt_pk_bf16_f32 v163, v166, v167\n"
    "v_sub_f32 v168, v168, v8\n"
    "v_sub_f32 v169, v169, v8\n"
    "v_mfma_f32_32x32x16_bf16 a[192:207], a[80:83], v[136:139], a[192:207]\n"
    "s_waitcnt lgkmcnt(3)\n"
    "ds_read_b128 a[68:71], v2 offset:33792\n"
    "v_exp_f32 v168, v168\n"
    "v_sub_f32 v170, v170, v8\n"
    "v_exp_f32 v169, v169\n"
    "v_mfma_f32_32x32x16_bf16 a[208:223], a[84:87], v[136:139], a[208:223]\n"
    "s_waitcnt lgkmcnt(3)\n"
    "ds_read_b128 a[72:75], v2 offset:34816\n"
    "v_sub_f32 v171, v171, v8\n"
    "v_exp_f32 v170, v170\n"
    "v_add_f32 v7, v7, v168\n"
    "v_sub_f32 v172, v172, v8\n"
    "v_mfma_f32_32x32x16_bf16 a[224:239], a[88:91], v[136:139], a[224:239]\n"
    "s_waitcnt lgkmcnt(3)\n"
    "ds_read_b128 a[76:79], v2 offset:35840\n"
    "v_exp_f32 v171, v171\n"
    "v_add_f32 v7, v7, v169\n"
    "v_cvt_pk_bf16_f32 v168, v168, v169\n"
    "v_sub_f32 v173, v173, v8\n"
    "v_mfma_f32_32x32x16_bf16 a[240:255], a[92:95], v[136:139], a[240:255]\n"
    "s_waitcnt lgkmcnt(3)\n"
    "ds_read_b128 a[80:83], v2 offset:36864\n"
    "v_exp_f32 v172, v172\n"
    "v_add_f32 v7, v7, v170\n"
    "v_sub_f32 v174, v174, v8\n"
    "v_mfma_f32_32x32x16_bf16 a[128:143], a[64:67], v[16:19], a[128:143]\n"
    "s_waitcnt lgkmcnt(3)\n"
    "ds_read_b128 a[84:87], v2 offset:37888\n"
    "v_exp_f32 v173, v173\n"
    "v_add_f32 v7, v7, v171\n"
    "v_cvt_pk_bf16_f32 v169, v170, v171\n"
    "v_sub_f32 v175, v175, v8\n"
    "v_mfma_f32_32x32x16_bf16 a[144:159], a[68:71], v[16:19], a[144:159]\n"
    "s_waitcnt lgkmcnt(3)\n"
    "s_add_u32 m0, s65, 0x8000\n"
    "ds_read_b128 a[88:91], v2 offset:38912\n"
    "v_exp_f32 v174, v174\n"
    "v_add_f32 v7, v7, v172\n"
    "v_mfma_f32_32x32x16_bf16 a[160:175], a[72:75], v[16:19], a[160:175]\n"
    "s_waitcnt lgkmcnt(3)\n"
    "s_add_u32 s64, s67, 0x30000\n"
    "ds_read_b128 a[92:95], v2 offset:39936\n"
    "v_exp_f32 v175, v175\n"
    "v_add_f32 v7, v7, v173\n"
    "v_cvt_pk_bf16_f32 v170, v172, v173\n"
    "v_mfma_f32_32x32x16_bf16 a[176:191], a[76:79], v[16:19], a[176:191]\n"
    "s_waitcnt lgkmcnt(3)\n"
    "ds_read_b128 a[64:67], v2 offset:40960\n"
    "buffer_load_dwordx4 v0, s[48:51], s64 offen offset:0 lds\n"
    "v_add_f32 v7, v7, v174\n"
    "v_add_f32 v7, v7, v175\n"
    "v_cvt_pk_bf16_f32 v171, v174, v175\n"
    "v_mfma_f32_32x32x16_bf16 a[192:207], a[80:83], v[16:19], a[192:207]\n"
    "s_waitcnt lgkmcnt(3)\n"
    "ds_read_b128 a[68:71], v2 offset:41984\n"
    "v_sub_f32 v176, v176, v8\n"
    "v_sub_f32 v177, v177, v8\n"
    "v_exp_f32 v176, v176\n"
    "v_sub_f32 v178, v178, v8\n"
    "v_mfma_f32_32x32x16_bf16 a[208:223], a[84:87], v[16:19], a[208:223]\n"
    "s_waitcnt lgkmcnt(3)\n"
    "ds_read_b128 a[72:75], v2 offset:43008\n"
    "buffer_load_dwordx4 v0, s[48:51], s64 offen offset:1024 lds\n"
    "v_exp_f32 v177, v177\n"
    "v_sub_f32 v179, v179, v8\n"
    "v_mfma_f32_32x32x16_bf16 a[224:239], a[88:91], v[16:19], a[224:239]\n"
    "s_waitcnt lgkmcnt(3)\n"
    "ds_read_b128 a[76:79], v2 offset:44032\n"
    "v_exp_f32 v178, v178\n"
    "v_add_f32 v7, v7, v176\n"
    "v_sub_f32 v180, v180, v8\n"
    "v_mfma_f32_32x32x16_bf16 a[240:255], a[92:95], v[16:19], a[240:255]\n"
    "s_waitcnt lgkmcnt(3)\n"
    "ds_read_b128 a[80:83], v2 offset:45056\n"
    "buffer_load_dwordx4 v0, s[48:51], s64 offen offset:2048 lds\n"
    "v_exp_f32 v179, v179\n"
    "v_add_f32 v7, v7, v177\n"
    "v_mfma_f32_32x32x16_bf16 a[128:143], a[64:67], v[24:27], a[128:143]\n"
    "s_waitcnt lgkmcnt(3)\n"
    "ds_read_b128 a[84:87], v2 offset:46080\n"
    "v_cvt_pk_bf16_f32 v176, v176, v177\n"
    "v_sub_f32 v181, v181, v8\n"
    "v_exp_f32 v180, v180\n"
    "v_add_f32 v7, v7, v178\n"
    "v_mfma_f32_32x32x16_bf16 a[144:159], a[68:71], v[24:27], a[144:159]\n"
    "s_waitcnt lgkmcnt(3)\n"
    "ds_read_b128 a[88:91], v2 offset:47104\n"
    "buffer_load_dwordx4 v0, s[48:51], s64 offen offset:3072 lds\n"
    "v_sub_f32 v182, v182, v8\n"
    "v_exp_f32 v181, v181\n"
    "v_mfma_f32_32x32x16_bf16 a[160:175], a[72:75], v[24:27], a[160:175]\n"
    "s_waitcnt lgkmcnt(3)\n"
    "s_add_u32 m0, s65, 0xc000\n"
    "ds_read_b128 a[92:95], v2 offset:48128\n"
    "v_add_f32 v7, v7, v179\n"
    "v_cvt_pk_bf16_f32 v177, v178, v179\n"
    "v_sub_f32 v183, v183, v8\n"
    "v_mfma_f32_32x32x16_bf16 a[176:191], a[76:79], v[24:27], a[176:191]\n"
    "s_waitcnt lgkmcnt(3)\n"
    "s_add_u32 s64, s67, 0x34000\n"
    "ds_read_b128 a[64:67], v2 offset:49152\n"
    "v_exp_f32 v182, v182\n"
    "v_add_f32 v7, v7, v180\n"
    "v_mfma_f32_32x32x16_bf16 a[192:207], a[80:83], v[24:27], a[192:207]\n"
    "s_waitcnt lgkmcnt(3)\n"
    "ds_read_b128 a[68:71], v2 offset:50176\n"
    "buffer_load_dwordx4 v0, s[48:51], s64 offen offset:0 lds\n"
    "v_exp_f32 v183, v183\n"
    "v_add_f32 v7, v7, v181\n"
    "v_mfma_f32_32x32x16_bf16 a[208:223], a[84:87], v[24:27], a[208:223]\n"
    "s_waitcnt lgkmcnt(3)\n"
    "ds_read_b128 a[72:75], v2 offset:51200\n"
    "v_cvt_pk_bf16_f32 v178, v180, v181\n"
    "v_add_f32 v7, v7, v182\n"
    "v_add_f32 v7, v7, v183\n"
    "v_cvt_pk_bf16_f32 v179, v182, v183\n"
    "v_sub_f32 v184, v184, v8\n"
    "v_mfma_f32_32x32x16_bf16 a[224:239], a[88:91], v[24:27], a[224:239]\n"
    "s_waitcnt lgkmcnt(3)\n"
    "ds_read_b128 a[76:79], v2 offset:52224\n"
    "buffer_load_dwordx4 v0, s[48:51], s64 offen offset:1024 lds\n"
    "v_sub_f32 v185, v185, v8\n"
    "v_exp_f32 v184, v184\n"
    "v_mfma_f32_32x32x16_bf16 a[240:255], a[92:95], v[24:27], a[240:255]\n"
    "s_waitcnt lgkmcnt(3)\n"
    "ds_read_b128 a[80:83], v2 offset:53248\n"
    "v_sub_f32 v186, v186, v8\n"
    "v_exp_f32 v185, v185\n"
    "v_sub_f32 v187, v187, v8\n"
    "v_mfma_f32_32x32x16_bf16 a[128:143], a[64:67], v[144:147], a[128:143]\n"
    "s_waitcnt lgkmcnt(3)\n"
    "ds_read_b128 a[84:87], v2 offset:54272\n"
    "buffer_load_dwordx4 v0, s[48:51], s64 offen offset:2048 lds\n"
    "v_exp_f32 v186, v186\n"
    "v_add_f32 v7, v7, v184\n"
    "v_mfma_f32_32x32x16_bf16 a[144:159], a[68:71], v[144:147], a[144:159]\n"
    "s_waitcnt lgkmcnt(3)\n"
    "ds_read_b128 a[88:91], v2 offset:55296\n"
    "v_sub_f32 v188, v188, v8\n"
    "v_exp_f32 v187, v187\n"
    "v_add_f32 v7, v7, v185\n"
    "v_cvt_pk_bf16_f32 v184, v184, v185\n"
    "v_mfma_f32_32x32x16_bf16 a[160:175], a[72:75], v[144:147], a[160:175]\n"
    "s_waitcnt lgkmcnt(3)\n"
    "ds_read_b128 a[92:95], v2 offset:56320\n"
    "buffer_load_dwordx4 v0, s[48:51], s64 offen offset:3072 lds\n"
    "v_sub_f32 v189, v189, v8\n"
    "v_exp_f32 v188, v188\n"
    "v_mfma_f32_32x32x16_bf16 a[176:191], a[76:79], v[144:147], a[176:191]\n"
    "s_waitcnt lgkmcnt(3)\n"
    "ds_read_b128 a[64:67], v2 offset:57344\n"
    "v_add_f32 v7, v7, v186\n"
    "v_sub_f32 v190, v190, v8\n"
    "v_exp_f32 v189, v189\n"
    "v_add_f32 v7, v7, v187\n"
    "v_mfma_f32_32x32x16_bf16 a[192:207], a[80:83], v[144:147], a[192:207]\n"
    "s_waitcnt lgkmcnt(3)\n"
    "ds_read_b128 a[68:71], v2 offset:58368\n"
    "v_cvt_pk_bf16_f32 v185, v186, v187\n"
    "v_sub_f32 v191, v191, v8\n"
    "v_exp_f32 v190, v190\n"
    "v_add_f32 v7, v7, v188\n"
    "v_mfma_f32_32x32x16_bf16 a[208:223], a[84:87], v[144:147], a[208:223]\n"
    "s_waitcnt lgkmcnt(3)\n"
    "ds_read_b128 a[72:75], v2 offset:59392\n"
    "v_exp_f32 v191, v191\n"
    "v_add_f32 v7, v7, v189\n"
    "v_cvt_pk_bf16_f32 v186, v188, v189\n"
    "v_add_f32 v7, v7, v190\n"
    "v_mfma_f32_32x32x16_bf16 a[224:239], a[88:91], v[144:147], a[224:239]\n"
    "s_waitcnt lgkmcnt(3)\n"
    "ds_read_b128 a[76:79], v2 offset:60416\n"
    "v_add_f32 v7, v7, v191\n"
    "v_cvt_pk_bf16_f32 v187, v190, v191\n"
    "v_sub_f32 v192, v192, v8\n"
    "v_sub_f32 v193, v193, v8\n"
    "v_mfma_f32_32x32x16_bf16 a[240:255], a[92:95], v[144:147], a[240:255]\n"
    "s_waitcnt lgkmcnt(3)\n"
    "ds_read_b128 a[80:83], v2 offset:61440\n"
    "v_exp_f32 v192, v192\n"
    "v_sub_f32 v194, v194, v8\n"
    "v_exp_f32 v193, v193\n"
    "v_mfma_f32_32x32x16_bf16 a[128:143], a[64:67], v[152:155], a[128:143]\n"
    "s_waitcnt lgkmcnt(3)\n"
    "ds_read_b128 a[84:87], v2 offset:62464\n"
    "v_sub_f32 v195, v195, v8\n"
    "v_exp_f32 v194, v194\n"
    "v_add_f32 v7, v7, v192\n"
    "v_sub_f32 v196, v196, v8\n"
    "v_mfma_f32_32x32x16_bf16 a[144:159], a[68:71], v[152:155], a[144:159]\n"
    "s_waitcnt lgkmcnt(3)\n"
    "ds_read_b128 a[88:91], v2 offset:63488\n"
    "v_exp_f32 v195, v195\n"
    "v_add_f32 v7, v7, v193\n"
    "v_cvt_pk_bf16_f32 v192, v192, v193\n"
    "v_sub_f32 v197, v197, v8\n"
    "v_mfma_f32_32x32x16_bf16 a[160:175], a[72:75], v[152:155], a[160:175]\n"
    "s_waitcnt lgkmcnt(3)\n"
    "ds_read_b128 a[92:95], v2 offset:64512\n"
    "v_exp_f32 v196, v196\n"
    "v_add_f32 v7, v7, v194\n"
    "v_sub_f32 v198, v198, v8\n"
    "v_mfma_f32_32x32x16_bf16 a[176:191], a[76:79], v[152:155], a[176:191]\n"
    "s_waitcnt vmcnt(8) lgkmcnt(3)\n"
    "s_barrier\n"
    "ds_read_b128 a[64:67], v1 offset:0\n"
    "v_exp_f32 v197, v197\n"
    "v_add_f32 v7, v7, v195\n"
    "v_cvt_pk_bf16_f32 v193, v194, v195\n"
    "v_mfma_f32_32x32x16_bf16 a[192:207], a[80:83], v[152:155], a[192:207]\n"
    "s_waitcnt lgkmcnt(3)\n"
    "ds_read_b128 a[68:71], v1 offset:1024\n"
    "v_sub_f32 v199, v199, v8\n"
    "v_exp_f32 v198, v198\n"
    "v_add_f32 v7, v7, v196\n"
    "v_mfma_f32_32x32x16_bf16 a[208:223], a[84:87], v[152:155], a[208:223]\n"
    "s_waitcnt lgkmcnt(3)\n"
    "ds_read_b128 a[72:75], v1 offset:2048\n"
    "v_exp_f32 v199, v199\n"
    "v_add_f32 v7, v7, v197\n"
    "v_cvt_pk_bf16_f32 v194, v196, v197\n"
    "v_add_f32 v7, v7, v198\n"
    "v_mfma_f32_32x32x16_bf16 a[224:239], a[88:91], v[152:155], a[224:239]\n"
    "s_waitcnt lgkmcnt(3)\n"
    "ds_read_b128 a[76:79], v1 offset:3072\n"
    "v_add_f32 v7, v7, v199\n"
    "v_cvt_pk_bf16_f32 v195, v198, v199\n"
    "v_sub_f32 v200, v200, v8\n"
    "v_sub_f32 v201, v201, v8\n"
    "v_mfma_f32_32x32x16_bf16 a[240:255], a[92:95], v[152:155], a[240:255]\n"
    "s_waitcnt lgkmcnt(3)\n"
    "ds_read_b128 a[80:83], v1 offset:4096\n"
    "v_exp_f32 v200, v200\n"
    "v_sub_f32 v202, v202, v8\n"
    "v_exp_f32 v201, v201\n"
    "v_mfma_f32_32x32x16_bf16 a[128:143], a[64:67], v[160:163], a[128:143]\n"
    "s_waitcnt lgkmcnt(3)\n"
    "ds_read_b128 a[84:87], v1 offset:5120\n"
    "v_sub_f32 v203, v203, v8\n"
    "v_exp_f32 v202, v202\n"
    "v_add_f32 v7, v7, v200\n"
    "v_sub_f32 v204, v204, v8\n"
    "v_mfma_f32_32x32x16_bf16 a[144:159], a[68:71], v[160:163], a[144:159]\n"
    "s_waitcnt lgkmcnt(3)\n"
    "s_add_u32 m0, s65, 0x10000\n"
    "ds_read_b128 a[88:91], v1 offset:6144\n"
    "v_exp_f32 v203, v203\n"
    "v_add_f32 v7, v7, v201\n"
    "v_cvt_pk_bf16_f32 v200, v200, v201\n"
    "v_mfma_f32_32x32x16_bf16 a[160:175], a[72:75], v[160:163], a[160:175]\n"
    "s_waitcnt lgkmcnt(3)\n"
    "s_add_u32 s64, s67, 0x38000\n"
    "ds_read_b128 a[92:95], v1 offset:7168\n"
    "v_sub_f32 v205, v205, v8\n"
    "v_exp_f32 v204, v204\n"
    "v_add_f32 v7, v7, v202\n"
    "v_mfma_f32_32x32x16_bf16 a[176:191], a[76:79], v[160:163], a[176:191]\n"
    "s_waitcnt lgkmcnt(3)\n"
    "ds_read_b128 a[64:67], v1 offset:8192\n"
    "buffer_load_dwordx4 v0, s[48:51], s64 offen offset:0 lds\n"
    "v_sub_f32 v206, v206, v8\n"
    "v_exp_f32 v205, v205\n"
    "v_mfma_f32_32x32x16_bf16 a[192:207], a[80:83], v[160:163], a[192:207]\n"
    "s_waitcnt lgkmcnt(3)\n"
    "ds_read_b128 a[68:71], v1 offset:9216\n"
    "v_add_f32 v7, v7, v203\n"
    "v_cvt_pk_bf16_f32 v201, v202, v203\n"
    "v_sub_f32 v207, v207, v8\n"
    "v_exp_f32 v206, v206\n"
    "v_mfma_f32_32x32x16_bf16 a[208:223], a[84:87], v[160:163], a[208:223]\n"
    "s_waitcnt lgkmcnt(3)\n"
    "ds_read_b128 a[72:75], v1 offset:10240\n"
    "buffer_load_dwordx4 v0, s[48:51], s64 offen offset:1024 lds\n"
    "v_add_f32 v7, v7, v204\n"
    "v_exp_f32 v207, v207\n"
    "v_mfma_f32_32x32x16_bf16 a[224:239], a[88:91], v[160:163], a[224:239]\n"
    "s_waitcnt lgkmcnt(3)\n"
    "ds_read_b128 a[76:79], v1 offset:11264\n"
    "v_add_f32 v7, v7, v205\n"
    "v_cvt_pk_bf16_f32 v202, v204, v205\n"
    "v_add_f32 v7, v7, v206\n"
    "v_add_f32 v7, v7, v207\n"
    "v_cvt_pk_bf16_f32 v203, v206, v207\n"
    "v_mfma_f32_32x32x16_bf16 a[240:255], a[92:95], v[160:163], a[240:255]\n"
    "s_waitcnt lgkmcnt(3)\n"
    "ds_read_b128 a[80:83], v1 offset:12288\n"
    "buffer_load_dwordx4 v0, s[48:51], s64 offen offset:2048 lds\n"
    "v_sub_f32 v240, v240, v8\n"
    "v_sub_f32 v241, v241, v8\n"
    "v_mfma_f32_32x32x16_bf16 a[128:143], a[64:67], v[168:171], a[128:143]\n"
    "s_waitcnt lgkmcnt(3)\n"
    "ds_read_b128 a[84:87], v1 offset:13312\n"
    "v_exp_f32 v240, v240\n"
    "v_sub_f32 v242, v242, v8\n"
    "v_exp_f32 v241, v241\n"
    "v_mfma_f32_32x32x16_bf16 a[144:159], a[68:71], v[168:171], a[144:159]\n"
    "s_waitcnt lgkmcnt(3)\n"
    "ds_read_b128 a[88:91], v1 offset:14336\n"
    "buffer_load_dwordx4 v0, s[48:51], s64 offen offset:3072 lds\n"
    "v_sub_f32 v243, v243, v8\n"
    "v_exp_f32 v242, v242\n"
    "v_mfma_f32_32x32x16_bf16 a[160:175], a[72:75], v[168:171], a[160:175]\n"
    "s_waitcnt lgkmcnt(3)\n"
    "s_add_u32 m0, s65, 0x14000\n"
    "ds_read_b128 a[92:95], v1 offset:15360\n"
    "v_add_f32 v7, v7, v240\n"
    "v_sub_f32 v244, v244, v8\n"
    "v_exp_f32 v243, v243\n"
    "v_mfma_f32_32x32x16_bf16 a[176:191], a[76:79], v[168:171], a[176:191]\n"
    "s_waitcnt lgkmcnt(3)\n"
    "s_add_u32 s64, s67, 0x3c000\n"
    "ds_read_b128 a[64:67], v1 offset:16384\n"
    "v_add_f32 v7, v7, v241\n"
    "v_cvt_pk_bf16_f32 v240, v240, v241\n"
    "v_sub_f32 v245, v245, v8\n"
    "v_mfma_f32_32x32x16_bf16 a[192:207], a[80:83], v[168:171], a[192:207]\n"
    "s_waitcnt lgkmcnt(3)\n"
    "ds_read_b128 a[68:71], v1 offset:17408\n"
    "buffer_load_dwordx4 v0, s[48:51], s64 offen offset:0 lds\n"
    "v_exp_f32 v244, v244\n"
    "v_add_f32 v7, v7, v242\n"
    "v_mfma_f32_32x32x16_bf16 a[208:223], a[84:87], v[168:171], a[208:223]\n"
    "s_waitcnt lgkmcnt(3)\n"
    "ds_read_b128 a[72:75], v1 offset:18432\n"
    "v_sub_f32 v246, v246, v8\n"
    "v_exp_f32 v245, v245\n"
    "v_add_f32 v7, v7, v243\n"
    "v_cvt_pk_bf16_f32 v241, v242, v243\n"
    "v_mfma_f32_32x32x16_bf16 a[224:239], a[88:91], v[168:171], a[224:239]\n"
    "s_waitcnt lgkmcnt(3)\n"
    "ds_read_b128 a[76:79], v1 offset:19456\n"
    "buffer_load_dwordx4 v0, s[48:51], s64 offen offset:1024 lds\n"
    "v_sub_f32 v247, v247, v8\n"
    "v_exp_f32 v246, v246\n"
    "v_mfma_f32_32x32x16_bf16 a[240:255], a[92:95], v[168:171], a[240:255]\n"
    "s_waitcnt lgkmcnt(3)\n"
    "ds_read_b128 a[80:83], v1 offset:20480\n"
    "v_add_f32 v7, v7, v244\n"
    "v_exp_f32 v247, v247\n"
    "v_add_f32 v7, v7, v245\n"
    "v_cvt_pk_bf16_f32 v242, v244, v245\n"
    "v_mfma_f32_32x32x16_bf16 a[128:143], a[64:67], v[176:179], a[128:143]\n"
    "s_waitcnt lgkmcnt(3)\n"
    "ds_read_b128 a[84:87], v1 offset:21504\n"
    "buffer_load_dwordx4 v0, s[48:51], s64 offen offset:2048 lds\n"
    "v_add_f32 v7, v7, v246\n"
    "v_add_f32 v7, v7, v247\n"
    "v_cvt_pk_bf16_f32 v243, v246, v247\n"
    "v_mfma_f32_32x32x16_bf16 a[144:159], a[68:71], v[176:179], a[144:159]\n"
    "s_waitcnt lgkmcnt(3)\n"
    "ds_read_b128 a[88:91], v1 offset:22528\n"
    "v_sub_f32 v248, v248, v8\n"
    "v_sub_f32 v249, v249, v8\n"
    "v_exp_f32 v248, v248\n"
    "v_sub_f32 v250, v250, v8\n"
    "v_mfma_f32_32x32x16_bf16 a[160:175], a[72:75], v[176:179], a[160:175]\n"
    "s_waitcnt lgkmcnt(3)\n"
    "ds_read_b128 a[92:95], v1 offset:23552\n"
    "buffer_load_dwordx4 v0, s[48:51], s64 offen offset:3072 lds\n"
    "v_exp_f32 v249, v249\n"
    "v_sub_f32 v251, v251, v8\n"
    "v_mfma_f32_32x32x16_bf16 a[176:191], a[76:79], v[176:179], a[176:191]\n"
    "s_waitcnt lgkmcnt(3)\n"
    "ds_read_b128 a[64:67], v1 offset:24576\n"
    "v_exp_f32 v250, v250\n"
    "v_add_f32 v7, v7, v248\n"
    "v_sub_f32 v252, v252, v8\n"
    "v_mfma_f32_32x32x16_bf16 a[192:207], a[80:83], v[176:179], a[192:207]\n"
    "s_waitcnt lgkmcnt(3)\n"
    "ds_read_b128 a[68:71], v1 offset:25600\n"
    "v_exp_f32 v251, v251\n"
    "v_add_f32 v7, v7, v249\n"
    "v_cvt_pk_bf16_f32 v248, v248, v249\n"
    "v_sub_f32 v253, v253, v8\n"
    "v_mfma_f32_32x32x16_bf16 a[208:223], a[84:87], v[176:179], a[208:223]\n"
    "s_waitcnt lgkmcnt(3)\n"
    "ds_read_b128 a[72:75], v1 offset:26624\n"
    "v_exp_f32 v252, v252\n"
    "v_add_f32 v7, v7, v250\n"
    "v_sub_f32 v254, v254, v8\n"
    "v_mfma_f32_32x32x16_bf16 a[224:239], a[88:91], v[176:179], a[224:239]\n"
    "s_waitcnt lgkmcnt(3)\n"
    "ds_read_b128 a[76:79], v1 offset:27648\n"
    "v_exp_f32 v253, v253\n"
    "v_add_f32 v7, v7, v251\n"
    "v_cvt_pk_bf16_f32 v249, v250, v251\n"
    "v_sub_f32 v255, v255, v8\n"
    "v_mfma_f32_32x32x16_bf16 a[240:255], a[92:95], v[176:179], a[240:255]\n"
    "s_waitcnt lgkmcnt(3)\n"
    "ds_read_b128 a[80:83], v1 offset:28672\n"
    "v_exp_f32 v254, v254\n"
    "v_add_f32 v7, v7, v252\n"
    "v_exp_f32 v255, v255\n"
    "v_mfma_f32_32x32x16_bf16 a[128:143], a[64:67], v[184:187], a[128:143]\n"
    "s_waitcnt lgkmcnt(3)\n"
    "ds_read_b128 a[84:87], v1 offset:29696\n"
    "v_add_f32 v7, v7, v253\n"
    "v_cvt_pk_bf16_f32 v250, v252, v253\n"
    "v_add_f32 v7, v7, v254\n"
    "v_add_f32 v7, v7, v255\n"
    "v_cvt_pk_bf16_f32 v251, v254, v255\n"
    "v_mfma_f32_32x32x16_bf16 a[144:159], a[68:71], v[184:187], a[144:159]\n"
    "s_waitcnt lgkmcnt(3)\n"
    "ds_read_b128 a[88:91], v1 offset:30720\n"
    "v_sub_f32 v208, v208, v8\n"
    "v_sub_f32 v209, v209, v8\n"
    "v_exp_f32 v208, v208\n"
    "v_sub_f32 v210, v210, v8\n"
    "v_mfma_f32_32x32x16_bf16 a[160:175], a[72:75], v[184:187], a[160:175]\n"
    "s_waitcnt lgkmcnt(3)\n"
    "ds_read_b128 a[92:95], v1 offset:31744\n"
    "v_exp_f32 v209, v209\n"
    "v_sub_f32 v211, v211, v8\n"
    "v_exp_f32 v210, v210\n"
    "v_mfma_f32_32x32x16_bf16 a[176:191], a[76:79], v[184:187], a[176:191]\n"
    "s_waitcnt vmcnt(8) lgkmcnt(3)\n"
    "s_barrier\n"
    "ds_read_b128 a[64:67], v1 offset:32768\n"
    "v_add_f32 v7, v7, v208\n"
    "v_sub_f32 v212, v212, v8\n"
    "v_exp_f32 v211, v211\n"
    "v_mfma_f32_32x32x16_bf16 a[192:207], a[80:83], v[184:187], a[192:207]\n"
    "s_waitcnt lgkmcnt(3)\n"
    "ds_read_b128 a[68:71], v1 offset:33792\n"
    "v_add_f32 v7, v7, v209\n"
    "v_cvt_pk_bf16_f32 v208, v208, v209\n"
    "v_sub_f32 v213, v213, v8\n"
    "v_exp_f32 v212, v212\n"
    "v_mfma_f32_32x32x16_bf16 a[208:223], a[84:87], v[184:187], a[208:223]\n"
    "s_waitcnt lgkmcnt(3)\n"
    "ds_read_b128 a[72:75], v1 offset:34816\n"
    "v_add_f32 v7, v7, v210\n"
    "v_sub_f32 v214, v214, v8\n"
    "v_exp_f32 v213, v213\n"
    "v_add_f32 v7, v7, v211\n"
    "v_mfma_f32_32x32x16_bf16 a[224:239], a[88:91], v[184:187], a[224:239]\n"
    "s_waitcnt lgkmcnt(3)\n"
    "ds_read_b128 a[76:79], v1 offset:35840\n"
    "v_cvt_pk_bf16_f32 v209, v210, v211\n"
    "v_sub_f32 v215, v215, v8\n"
    "v_exp_f32 v214, v214\n"
    "v_add_f32 v7, v7, v212\n"
    "v_mfma_f32_32x32x16_bf16 a[240:255], a[92:95], v[184:187], a[240:255]\n"
    "s_waitcnt lgkmcnt(3)\n"
    "ds_read_b128 a[80:83], v1 offset:36864\n"
    "v_exp_f32 v215, v215\n"
    "v_add_f32 v7, v7, v213\n"
    "v_cvt_pk_bf16_f32 v210, v212, v213\n"
    "v_add_f32 v7, v7, v214\n"
    "v_mfma_f32_32x32x16_bf16 a[128:143], a[64:67], v[192:195], a[128:143]\n"
    "s_waitcnt lgkmcnt(3)\n"
    "ds_read_b128 a[84:87], v1 offset:37888\n"
    "v_add_f32 v7, v7, v215\n"
    "v_cvt_pk_bf16_f32 v211, v214, v215\n"
    "v_sub_f32 v216, v216, v8\n"
    "v_sub_f32 v217, v217, v8\n"
    "v_mfma_f32_32x32x16_bf16 a[144:159], a[68:71], v[192:195], a[144:159]\n"
    "s_waitcnt lgkmcnt(3)\n"
    "ds_read_b128 a[88:91], v1 offset:38912\n"
    "v_exp_f32 v216, v216\n"
    "v_sub_f32 v218, v218, v8\n"
    "v_exp_f32 v217, v217\n"
    "v_mfma_f32_32x32x16_bf16 a[160:175], a[72:75], v[192:195], a[160:175]\n"
    "s_waitcnt lgkmcnt(3)\n"
    "s_add_u32 m0, s65, 0x18000\n"
    "ds_read_b128 a[92:95], v1 offset:39936\n"
    "v_sub_f32 v219, v219, v8\n"
    "v_exp_f32 v218, v218\n"
    "v_add_f32 v7, v7, v216\n"
    "v_mfma_f32_32x32x16_bf16 a[176:191], a[76:79], v[192:195], a[176:191]\n"
    "s_waitcnt lgkmcnt(3)\n"
    "s_add_u32 s64, s68, 0x0\n"
    "ds_read_b128 a[64:67], v1 offset:40960\n"
    "v_sub_f32 v220, v220, v8\n"
    "v_exp_f32 v219, v219\n"
    "v_add_f32 v7, v7, v217\n"
    "v_mfma_f32_32x32x16_bf16 a[192:207], a[80:83], v[192:195], a[192:207]\n"
    "s_waitcnt lgkmcnt(3)\n"
    "ds_read_b128 a[68:71], v1 offset:41984\n"
    "buffer_load_dwordx4 v11, s[52:55], s64 offen offset:0 lds\n"
    "v_cvt_pk_bf16_f32 v216, v216, v217\n"
    "v_sub_f32 v221, v221, v8\n"
    "v_mfma_f32_32x32x16_bf16 a[208:223], a[84:87], v[192:195], a[208:223]\n"
    "s_waitcnt lgkmcnt(3)\n"
    "ds_read_b128 a[72:75], v1 offset:43008\n"
    "v_exp_f32 v220, v220\n"
    "v_add_f32 v7, v7, v218\n"
    "v_sub_f32 v222, v222, v8\n"
    "v_mfma_f32_32x32x16_bf16 a[224:239], a[88:91], v[192:195], a[224:239]\n"
    "s_waitcnt lgkmcnt(3)\n"
    "ds_read_b128 a[76:79], v1 offset:44032\n"
    "buffer_load_dwordx4 v12, s[52:55], s64 offen offset:1024 lds\n"
    "v_exp_f32 v221, v221\n"
    "v_add_f32 v7, v7, v219\n"
    "v_mfma_f32_32x32x16_bf16 a[240:255], a[92:95], v[192:195], a[240:255]\n"
    "s_waitcnt lgkmcnt(3)\n"
    "ds_read_b128 a[80:83], v1 offset:45056\n"
    "v_cvt_pk_bf16_f32 v217, v218, v219\n"
    "v_sub_f32 v223, v223, v8\n"
    "v_exp_f32 v222, v222\n"
    "v_add_f32 v7, v7, v220\n"
    "v_mfma_f32_32x32x16_bf16 a[128:143], a[64:67], v[200:203], a[128:143]\n"
    "s_waitcnt lgkmcnt(3)\n"
    "ds_read_b128 a[84:87], v1 offset:46080\n"
    "buffer_load_dwordx4 v13, s[52:55], s64 offen offset:2048 lds\n"
    "v_exp_f32 v223, v223\n"
    "v_add_f32 v7, v7, v221\n"
    "v_mfma_f32_32x32x16_bf16 a[144:159], a[68:71], v[200:203], a[144:159]\n"
    "s_waitcnt lgkmcnt(3)\n"
    "ds_read_b128 a[88:91], v1 offset:47104\n"
    "v_cvt_pk_bf16_f32 v218, v220, v221\n"
    "v_add_f32 v7, v7, v222\n"
    "v_add_f32 v7, v7, v223\n"
    "v_cvt_pk_bf16_f32 v219, v222, v223\n"
    "v_sub_f32 v224, v224, v8\n"
    "v_mfma_f32_32x32x16_bf16 a[160:175], a[72:75], v[200:203], a[160:175]\n"
    "s_waitcnt lgkmcnt(3)\n"
    "ds_read_b128 a[92:95], v1 offset:48128\n"
    "buffer_load_dwordx4 v14, s[52:55], s64 offen offset:3072 lds\n"
    "v_sub_f32 v225, v225, v8\n"
    "v_exp_f32 v224, v224\n"
    "v_mfma_f32_32x32x16_bf16 a[176:191], a[76:79], v[200:203], a[176:191]\n"
    "s_waitcnt lgkmcnt(3)\n"
    "s_add_u32 m0, s65, 0x1c000\n"
    "ds_read_b128 a[64:67], v1 offset:49152\n"
    "v_sub_f32 v226, v226, v8\n"
    "v_exp_f32 v225, v225\n"
    "v_sub_f32 v227, v227, v8\n"
    "v_mfma_f32_32x32x16_bf16 a[192:207], a[80:83], v[200:203], a[192:207]\n"
    "s_waitcnt lgkmcnt(3)\n"
    "s_add_u32 s64, s68, 0x1c000\n"
    "ds_read_b128 a[68:71], v1 offset:50176\n"
    "v_exp_f32 v226, v226\n"
    "v_add_f32 v7, v7, v224\n"
    "v_sub_f32 v228, v228, v8\n"
    "v_mfma_f32_32x32x16_bf16 a[208:223], a[84:87], v[200:203], a[208:223]\n"
    "s_waitcnt lgkmcnt(3)\n"
    "ds_read_b128 a[72:75], v1 offset:51200\n"
    "buffer_load_dwordx4 v11, s[52:55], s64 offen offset:0 lds\n"
    "v_exp_f32 v227, v227\n"
    "v_add_f32 v7, v7, v225\n"
    "v_mfma_f32_32x32x16_bf16 a[224:239], a[88:91], v[200:203], a[224:239]\n"
    "s_waitcnt lgkmcnt(3)\n"
    "ds_read_b128 a[76:79], v1 offset:52224\n"
    "v_cvt_pk_bf16_f32 v224, v224, v225\n"
    "v_sub_f32 v229, v229, v8\n"
    "v_exp_f32 v228, v228\n"
    "v_add_f32 v7, v7, v226\n"
    "v_mfma_f32_32x32x16_bf16 a[240:255], a[92:95], v[200:203], a[240:255]\n"
    "s_waitcnt lgkmcnt(3)\n"
    "ds_read_b128 a[80:83], v1 offset:53248\n"
    "buffer_load_dwordx4 v12, s[52:55], s64 offen offset:1024 lds\n"
    "v_sub_f32 v230, v230, v8\n"
    "v_exp_f32 v229, v229\n"
    "v_mfma_f32_32x32x16_bf16 a[128:143], a[64:67], v[240:243], a[128:143]\n"
    "s_waitcnt lgkmcnt(3)\n"
    "ds_read_b128 a[84:87], v1 offset:54272\n"
    "v_add_f32 v7, v7, v227\n"
    "v_cvt_pk_bf16_f32 v225, v226, v227\n"
    "v_sub_f32 v231, v231, v8\n"
    "v_exp_f32 v230, v230\n"
    "v_mfma_f32_32x32x16_bf16 a[144:159], a[68:71], v[240:243], a[144:159]\n"
    "s_waitcnt lgkmcnt(3)\n"
    "ds_read_b128 a[88:91], v1 offset:55296\n"
    "buffer_load_dwordx4 v13, s[52:55], s64 offen offset:2048 lds\n"
    "v_add_f32 v7, v7, v228\n"
    "v_exp_f32 v231, v231\n"
    "v_mfma_f32_32x32x16_bf16 a[160:175], a[72:75], v[240:243], a[160:175]\n"
    "s_waitcnt lgkmcnt(3)\n"
    "ds_read_b128 a[92:95], v1 offset:56320\n"
    "v_add_f32 v7, v7, v229\n"
    "v_cvt_pk_bf16_f32 v226, v228, v229\n"
    "v_add_f32 v7, v7, v230\n"
    "v_add_f32 v7, v7, v231\n"
    "v_cvt_pk_bf16_f32 v227, v230, v231\n"
    "v_mfma_f32_32x32x16_bf16 a[176:191], a[76:79], v[240:243], a[176:191]\n"
    "s_waitcnt lgkmcnt(3)\n"
    "ds_read_b128 a[64:67], v1 offset:57344\n"
    "buffer_load_dwordx4 v14, s[52:55], s64 offen offset:3072 lds\n"
    "v_sub_f32 v232, v232, v8\n"
    "v_sub_f32 v233, v233, v8\n"
    "v_mfma_f32_32x32x16_bf16 a[192:207], a[80:83], v[240:243], a[192:207]\n"
    "s_waitcnt lgkmcnt(3)\n"
    "ds_read_b128 a[68:71], v1 offset:58368\n"
    "v_exp_f32 v232, v232\n"
    "v_sub_f32 v234, v234, v8\n"
    "v_exp_f32 v233, v233\n"
    "v_mfma_f32_32x32x16_bf16 a[208:223], a[84:87], v[240:243], a[208:223]\n"
    "s_waitcnt lgkmcnt(3)\n"
    "ds_read_b128 a[72:75], v1 offset:59392\n"
    "v_sub_f32 v235, v235, v8\n"
    "v_exp_f32 v234, v234\n"
    "v_add_f32 v7, v7, v232\n"
    "v_sub_f32 v236, v236, v8\n"
    "v_mfma_f32_32x32x16_bf16 a[224:239], a[88:91], v[240:243], a[224:239]\n"
    "s_waitcnt lgkmcnt(3)\n"
    "ds_read_b128 a[76:79], v1 offset:60416\n"
    "v_exp_f32 v235, v235\n"
    "v_add_f32 v7, v7, v233\n"
    "v_cvt_pk_bf16_f32 v232, v232, v233\n"
    "v_sub_f32 v237, v237, v8\n"
    "v_mfma_f32_32x32x16_bf16 a[240:255], a[92:95], v[240:243], a[240:255]\n"
    "s_waitcnt lgkmcnt(3)\n"
    "ds_read_b128 a[80:83], v1 offset:61440\n"
    "v_exp_f32 v236, v236\n"
    "v_add_f32 v7, v7, v234\n"
    "v_sub_f32 v238, v238, v8\n"
    "v_mfma_f32_32x32x16_bf16 a[128:143], a[64:67], v[248:251], a[128:143]\n"
    "s_waitcnt lgkmcnt(3)\n"
    "ds_read_b128 a[84:87], v1 offset:62464\n"
    "v_exp_f32 v237, v237\n"
    "v_add_f32 v7, v7, v235\n"
    "v_cvt_pk_bf16_f32 v233, v234, v235\n"
    "v_sub_f32 v239, v239, v8\n"
    "v_mfma_f32_32x32x16_bf16 a[144:159], a[68:71], v[248:251], a[144:159]\n"
    "s_waitcnt lgkmcnt(3)\n"
    "ds_read_b128 a[88:91], v1 offset:63488\n"
    "v_exp_f32 v238, v238\n"
    "v_add_f32 v7, v7, v236\n"
    "v_exp_f32 v239, v239\n"
    "v_mfma_f32_32x32x16_bf16 a[160:175], a[72:75], v[248:251], a[160:175]\n"
    "s_waitcnt lgkmcnt(3)\n"
    "ds_read_b128 a[92:95], v1 offset:64512\n"
    "v_add_f32 v7, v7, v237\n"
    "v_cvt_pk_bf16_f32 v234, v236, v237\n"
    "v_add_f32 v7, v7, v238\n"
    "v_add_f32 v7, v7, v239\n"
    "v_cvt_pk_bf16_f32 v235, v238, v239\n"
    "v_mfma_f32_32x32x16_bf16 a[176:191], a[76:79], v[248:251], a[176:191]\n"
    "s_waitcnt vmcnt(8) lgkmcnt(3)\n"
    "s_barrier\n"
    "ds_read_b128 a[64:67], v2 offset:0\n"
    "v_mfma_f32_32x32x16_bf16 a[192:207], a[80:83], v[248:251], a[192:207]\n"
    "s_waitcnt lgkmcnt(3)\n"
    "ds_read_b128 a[68:71], v2 offset:8192\n"
    "v_mfma_f32_32x32x16_bf16 a[208:223], a[84:87], v[248:251], a[208:223]\n"
    "s_waitcnt lgkmcnt(3)\n"
    "ds_read_b128 a[72:75], v2 offset:16384\n"
    "v_mfma_f32_32x32x16_bf16 a[224:239], a[88:91], v[248:251], a[224:239]\n"
    "s_waitcnt lgkmcnt(3)\n"
    "ds_read_b128 a[76:79], v2 offset:24576\n"
    "v_mfma_f32_32x32x16_bf16 a[240:255], a[92:95], v[248:251], a[240:255]\n"
    "s_waitcnt lgkmcnt(3)\n"
    "ds_read_b128 a[80:83], v2 offset:1024\n"
    "v_fmac_f32 v7, v6, v4\n"
    "v_mov_b32 v6, v7\n"
    "s_nop 1\n"
    "v_permlane32_swap_b32 v6, v7\n"
    "v_add_f32 v6, v6, v7\n"
    "v_mfma_f32_32x32x16_bf16 a[128:143], a[64:67], v[208:211], a[128:143]\n"
    "s_waitcnt lgkmcnt(3)\n"
    "ds_read_b128 a[84:87], v2 offset:9216\n"
    "v_rcp_f32 v8, v6\n"
    "v_mfma_f32_32x32x16_bf16 a[128:143], a[68:71], v[216:219], a[128:143]\n"
    "s_waitcnt lgkmcnt(3)\n"
    "ds_read_b128 a[88:91], v2 offset:17408\n"
    "v_accvgpr_read_b32 v240, a112\n"
    "v_accvgpr_read_b32 v241, a113\n"
    "v_accvgpr_read_b32 v242, a114\n"
    "v_accvgpr_read_b32 v243, a115\n"
    "v_mfma_f32_32x32x16_bf16 a[128:143], a[72:75], v[224:227], a[128:143]\n"
    "s_waitcnt lgkmcnt(3)\n"
    "s_add_u32 m0, s65, 0x0\n"
    "ds_read_b128 a[92:95], v2 offset:25600\n"
    "v_mfma_f32_32x32x16_bf16 a[128:143], a[76:79], v[232:235], a[128:143]\n"
    "s_waitcnt lgkmcnt(3)\n"
    "s_add_u32 s64, s68, 0x80\n"
    "ds_read_b128 a[64:67], v2 offset:2048\n"
    "v_mfma_f32_32x32x16_bf16 a[144:159], a[80:83], v[208:211], a[144:159]\n"
    "s_waitcnt lgkmcnt(3)\n"
    "ds_read_b128 a[68:71], v2 offset:10240\n"
    "buffer_load_dwordx4 v11, s[52:55], s64 offen offset:0 lds\n"
    "v_mfma_f32_32x32x16_bf16 a[144:159], a[84:87], v[216:219], a[144:159]\n"
    "s_waitcnt lgkmcnt(3)\n"
    "ds_read_b128 a[72:75], v2 offset:18432\n"
    "v_mfma_f32_32x32x16_bf16 a[144:159], a[88:91], v[224:227], a[144:159]\n"
    "s_waitcnt lgkmcnt(3)\n"
    "ds_read_b128 a[76:79], v2 offset:26624\n"
    "buffer_load_dwordx4 v12, s[52:55], s64 offen offset:1024 lds\n"
    "v_accvgpr_read_b32 v248, a128\n"
    "v_accvgpr_read_b32 v249, a129\n"
    "v_accvgpr_read_b32 v250, a130\n"
    "v_mfma_f32_32x32x16_bf16 a[144:159], a[92:95], v[232:235], a[144:159]\n"
    "s_waitcnt lgkmcnt(3)\n"
    "ds_read_b128 a[80:83], v2 offset:3072\n"
    "v_accvgpr_read_b32 v251, a131\n"
    "v_accvgpr_read_b32 v252, a132\n"
    "v_accvgpr_read_b32 v253, a133\n"
    "v_accvgpr_read_b32 v254, a134\n"
    "v_accvgpr_read_b32 v255, a135\n"
    "v_mfma_f32_32x32x16_bf16 a[160:175], a[64:67], v[208:211], a[160:175]\n"
    "s_waitcnt lgkmcnt(3)\n"
    "ds_read_b128 a[84:87], v2 offset:11264\n"
    "buffer_load_dwordx4 v13, s[52:55], s64 offen offset:2048 lds\n"
    "v_cvt_pk_bf16_f32 v112, v248, v249\n"
    "v_cvt_pk_bf16_f32 v113, v250, v251\n"
    "v_cvt_pk_bf16_f32 v114, v252, v253\n"
    "v_mfma_f32_32x32x16_bf16 a[160:175], a[68:71], v[216:219], a[160:175]\n"
    "s_waitcnt lgkmcnt(3)\n"
    "ds_read_b128 a[88:91], v2 offset:19456\n"
    "v_cvt_pk_bf16_f32 v115, v254, v255\n"
    "v_accvgpr_read_b32 v248, a136\n"
    "v_accvgpr_read_b32 v249, a137\n"
    "v_accvgpr_read_b32 v250, a138\n"
    "v_accvgpr_read_b32 v251, a139\n"
    "v_mfma_f32_32x32x16_bf16 a[160:175], a[72:75], v[224:227], a[160:175]\n"
    "s_waitcnt lgkmcnt(3)\n"
    "ds_read_b128 a[92:95], v2 offset:27648\n"
    "buffer_load_dwordx4 v14, s[52:55], s64 offen offset:3072 lds\n"
    "v_accvgpr_read_b32 v252, a140\n"
    "v_accvgpr_read_b32 v253, a141\n"
    "v_accvgpr_read_b32 v254, a142\n"
    "v_mfma_f32_32x32x16_bf16 a[160:175], a[76:79], v[232:235], a[160:175]\n"
    "s_waitcnt lgkmcnt(3)\n"
    "s_add_u32 m0, s65, 0x4000\n"
    "ds_read_b128 a[64:67], v2 offset:4096\n"
    "v_accvgpr_read_b32 v255, a143\n"
    "v_cvt_pk_bf16_f32 v116, v248, v249\n"
    "v_cvt_pk_bf16_f32 v117, v250, v251\n"
    "v_cvt_pk_bf16_f32 v118, v252, v253\n"
    "v_mfma_f32_32x32x16_bf16 a[176:191], a[80:83], v[208:211], a[176:191]\n"
    "s_waitcnt lgkmcnt(3)\n"
    "s_add_u32 s64, s68, 0x1c080\n"
    "ds_read_b128 a[68:71], v2 offset:12288\n"
    "v_cvt_pk_bf16_f32 v119, v254, v255\n"
    "v_accvgpr_read_b32 v248, a144\n"
    "v_accvgpr_read_b32 v249, a145\n"
    "v_accvgpr_read_b32 v250, a146\n"
    "v_mfma_f32_32x32x16_bf16 a[176:191], a[84:87], v[216:219], a[176:191]\n"
    "s_waitcnt lgkmcnt(3)\n"
    "ds_read_b128 a[72:75], v2 offset:20480\n"
    "buffer_load_dwordx4 v11, s[52:55], s64 offen offset:0 lds\n"
    "v_accvgpr_read_b32 v251, a147\n"
    "v_accvgpr_read_b32 v252, a148\n"
    "v_accvgpr_read_b32 v253, a149\n"
    "v_mfma_f32_32x32x16_bf16 a[176:191], a[88:91], v[224:227], a[176:191]\n"
    "s_waitcnt lgkmcnt(3)\n"
    "ds_read_b128 a[76:79], v2 offset:28672\n"
    "v_accvgpr_read_b32 v254, a150\n"
    "v_accvgpr_read_b32 v255, a151\n"
    "v_cvt_pk_bf16_f32 v120, v248, v249\n"
    "v_cvt_pk_bf16_f32 v121, v250, v251\n"
    "v_cvt_pk_bf16_f32 v122, v252, v253\n"
    "v_mfma_f32_32x32x16_bf16 a[176:191], a[92:95], v[232:235], a[176:191]\n"
    "s_waitcnt lgkmcnt(3)\n"
    "ds_read_b128 a[80:83], v2 offset:5120\n"
    "buffer_load_dwordx4 v12, s[52:55], s64 offen offset:1024 lds\n"
    "v_cvt_pk_bf16_f32 v123, v254, v255\n"
    "v_accvgpr_read_b32 v248, a152\n"
    "v_accvgpr_read_b32 v249, a153\n"
    "v_mfma_f32_32x32x16_bf16 a[192:207], a[64:67], v[208:211], a[192:207]\n"
    "s_waitcnt lgkmcnt(3)\n"
    "ds_read_b128 a[84:87], v2 offset:13312\n"
    "v_accvgpr_read_b32 v250, a154\n"
    "v_accvgpr_read_b32 v251, a155\n"
    "v_accvgpr_read_b32 v252, a156\n"
    "v_accvgpr_read_b32 v253, a157\n"
    "v_accvgpr_read_b32 v254, a158\n"
    "v_mfma_f32_32x32x16_bf16 a[192:207], a[68:71], v[216:219], a[192:207]\n"
    "s_waitcnt lgkmcnt(3)\n"
    "ds_read_b128 a[88:91], v2 offset:21504\n"
    "buffer_load_dwordx4 v13, s[52:55], s64 offen offset:2048 lds\n"
    "v_accvgpr_read_b32 v255, a159\n"
    "v_cvt_pk_bf16_f32 v124, v248, v249\n"
    "v_cvt_pk_bf16_f32 v125, v250, v251\n"
    "v_cvt_pk_bf16_f32 v126, v252, v253\n"
    "v_cvt_pk_bf16_f32 v127, v254, v255\n"
    "v_mfma_f32_32x32x16_bf16 a[192:207], a[72:75], v[224:227], a[192:207]\n"
    "s_waitcnt lgkmcnt(3)\n"
    "ds_read_b128 a[92:95], v2 offset:29696\n"
    "v_accvgpr_read_b32 v248, a160\n"
    "v_accvgpr_read_b32 v249, a161\n"
    "v_accvgpr_read_b32 v250, a162\n"
    "v_accvgpr_read_b32 v251, a163\n"
    "v_accvgpr_read_b32 v252, a164\n"
    "v_mfma_f32_32x32x16_bf16 a[192:207], a[76:79], v[232:235], a[192:207]\n"
    "s_waitcnt lgkmcnt(3)\n"
    "ds_read_b128 a[64:67], v2 offset:6144\n"
    "buffer_load_dwordx4 v14, s[52:55], s64 offen offset:3072 lds\n"
    "v_accvgpr_read_b32 v253, a165\n"
    "v_accvgpr_read_b32 v254, a166\n"
    "v_accvgpr_read_b32 v255, a167\n"
    "v_mfma_f32_32x32x16_bf16 a[208:223], a[80:83], v[208:211], a[208:223]\n"
    "s_waitcnt lgkmcnt(3)\n"
    "ds_read_b128 a[68:71], v2 offset:14336\n"
    "v_cvt_pk_bf16_f32 v128, v248, v249\n"
    "v_cvt_pk_bf16_f32 v129, v250, v251\n"
    "v_cvt_pk_bf16_f32 v130, v252, v253\n"
    "v_cvt_pk_bf16_f32 v131, v254, v255\n"
    "v_accvgpr_read_b32 v248, a168\n"
    "v_mfma_f32_32x32x16_bf16 a[208:223], a[84:87], v[216:219], a[208:223]\n"
    "s_waitcnt lgkmcnt(3)\n"
    "ds_read_b128 a[72:75], v2 offset:22528\n"
    "v_accvgpr_read_b32 v249, a169\n"
    "v_accvgpr_read_b32 v250, a170\n"
    "v_accvgpr_read_b32 v251, a171\n"
    "v_accvgpr_read_b32 v252, a172\n"
    "v_accvgpr_read_b32 v253, a173\n"
    "v_accvgpr_read_b32 v254, a174\n"
    "v_accvgpr_read_b32 v255, a175\n"
    "v_cvt_pk_bf16_f32 v132, v248, v249\n"
    "v_cvt_pk_bf16_f32 v133, v250, v251\n"
    "v_cvt_pk_bf16_f32 v134, v252, v253\n"
    "v_cvt_pk_bf16_f32 v135, v254, v255\n"
    "v_mfma_f32_32x32x16_bf16 a[208:223], a[88:91], v[224:227], a[208:223]\n"
    "s_waitcnt lgkmcnt(3)\n"
    "ds_read_b128 a[76:79], v2 offset:30720\n"
    "v_accvgpr_read_b32 v248, a176\n"
    "v_accvgpr_read_b32 v249, a177\n"
    "v_accvgpr_read_b32 v250, a178\n"
    "v_accvgpr_read_b32 v251, a179\n"
    "v_accvgpr_read_b32 v252, a180\n"
    "v_mfma_f32_32x32x16_bf16 a[208:223], a[92:95], v[232:235], a[208:223]\n"
    "s_waitcnt lgkmcnt(3)\n"
    "ds_read_b128 a[80:83], v2 offset:7168\n"
    "v_accvgpr_read_b32 v253, a181\n"
    "v_accvgpr_read_b32 v254, a182\n"
    "v_accvgpr_read_b32 v255, a183\n"
    "v_cvt_pk_bf16_f32 v136, v248, v249\n"
    "v_cvt_pk_bf16_f32 v137, v250, v251\n"
    "v_mfma_f32_32x32x16_bf16 a[224:239], a[64:67], v[208:211], a[224:239]\n"
    "s_waitcnt lgkmcnt(3)\n"
    "ds_read_b128 a[84:87], v2 offset:15360\n"
    "v_cvt_pk_bf16_f32 v138, v252, v253\n"
    "v_cvt_pk_bf16_f32 v139, v254, v255\n"
    "v_accvgpr_read_b32 v248, a184\n"
    "v_accvgpr_read_b32 v249, a185\n"
    "v_accvgpr_read_b32 v250, a186\n"
    "v_mfma_f32_32x32x16_bf16 a[224:239], a[68:71], v[216:219], a[224:239]\n"
    "s_waitcnt lgkmcnt(3)\n"
    "ds_read_b128 a[88:91], v2 offset:23552\n"
    "v_accvgpr_read_b32 v251, a187\n"
    "v_accvgpr_read_b32 v252, a188\n"
    "v_accvgpr_read_b32 v253, a189\n"
    "v_accvgpr_read_b32 v254, a190\n"
    "v_accvgpr_read_b32 v255, a191\n"
    "v_cvt_pk_bf16_f32 v140, v248, v249\n"
    "v_cvt_pk_bf16_f32 v141, v250, v251\n"
    "v_cvt_pk_bf16_f32 v142, v252, v253\n"
    "v_cvt_pk_bf16_f32 v143, v254, v255\n"
    "v_mfma_f32_32x32x16_bf16 a[224:239], a[72:75], v[224:227], a[224:239]\n"
    "s_waitcnt lgkmcnt(3)\n"
    "ds_read_b128 a[92:95], v2 offset:31744\n"
    "v_accvgpr_read_b32 v248, a192\n"
    "v_accvgpr_read_b32 v249, a193\n"
    "v_accvgpr_read_b32 v250, a194\n"
    "v_accvgpr_read_b32 v251, a195\n"
    "v_accvgpr_read_b32 v252, a196\n"
    "v_mfma_f32_32x32x16_bf16 a[224:239], a[76:79], v[232:235], a[224:239]\n"
    "s_waitcnt lgkmcnt(3)\n"
    "v_accvgpr_read_b32 v253, a197\n"
    "v_accvgpr_read_b32 v254, a198\n"
    "v_accvgpr_read_b32 v255, a199\n"
    "v_cvt_pk_bf16_f32 v144, v248, v249\n"
    "v_cvt_pk_bf16_f32 v145, v250, v251\n"
    "v_cvt_pk_bf16_f32 v146, v252, v253\n"
    "v_mfma_f32_32x32x16_bf16 a[240:255], a[80:83], v[208:211], a[240:255]\n"
    "s_waitcnt vmcnt(8) lgkmcnt(2)\n"
    "s_barrier\n"
    "ds_read_b64_tr_b16 a[64:65], v242 offset:32768\n"
    "ds_read_b64_tr_b16 a[66:67], v242 offset:33792\n"
    "v_cvt_pk_bf16_f32 v147, v254, v255\n"
    "v_accvgpr_read_b32 v248, a200\n"
    "v_accvgpr_read_b32 v249, a201\n"
    "v_mfma_f32_32x32x16_bf16 a[240:255], a[84:87], v[216:219], a[240:255]\n"
    "s_waitcnt lgkmcnt(3)\n"
    "ds_read_b64_tr_b16 a[68:69], v243 offset:32768\n"
    "ds_read_b64_tr_b16 a[70:71], v243 offset:33792\n"
    "v_accvgpr_read_b32 v250, a202\n"
    "v_accvgpr_read_b32 v251, a203\n"
    "v_accvgpr_read_b32 v252, a204\n"
    "v_accvgpr_read_b32 v253, a205\n"
    "v_accvgpr_read_b32 v254, a206\n"
    "v_accvgpr_read_b32 v255, a207\n"
    "v_cvt_pk_bf16_f32 v148, v248, v249\n"
    "v_cvt_pk_bf16_f32 v149, v250, v251\n"
    "v_cvt_pk_bf16_f32 v150, v252, v253\n"
    "v_cvt_pk_bf16_f32 v151, v254, v255\n"
    "v_mfma_f32_32x32x16_bf16 a[240:255], a[88:91], v[224:227], a[240:255]\n"
    "s_waitcnt lgkmcnt(4)\n"
    "ds_read_b64_tr_b16 a[72:73], v242 offset:34816\n"
    "ds_read_b64_tr_b16 a[74:75], v242 offset:35840\n"
    "v_accvgpr_read_b32 v248, a208\n"
    "v_accvgpr_read_b32 v249, a209\n"
    "v_accvgpr_read_b32 v250, a210\n"
    "v_accvgpr_read_b32 v251, a211\n"
    "v_mfma_f32_32x32x16_bf16 a[240:255], a[92:95], v[232:235], a[240:255]\n"
    "s_waitcnt lgkmcnt(4)\n"
    "ds_read_b64_tr_b16 a[76:77], v243 offset:34816\n"
    "ds_read_b64_tr_b16 a[78:79], v243 offset:35840\n"
    "v_accvgpr_read_b32 v252, a212\n"
    "v_accvgpr_read_b32 v253, a213\n"
    "v_accvgpr_read_b32 v254, a214\n"
    "v_accvgpr_read_b32 v255, a215\n"
    "v_mfma_f32_32x32x16_bf16 v[16:31], a[64:67], v[112:115], 0\n"
    "s_waitcnt lgkmcnt(4)\n"
    "ds_read_b64_tr_b16 a[80:81], v242 offset:36864\n"
    "ds_read_b64_tr_b16 a[82:83], v242 offset:37888\n"
    "v_cvt_pk_bf16_f32 v152, v248, v249\n"
    "v_cvt_pk_bf16_f32 v153, v250, v251\n"
    "v_cvt_pk_bf16_f32 v154, v252, v253\n"
    "v_cvt_pk_bf16_f32 v155, v254, v255\n"
    "v_mfma_f32_32x32x16_bf16 v[32:47], a[68:71], v[112:115], 0\n"
    "s_waitcnt lgkmcnt(4)\n"
    "ds_read_b64_tr_b16 a[84:85], v243 offset:36864\n"
    "ds_read_b64_tr_b16 a[86:87], v243 offset:37888\n"
    "v_accvgpr_read_b32 v248, a216\n"
    "v_accvgpr_read_b32 v249, a217\n"
    "v_accvgpr_read_b32 v250, a218\n"
    "v_accvgpr_read_b32 v251, a219\n"
    "v_accvgpr_read_b32 v252, a220\n"
    "v_accvgpr_read_b32 v253, a221\n"
    "v_accvgpr_read_b32 v254, a222\n"
    "v_accvgpr_read_b32 v255, a223\n"
    "v_cvt_pk_bf16_f32 v156, v248, v249\n"
    "v_cvt_pk_bf16_f32 v157, v250, v251\n"
    "v_cvt_pk_bf16_f32 v158, v252, v253\n"
    "v_cvt_pk_bf16_f32 v159, v254, v255\n"
    "v_mfma_f32_32x32x16_bf16 v[16:31], a[72:75], v[116:119], v[16:31]\n"
    "s_waitcnt lgkmcnt(4)\n"
    "s_add_u32 m0, s65, 0x8000\n"
    "ds_read_b64_tr_b16 a[88:89], v242 offset:38912\n"
    "ds_read_b64_tr_b16 a[90:91], v242 offset:39936\n"
    "v_accvgpr_read_b32 v248, a224\n"
    "v_accvgpr_read_b32 v249, a225\n"
    "v_accvgpr_read_b32 v250, a226\n"
    "v_mfma_f32_32x32x16_bf16 v[32:47], a[76:79], v[116:119], v[32:47]\n"
    "s_waitcnt lgkmcnt(4)\n"
    "s_add_u32 s64, s68, 0x100\n"
    "ds_read_b64_tr_b16 a[92:93], v243 offset:38912\n"
    "ds_read_b64_tr_b16 a[94:95], v243 offset:39936\n"
    "v_accvgpr_read_b32 v251, a227\n"
    "v_accvgpr_read_b32 v252, a228\n"
    "v_accvgpr_read_b32 v253, a229\n"
    "v_mfma_f32_32x32x16_bf16 v[16:31], a[80:83], v[120:123], v[16:31]\n"
    "s_waitcnt lgkmcnt(4)\n"
    "ds_read_b64_tr_b16 a[64:65], v242 offset:40960\n"
    "ds_read_b64_tr_b16 a[66:67], v242 offset:41984\n"
    "buffer_load_dwordx4 v11, s[52:55], s64 offen offset:0 lds\n"
    "v_accvgpr_read_b32 v254, a230\n"
    "v_accvgpr_read_b32 v255, a231\n"
    "v_mfma_f32_32x32x16_bf16 v[32:47], a[84:87], v[120:123], v[32:47]\n"
    "s_waitcnt lgkmcnt(4)\n"
    "ds_read_b64_tr_b16 a[68:69], v243 offset:40960\n"
    "ds_read_b64_tr_b16 a[70:71], v243 offset:41984\n"
    "v_cvt_pk_bf16_f32 v160, v248, v249\n"
    "v_cvt_pk_bf16_f32 v161, v250, v251\n"
    "v_cvt_pk_bf16_f32 v162, v252, v253\n"
    "v_cvt_pk_bf16_f32 v163, v254, v255\n"
    "v_accvgpr_read_b32 v248, a232\n"
    "v_accvgpr_read_b32 v249, a233\n"
    "v_accvgpr_read_b32 v250, a234\n"
    "v_accvgpr_read_b32 v251, a235\n"
    "v_accvgpr_read_b32 v252, a236\n"
    "v_accvgpr_read_b32 v253, a237\n"
    "v_accvgpr_read_b32 v254, a238\n"
    "v_accvgpr_read_b32 v255, a239\n"
    "v_cvt_pk_bf16_f32 v164, v248, v249\n"
    "v_cvt_pk_bf16_f32 v165, v250, v251\n"
    "v_cvt_pk_bf16_f32 v166, v252, v253\n"
    "v_cvt_pk_bf16_f32 v167, v254, v255\n"
    "v_mfma_f32_32x32x16_bf16 v[16:31], a[88:91], v[124:127], v[16:31]\n"
    "s_waitcnt lgkmcnt(4)\n"
    "ds_read_b64_tr_b16 a[72:73], v242 offset:43008\n"
    "ds_read_b64_tr_b16 a[74:75], v242 offset:44032\n"
    "buffer_load_dwordx4 v12, s[52:55], s64 offen offset:1024 lds\n"
    "v_accvgpr_read_b32 v248, a240\n"
    "v_accvgpr_read_b32 v249, a241\n"
    "v_mfma_f32_32x32x16_bf16 v[32:47], a[92:95], v[124:127], v[32:47]\n"
    "s_waitcnt lgkmcnt(4)\n"
    "ds_read_b64_tr_b16 a[76:77], v243 offset:43008\n"
    "ds_read_b64_tr_b16 a[78:79], v243 offset:44032\n"
    "v_accvgpr_read_b32 v250, a242\n"
    "v_accvgpr_read_b32 v251, a243\n"
    "v_accvgpr_read_b32 v252, a244\n"
    "v_accvgpr_read_b32 v253, a245\n"
    "v_mfma_f32_32x32x16_bf16 v[16:31], a[64:67], v[128:131], v[16:31]\n"
    "s_waitcnt lgkmcnt(4)\n"
    "ds_read_b64_tr_b16 a[80:81], v242 offset:45056\n"
    "ds_read_b64_tr_b16 a[82:83], v242 offset:46080\n"
    "buffer_load_dwordx4 v13, s[52:55], s64 offen offset:2048 lds\n"
    "v_accvgpr_read_b32 v254, a246\n"
    "v_accvgpr_read_b32 v255, a247\n"
    "v_mfma_f32_32x32x16_bf16 v[32:47], a[68:71], v[128:131], v[32:47]\n"
    "s_waitcnt lgkmcnt(4)\n"
    "ds_read_b64_tr_b16 a[84:85], v243 offset:45056\n"
    "ds_read_b64_tr_b16 a[86:87], v243 offset:46080\n"
    "v_cvt_pk_bf16_f32 v168, v248, v249\n"
    "v_cvt_pk_bf16_f32 v169, v250, v251\n"
    "v_cvt_pk_bf16_f32 v170, v252, v253\n"
    "v_cvt_pk_bf16_f32 v171, v254, v255\n"
    "v_accvgpr_read_b32 v248, a248\n"
    "v_accvgpr_read_b32 v249, a249\n"
    "v_accvgpr_read_b32 v250, a250\n"
    "v_accvgpr_read_b32 v251, a251\n"
    "v_accvgpr_read_b32 v252, a252\n"
    "v_accvgpr_read_b32 v253, a253\n"
    "v_accvgpr_read_b32 v254, a254\n"
    "v_accvgpr_read_b32 v255, a255\n"
    "v_cvt_pk_bf16_f32 v172, v248, v249\n"
    "v_cvt_pk_bf16_f32 v173, v250, v251\n"
    "v_cvt_pk_bf16_f32 v174, v252, v253\n"
    "v_cvt_pk_bf16_f32 v175, v254, v255\n"
    "v_mfma_f32_32x32x16_bf16 v[16:31], a[72:75], v[132:135], v[16:31]\n"
    "s_waitcnt lgkmcnt(4)\n"
    "ds_read_b64_tr_b16 a[88:89], v242 offset:47104\n"
    "ds_read_b64_tr_b16 a[90:91], v242 offset:48128\n"
    "buffer_load_dwordx4 v14, s[52:55], s64 offen offset:3072 lds\n"
    "v_mfma_f32_32x32x16_bf16 v[32:47], a[76:79], v[132:135], v[32:47]\n"
    "s_waitcnt lgkmcnt(4)\n"
    "s_add_u32 m0, s65, 0xc000\n"
    "ds_read_b64_tr_b16 a[92:93], v243 offset:47104\n"
    "ds_read_b64_tr_b16 a[94:95], v243 offset:48128\n"
    "v_mfma_f32_32x32x16_bf16 v[16:31], a[80:83], v[136:139], v[16:31]\n"
    "s_waitcnt lgkmcnt(4)\n"
    "s_add_u32 s64, s68, 0x1c100\n"
    "ds_read_b64_tr_b16 a[64:65], v242 offset:49152\n"
    "ds_read_b64_tr_b16 a[66:67], v242 offset:50176\n"
    "v_mfma_f32_32x32x16_bf16 v[32:47], a[84:87], v[136:139], v[32:47]\n"
    "s_waitcnt lgkmcnt(4)\n"
    "ds_read_b64_tr_b16 a[68:69], v243 offset:49152\n"
    "ds_read_b64_tr_b16 a[70:71], v243 offset:50176\n"
    "buffer_load_dwordx4 v11, s[52:55], s64 offen offset:0 lds\n"
    "v_mfma_f32_32x32x16_bf16 v[16:31], a[88:91], v[140:143], v[16:31]\n"
    "s_waitcnt lgkmcnt(4)\n"
    "ds_read_b64_tr_b16 a[72:73], v242 offset:51200\n"
    "ds_read_b64_tr_b16 a[74:75], v242 offset:52224\n"
    "v_mfma_f32_32x32x16_bf16 v[32:47], a[92:95], v[140:143], v[32:47]\n"
    "s_waitcnt lgkmcnt(4)\n"
    "ds_read_b64_tr_b16 a[76:77], v243 offset:51200\n"
    "ds_read_b64_tr_b16 a[78:79], v243 offset:52224\n"
    "buffer_load_dwordx4 v12, s[52:55], s64 offen offset:1024 lds\n"
    "v_mfma_f32_32x32x16_bf16 v[16:31], a[64:67], v[144:147], v[16:31]\n"
    "s_waitcnt lgkmcnt(4)\n"
    "ds_read_b64_tr_b16 a[80:81], v242 offset:53248\n"
    "ds_read_b64_tr_b16 a[82:83], v242 offset:54272\n"
    "v_mfma_f32_32x32x16_bf16 v[32:47], a[68:71], v[144:147], v[32:47]\n"
    "s_waitcnt lgkmcnt(4)\n"
    "ds_read_b64_tr_b16 a[84:85], v243 offset:53248\n"
    "ds_read_b64_tr_b16 a[86:87], v243 offset:54272\n"
    "buffer_load_dwordx4 v13, s[52:55], s64 offen offset:2048 lds\n"
    "v_mfma_f32_32x32x16_bf16 v[16:31], a[72:75], v[148:151], v[16:31]\n"
    "s_waitcnt lgkmcnt(4)\n"
    "ds_read_b64_tr_b16 a[88:89], v242 offset:55296\n"
    "ds_read_b64_tr_b16 a[90:91], v242 offset:56320\n"
    "v_mfma_f32_32x32x16_bf16 v[32:47], a[76:79], v[148:151], v[32:47]\n"
    "s_waitcnt lgkmcnt(4)\n"
    "ds_read_b64_tr_b16 a[92:93], v243 offset:55296\n"
    "ds_read_b64_tr_b16 a[94:95], v243 offset:56320\n"
    "buffer_load_dwordx4 v14, s[52:55], s64 offen offset:3072 lds\n"
    "v_mfma_f32_32x32x16_bf16 v[16:31], a[80:83], v[152:155], v[16:31]\n"
    "s_waitcnt lgkmcnt(4)\n"
    "ds_read_b64_tr_b16 a[64:65], v242 offset:57344\n"
    "ds_read_b64_tr_b16 a[66:67], v242 offset:58368\n"
    "v_mfma_f32_32x32x16_bf16 v[32:47], a[84:87], v[152:155], v[32:47]\n"
    "s_waitcnt lgkmcnt(4)\n"
    "ds_read_b64_tr_b16 a[68:69], v243 offset:57344\n"
    "ds_read_b64_tr_b16 a[70:71], v243 offset:58368\n"
    "v_mfma_f32_32x32x16_bf16 v[16:31], a[88:91], v[156:159], v[16:31]\n"
    "s_waitcnt lgkmcnt(4)\n"
    "ds_read_b64_tr_b16 a[72:73], v242 offset:59392\n"
    "ds_read_b64_tr_b16 a[74:75], v242 offset:60416\n"
    "v_mfma_f32_32x32x16_bf16 v[32:47], a[92:95], v[156:159], v[32:47]\n"
    "s_waitcnt lgkmcnt(4)\n"
    "ds_read_b64_tr_b16 a[76:77], v243 offset:59392\n"
    "ds_read_b64_tr_b16 a[78:79], v243 offset:60416\n"
    "v_mfma_f32_32x32x16_bf16 v[16:31], a[64:67], v[160:163], v[16:31]\n"
    "s_waitcnt lgkmcnt(4)\n"
    "ds_read_b64_tr_b16 a[80:81], v242 offset:61440\n"
    "ds_read_b64_tr_b16 a[82:83], v242 offset:62464\n"
    "v_mfma_f32_32x32x16_bf16 v[32:47], a[68:71], v[160:163], v[32:47]\n"
    "s_waitcnt lgkmcnt(4)\n"
    "ds_read_b64_tr_b16 a[84:85], v243 offset:61440\n"
    "ds_read_b64_tr_b16 a[86:87], v243 offset:62464\n"
    "v_mfma_f32_32x32x16_bf16 v[16:31], a[72:75], v[164:167], v[16:31]\n"
    "s_waitcnt lgkmcnt(4)\n"
    "ds_read_b64_tr_b16 a[88:89], v242 offset:63488\n"
    "ds_read_b64_tr_b16 a[90:91], v242 offset:64512\n"
    "v_mfma_f32_32x32x16_bf16 v[32:47], a[76:79], v[164:167], v[32:47]\n"
    "s_waitcnt lgkmcnt(4)\n"
    "ds_read_b64_tr_b16 a[92:93], v243 offset:63488\n"
    "ds_read_b64_tr_b16 a[94:95], v243 offset:64512\n"
    "v_mfma_f32_32x32x16_bf16 v[16:31], a[80:83], v[168:171], v[16:31]\n"
    "s_waitcnt vmcnt(8) lgkmcnt(4)\n"
    "s_barrier\n"
    "ds_read_b64_tr_b16 a[64:65], v240 offset:0\n"
    "ds_read_b64_tr_b16 a[66:67], v240 offset:1024\n"
    "v_mfma_f32_32x32x16_bf16 v[32:47], a[84:87], v[168:171], v[32:47]\n"
    "s_waitcnt lgkmcnt(4)\n"
    "ds_read_b64_tr_b16 a[68:69], v241 offset:0\n"
    "ds_read_b64_tr_b16 a[70:71], v241 offset:1024\n"
    "v_mfma_f32_32x32x16_bf16 v[16:31], a[88:91], v[172:175], v[16:31]\n"
    "s_waitcnt lgkmcnt(4)\n"
    "ds_read_b64_tr_b16 a[72:73], v240 offset:2048\n"
    "ds_read_b64_tr_b16 a[74:75], v240 offset:3072\n"
    "v_mfma_f32_32x32x16_bf16 v[32:47], a[92:95], v[172:175], v[32:47]\n"
    "s_waitcnt lgkmcnt(4)\n"
    "ds_read_b64_tr_b16 a[76:77], v241 offset:2048\n"
    "ds_read_b64_tr_b16 a[78:79], v241 offset:3072\n"
    "v_mfma_f32_32x32x16_bf16 v[48:63], a[64:67], v[112:115], 0\n"
    "s_waitcnt lgkmcnt(4)\n"
    "ds_read_b64_tr_b16 a[80:81], v240 offset:4096\n"
    "ds_read_b64_tr_b16 a[82:83], v240 offset:5120\n"
    "v_mfma_f32_32x32x16_bf16 v[64:79], a[68:71], v[112:115], 0\n"
    "s_waitcnt lgkmcnt(4)\n"
    "ds_read_b64_tr_b16 a[84:85], v241 offset:4096\n"
    "ds_read_b64_tr_b16 a[86:87], v241 offset:5120\n"
    "v_mul_f32 v16, v16, v8\n"
    "v_mul_f32 v17, v17, v8\n"
    "v_mul_f32 v18, v18, v8\n"
    "v_mul_f32 v19, v19, v8\n"
    "v_mfma_f32_32x32x16_bf16 v[48:63], a[72:75], v[116:119], v[48:63]\n"
    "s_waitcnt lgkmcnt(4)\n"
    "s_add_u32 m0, s65, 0x10000\n"
    "ds_read_b64_tr_b16 a[88:89], v240 offset:6144\n"
    "ds_read_b64_tr_b16 a[90:91], v240 offset:7168\n"
    "v_mul_f32 v20, v20, v8\n"
    "v_mul_f32 v21, v21, v8\n"
    "v_mul_f32 v22, v22, v8\n"
    "v_mfma_f32_32x32x16_bf16 v[64:79], a[76:79], v[116:119], v[64:79]\n"
    "s_waitcnt lgkmcnt(4)\n"
    "s_add_u32 s64, s68, 0x180\n"
    "ds_read_b64_tr_b16 a[92:93], v241 offset:6144\n"
    "ds_read_b64_tr_b16 a[94:95], v241 offset:7168\n"
    "v_mul_f32 v23, v23, v8\n"
    "v_cvt_pk_bf16_f32 v16, v16, v17\n"
    "v_cvt_pk_bf16_f32 v17, v18, v19\n"
    "v_mfma_f32_32x32x16_bf16 v[48:63], a[80:83], v[120:123], v[48:63]\n"
    "s_waitcnt lgkmcnt(4)\n"
    "ds_read_b64_tr_b16 a[64:65], v240 offset:8192\n"
    "ds_read_b64_tr_b16 a[66:67], v240 offset:9216\n"
    "buffer_load_dwordx4 v11, s[52:55], s64 offen offset:0 lds\n"
    "v_cvt_pk_bf16_f32 v18, v20, v21\n"
    "v_cvt_pk_bf16_f32 v19, v22, v23\n"
    "v_mfma_f32_32x32x16_bf16 v[64:79], a[84:87], v[120:123], v[64:79]\n"
    "s_waitcnt lgkmcnt(4)\n"
    "ds_read_b64_tr_b16 a[68:69], v241 offset:8192\n"
    "ds_read_b64_tr_b16 a[70:71], v241 offset:9216\n"
    "v_mul_f32 v24, v24, v8\n"
    "v_mul_f32 v25, v25, v8\n"
    "v_mul_f32 v26, v26, v8\n"
    "v_mul_f32 v27, v27, v8\n"
    "v_mfma_f32_32x32x16_bf16 v[48:63], a[88:91], v[124:127], v[48:63]\n"
    "s_waitcnt lgkmcnt(4)\n"
    "ds_read_b64_tr_b16 a[72:73], v240 offset:10240\n"
    "ds_read_b64_tr_b16 a[74:75], v240 offset:11264\n"
    "buffer_load_dwordx4 v12, s[52:55], s64 offen offset:1024 lds\n"
    "v_mul_f32 v28, v28, v8\n"
    "v_mul_f32 v29, v29, v8\n"
    "v_mfma_f32_32x32x16_bf16 v[64:79], a[92:95], v[124:127], v[64:79]\n"
    "s_waitcnt lgkmcnt(4)\n"
    "ds_read_b64_tr_b16 a[76:77], v241 offset:10240\n"
    "ds_read_b64_tr_b16 a[78:79], v241 offset:11264\n"
    "v_mul_f32 v30, v30, v8\n"
    "v_mul_f32 v31, v31, v8\n"
    "v_cvt_pk_bf16_f32 v24, v24, v25\n"
    "v_cvt_pk_bf16_f32 v25, v26, v27\n"
    "v_mfma_f32_32x32x16_bf16 v[48:63], a[64:67], v[128:131], v[48:63]\n"
    "s_waitcnt lgkmcnt(4)\n"
    "ds_read_b64_tr_b16 a[80:81], v240 offset:12288\n"
    "ds_read_b64_tr_b16 a[82:83], v240 offset:13312\n"
    "buffer_load_dwordx4 v13, s[52:55], s64 offen offset:2048 lds\n"
    "v_cvt_pk_bf16_f32 v26, v28, v29\n"
    "v_cvt_pk_bf16_f32 v27, v30, v31\n"
    "v_mfma_f32_32x32x16_bf16 v[64:79], a[68:71], v[128:131], v[64:79]\n"
    "s_waitcnt lgkmcnt(4)\n"
    "ds_read_b64_tr_b16 a[84:85], v241 offset:12288\n"
    "ds_read_b64_tr_b16 a[86:87], v241 offset:13312\n"
    "v_mfma_f32_32x32x16_bf16 v[48:63], a[72:75], v[132:135], v[48:63]\n"
    "s_waitcnt lgkmcnt(4)\n"
    "ds_read_b64_tr_b16 a[88:89], v240 offset:14336\n"
    "ds_read_b64_tr_b16 a[90:91], v240 offset:15360\n"
    "buffer_load_dwordx4 v14, s[52:55], s64 offen offset:3072 lds\n"
    "v_mfma_f32_32x32x16_bf16 v[64:79], a[76:79], v[132:135], v[64:79]\n"
    "s_waitcnt lgkmcnt(4)\n"
    "s_add_u32 m0, s65, 0x14000\n"
    "ds_read_b64_tr_b16 a[92:93], v241 offset:14336\n"
    "ds_read_b64_tr_b16 a[94:95], v241 offset:15360\n"
    "v_mfma_f32_32x32x16_bf16 v[48:63], a[80:83], v[136:139], v[48:63]\n"
    "s_waitcnt lgkmcnt(4)\n"
    "s_add_u32 s64, s68, 0x1c180\n"
    "ds_read_b64_tr_b16 a[64:65], v240 offset:16384\n"
    "ds_read_b64_tr_b16 a[66:67], v240 offset:17408\n"
    "v_mul_f32 v32, v32, v8\n"
    "v_mul_f32 v33, v33, v8\n"
    "v_mul_f32 v34, v34, v8\n"
    "v_mfma_f32_32x32x16_bf16 v[64:79], a[84:87], v[136:139], v[64:79]\n"
    "s_waitcnt lgkmcnt(4)\n"
    "ds_read_b64_tr_b16 a[68:69], v241 offset:16384\n"
    "ds_read_b64_tr_b16 a[70:71], v241 offset:17408\n"
    "buffer_load_dwordx4 v11, s[52:55], s64 offen offset:0 lds\n"
    "v_mul_f32 v35, v35, v8\n"
    "v_mul_f32 v36, v36, v8\n"
    "v_mfma_f32_32x32x16_bf16 v[48:63], a[88:91], v[140:143], v[48:63]\n"
    "s_waitcnt lgkmcnt(4)\n"
    "s_mov_b32 s76, 0x0\n"
    "ds_read_b64_tr_b16 a[72:73], v240 offset:18432\n"
    "ds_read_b64_tr_b16 a[74:75], v240 offset:19456\n"
    "buffer_store_dwordx4 v[16:19], v0, s[56:59], s76 offen offset:0 sc1\n"
    "v_mfma_f32_32x32x16_bf16 v[64:79], a[92:95], v[140:143], v[64:79]\n"
    "s_waitcnt lgkmcnt(4)\n"
    "ds_read_b64_tr_b16 a[76:77], v241 offset:18432\n"
    "ds_read_b64_tr_b16 a[78:79], v241 offset:19456\n"
    "buffer_load_dwordx4 v12, s[52:55], s64 offen offset:1024 lds\n"
    "buffer_store_dwordx4 v[24:27], v0, s[56:59], s76 offen offset:1024 sc1\n"
    "v_mfma_f32_32x32x16_bf16 v[48:63], a[64:67], v[144:147], v[48:63]\n"
    "s_waitcnt lgkmcnt(4)\n"
    "ds_read_b64_tr_b16 a[80:81], v240 offset:20480\n"
    "ds_read_b64_tr_b16 a[82:83], v240 offset:21504\n"
    "v_mul_f32 v37, v37, v8\n"
    "v_mul_f32 v38, v38, v8\n"
    "v_mul_f32 v39, v39, v8\n"
    "v_cvt_pk_bf16_f32 v32, v32, v33\n"
    "v_mfma_f32_32x32x16_bf16 v[64:79], a[68:71], v[144:147], v[64:79]\n"
    "s_waitcnt lgkmcnt(4)\n"
    "ds_read_b64_tr_b16 a[84:85], v241 offset:20480\n"
    "ds_read_b64_tr_b16 a[86:87], v241 offset:21504\n"
    "buffer_load_dwordx4 v13, s[52:55], s64 offen offset:2048 lds\n"
    "v_cvt_pk_bf16_f32 v33, v34, v35\n"
    "v_cvt_pk_bf16_f32 v34, v36, v37\n"
    "v_mfma_f32_32x32x16_bf16 v[48:63], a[72:75], v[148:151], v[48:63]\n"
    "s_waitcnt lgkmcnt(4)\n"
    "ds_read_b64_tr_b16 a[88:89], v240 offset:22528\n"
    "ds_read_b64_tr_b16 a[90:91], v240 offset:23552\n"
    "v_cvt_pk_bf16_f32 v35, v38, v39\n"
    "v_mul_f32 v40, v40, v8\n"
    "v_mul_f32 v41, v41, v8\n"
    "v_mul_f32 v42, v42, v8\n"
    "v_mfma_f32_32x32x16_bf16 v[64:79], a[76:79], v[148:151], v[64:79]\n"
    "s_waitcnt lgkmcnt(4)\n"
    "ds_read_b64_tr_b16 a[92:93], v241 offset:22528\n"
    "ds_read_b64_tr_b16 a[94:95], v241 offset:23552\n"
    "buffer_load_dwordx4 v14, s[52:55], s64 offen offset:3072 lds\n"
    "v_mul_f32 v43, v43, v8\n"
    "v_mul_f32 v44, v44, v8\n"
    "v_mfma_f32_32x32x16_bf16 v[48:63], a[80:83], v[152:155], v[48:63]\n"
    "s_waitcnt lgkmcnt(4)\n"
    "ds_read_b64_tr_b16 a[64:65], v240 offset:24576\n"
    "ds_read_b64_tr_b16 a[66:67], v240 offset:25600\n"
    "v_mul_f32 v45, v45, v8\n"
    "v_mul_f32 v46, v46, v8\n"
    "v_mul_f32 v47, v47, v8\n"
    "v_cvt_pk_bf16_f32 v40, v40, v41\n"
    "v_mfma_f32_32x32x16_bf16 v[64:79], a[84:87], v[152:155], v[64:79]\n"
    "s_waitcnt lgkmcnt(4)\n"
    "ds_read_b64_tr_b16 a[68:69], v241 offset:24576\n"
    "ds_read_b64_tr_b16 a[70:71], v241 offset:25600\n"
    "v_cvt_pk_bf16_f32 v41, v42, v43\n"
    "v_cvt_pk_bf16_f32 v42, v44, v45\n"
    "v_cvt_pk_bf16_f32 v43, v46, v47\n"
    "v_mfma_f32_32x32x16_bf16 v[48:63], a[88:91], v[156:159], v[48:63]\n"
    "s_waitcnt lgkmcnt(4)\n"
    "ds_read_b64_tr_b16 a[72:73], v240 offset:26624\n"
    "ds_read_b64_tr_b16 a[74:75], v240 offset:27648\n"
    "v_mfma_f32_32x32x16_bf16 v[64:79], a[92:95], v[156:159], v[64:79]\n"
    "s_waitcnt lgkmcnt(4)\n"
    "ds_read_b64_tr_b16 a[76:77], v241 offset:26624\n"
    "ds_read_b64_tr_b16 a[78:79], v241 offset:27648\n"
    "v_mfma_f32_32x32x16_bf16 v[48:63], a[64:67], v[160:163], v[48:63]\n"
    "s_waitcnt lgkmcnt(4)\n"
    "ds_read_b64_tr_b16 a[80:81], v240 offset:28672\n"
    "ds_read_b64_tr_b16 a[82:83], v240 offset:29696\n"
    "v_mfma_f32_32x32x16_bf16 v[64:79], a[68:71], v[160:163], v[64:79]\n"
    "s_waitcnt lgkmcnt(4)\n"
    "s_mov_b32 s76, 0x800\n"
    "ds_read_b64_tr_b16 a[84:85], v241 offset:28672\n"
    "ds_read_b64_tr_b16 a[86:87], v241 offset:29696\n"
    "buffer_store_dwordx4 v[32:35], v0, s[56:59], s76 offen offset:0 sc1\n"
    "v_mfma_f32_32x32x16_bf16 v[48:63], a[72:75], v[164:167], v[48:63]\n"
    "s_waitcnt lgkmcnt(4)\n"
    "ds_read_b64_tr_b16 a[88:89], v240 offset:30720\n"
    "ds_read_b64_tr_b16 a[90:91], v240 offset:31744\n"
    "buffer_store_dwordx4 v[40:43], v0, s[56:59], s76 offen offset:1024 sc1\n"
    "v_mfma_f32_32x32x16_bf16 v[64:79], a[76:79], v[164:167], v[64:79]\n"
    "s_waitcnt lgkmcnt(4)\n"
    "ds_read_b64_tr_b16 a[92:93], v241 offset:30720\n"
    "ds_read_b64_tr_b16 a[94:95], v241 offset:31744\n"
    "v_mfma_f32_32x32x16_bf16 v[48:63], a[80:83], v[168:171], v[48:63]\n"
    "s_waitcnt vmcnt(12) lgkmcnt(4)\n"
    "s_barrier\n"
    "ds_read_b64_tr_b16 a[64:65], v240 offset:32768\n"
    "ds_read_b64_tr_b16 a[66:67], v240 offset:33792\n"
    "v_mfma_f32_32x32x16_bf16 v[64:79], a[84:87], v[168:171], v[64:79]\n"
    "s_waitcnt lgkmcnt(4)\n"
    "ds_read_b64_tr_b16 a[68:69], v241 offset:32768\n"
    "ds_read_b64_tr_b16 a[70:71], v241 offset:33792\n"
    "v_mfma_f32_32x32x16_bf16 v[48:63], a[88:91], v[172:175], v[48:63]\n"
    "s_waitcnt lgkmcnt(4)\n"
    "ds_read_b64_tr_b16 a[72:73], v240 offset:34816\n"
    "ds_read_b64_tr_b16 a[74:75], v240 offset:35840\n"
    "v_mfma_f32_32x32x16_bf16 v[64:79], a[92:95], v[172:175], v[64:79]\n"
    "s_waitcnt lgkmcnt(4)\n"
    "ds_read_b64_tr_b16 a[76:77], v241 offset:34816\n"
    "ds_read_b64_tr_b16 a[78:79], v241 offset:35840\n"
    "v_mfma_f32_32x32x16_bf16 v[80:95], a[64:67], v[112:115], 0\n"
    "s_waitcnt lgkmcnt(4)\n"
    "ds_read_b64_tr_b16 a[80:81], v240 offset:36864\n"
    "ds_read_b64_tr_b16 a[82:83], v240 offset:37888\n"
    "v_mfma_f32_32x32x16_bf16 v[96:111], a[68:71], v[112:115], 0\n"
    "s_waitcnt lgkmcnt(4)\n"
    "ds_read_b64_tr_b16 a[84:85], v241 offset:36864\n"
    "ds_read_b64_tr_b16 a[86:87], v241 offset:37888\n"
    "v_mul_f32 v48, v48, v8\n"
    "v_mul_f32 v49, v49, v8\n"
    "v_mul_f32 v50, v50, v8\n"
    "v_mul_f32 v51, v51, v8\n"
    "v_mfma_f32_32x32x16_bf16 v[80:95], a[72:75], v[116:119], v[80:95]\n"
    "s_waitcnt lgkmcnt(4)\n"
    "s_add_u32 m0, s65, 0x18000\n"
    "ds_read_b64_tr_b16 a[88:89], v240 offset:38912\n"
    "ds_read_b64_tr_b16 a[90:91], v240 offset:39936\n"
    "v_mul_f32 v52, v52, v8\n"
    "v_mul_f32 v53, v53, v8\n"
    "v_mul_f32 v54, v54, v8\n"
    "v_mfma_f32_32x32x16_bf16 v[96:111], a[76:79], v[116:119], v[96:111]\n"
    "s_waitcnt lgkmcnt(4)\n"
    "s_add_u32 s64, s68, 0x200\n"
    "ds_read_b64_tr_b16 a[92:93], v241 offset:38912\n"
    "ds_read_b64_tr_b16 a[94:95], v241 offset:39936\n"
    "v_mul_f32 v55, v55, v8\n"
    "v_cvt_pk_bf16_f32 v48, v48, v49\n"
    "v_cvt_pk_bf16_f32 v49, v50, v51\n"
    "v_mfma_f32_32x32x16_bf16 v[80:95], a[80:83], v[120:123], v[80:95]\n"
    "s_waitcnt lgkmcnt(4)\n"
    "ds_read_b64_tr_b16 a[64:65], v240 offset:40960\n"
    "ds_read_b64_tr_b16 a[66:67], v240 offset:41984\n"
    "buffer_load_dwordx4 v11, s[52:55], s64 offen offset:0 lds\n"
    "v_cvt_pk_bf16_f32 v50, v52, v53\n"
    "v_cvt_pk_bf16_f32 v51, v54, v55\n"
    "v_mfma_f32_32x32x16_bf16 v[96:111], a[84:87], v[120:123], v[96:111]\n"
    "s_waitcnt lgkmcnt(4)\n"
    "ds_read_b64_tr_b16 a[68:69], v241 offset:40960\n"
    "ds_read_b64_tr_b16 a[70:71], v241 offset:41984\n"
    "v_mul_f32 v56, v56, v8\n"
    "v_mul_f32 v57, v57, v8\n"
    "v_mul_f32 v58, v58, v8\n"
    "v_mul_f32 v59, v59, v8\n"
    "v_mfma_f32_32x32x16_bf16 v[80:95], a[88:91], v[124:127], v[80:95]\n"
    "s_waitcnt lgkmcnt(4)\n"
    "ds_read_b64_tr_b16 a[72:73], v240 offset:43008\n"
    "ds_read_b64_tr_b16 a[74:75], v240 offset:44032\n"
    "buffer_load_dwordx4 v12, s[52:55], s64 offen offset:1024 lds\n"
    "v_mul_f32 v60, v60, v8\n"
    "v_mul_f32 v61, v61, v8\n"
    "v_mfma_f32_32x32x16_bf16 v[96:111], a[92:95], v[124:127], v[96:111]\n"
    "s_waitcnt lgkmcnt(4)\n"
    "ds_read_b64_tr_b16 a[76:77], v241 offset:43008\n"
    "ds_read_b64_tr_b16 a[78:79], v241 offset:44032\n"
    "v_mul_f32 v62, v62, v8\n"
    "v_mul_f32 v63, v63, v8\n"
    "v_cvt_pk_bf16_f32 v56, v56, v57\n"
    "v_cvt_pk_bf16_f32 v57, v58, v59\n"
    "v_mfma_f32_32x32x16_bf16 v[80:95], a[64:67], v[128:131], v[80:95]\n"
    "s_waitcnt lgkmcnt(4)\n"
    "ds_read_b64_tr_b16 a[80:81], v240 offset:45056\n"
    "ds_read_b64_tr_b16 a[82:83], v240 offset:46080\n"
    "buffer_load_dwordx4 v13, s[52:55], s64 offen offset:2048 lds\n"
    "v_cvt_pk_bf16_f32 v58, v60, v61\n"
    "v_cvt_pk_bf16_f32 v59, v62, v63\n"
    "v_mfma_f32_32x32x16_bf16 v[96:111], a[68:71], v[128:131], v[96:111]\n"
    "s_waitcnt lgkmcnt(4)\n"
    "ds_read_b64_tr_b16 a[84:85], v241 offset:45056\n"
    "ds_read_b64_tr_b16 a[86:87], v241 offset:46080\n"
    "v_mfma_f32_32x32x16_bf16 v[80:95], a[72:75], v[132:135], v[80:95]\n"
    "s_waitcnt lgkmcnt(4)\n"
    "ds_read_b64_tr_b16 a[88:89], v240 offset:47104\n"
    "ds_read_b64_tr_b16 a[90:91], v240 offset:48128\n"
    "buffer_load_dwordx4 v14, s[52:55], s64 offen offset:3072 lds\n"
    "v_mfma_f32_32x32x16_bf16 v[96:111], a[76:79], v[132:135], v[96:111]\n"
    "s_waitcnt lgkmcnt(4)\n"
    "s_add_u32 m0, s65, 0x1c000\n"
    "ds_read_b64_tr_b16 a[92:93], v241 offset:47104\n"
    "ds_read_b64_tr_b16 a[94:95], v241 offset:48128\n"
    "v_mfma_f32_32x32x16_bf16 v[80:95], a[80:83], v[136:139], v[80:95]\n"
    "s_waitcnt lgkmcnt(4)\n"
    "s_add_u32 s64, s68, 0x1c200\n"
    "ds_read_b64_tr_b16 a[64:65], v240 offset:49152\n"
    "ds_read_b64_tr_b16 a[66:67], v240 offset:50176\n"
    "v_mul_f32 v64, v64, v8\n"
    "v_mul_f32 v65, v65, v8\n"
    "v_mul_f32 v66, v66, v8\n"
    "v_mfma_f32_32x32x16_bf16 v[96:111], a[84:87], v[136:139], v[96:111]\n"
    "s_waitcnt lgkmcnt(4)\n"
    "ds_read_b64_tr_b16 a[68:69], v241 offset:49152\n"
    "ds_read_b64_tr_b16 a[70:71], v241 offset:50176\n"
    "buffer_load_dwordx4 v11, s[52:55], s64 offen offset:0 lds\n"
    "v_mul_f32 v67, v67, v8\n"
    "v_mul_f32 v68, v68, v8\n"
    "v_mfma_f32_32x32x16_bf16 v[80:95], a[88:91], v[140:143], v[80:95]\n"
    "s_waitcnt lgkmcnt(4)\n"
    "s_mov_b32 s76, 0x1000\n"
    "ds_read_b64_tr_b16 a[72:73], v240 offset:51200\n"
    "ds_read_b64_tr_b16 a[74:75], v240 offset:52224\n"
    "buffer_store_dwordx4 v[48:51], v0, s[56:59], s76 offen offset:0 sc1\n"
    "v_mfma_f32_32x32x16_bf16 v[96:111], a[92:95], v[140:143], v[96:111]\n"
    "s_waitcnt lgkmcnt(4)\n"
    "ds_read_b64_tr_b16 a[76:77], v241 offset:51200\n"
    "ds_read_b64_tr_b16 a[78:79], v241 offset:52224\n"
    "buffer_load_dwordx4 v12, s[52:55], s64 offen offset:1024 lds\n"
    "buffer_store_dwordx4 v[56:59], v0, s[56:59], s76 offen offset:1024 sc1\n"
    "v_mfma_f32_32x32x16_bf16 v[80:95], a[64:67], v[144:147], v[80:95]\n"
    "s_waitcnt lgkmcnt(4)\n"
    "ds_read_b64_tr_b16 a[80:81], v240 offset:53248\n"
    "ds_read_b64_tr_b16 a[82:83], v240 offset:54272\n"
    "v_mul_f32 v69, v69, v8\n"
    "v_mul_f32 v70, v70, v8\n"
    "v_mul_f32 v71, v71, v8\n"
    "v_cvt_pk_bf16_f32 v64, v64, v65\n"
    "v_mfma_f32_32x32x16_bf16 v[96:111], a[68:71], v[144:147], v[96:111]\n"
    "s_waitcnt lgkmcnt(4)\n"
    "ds_read_b64_tr_b16 a[84:85], v241 offset:53248\n"
    "ds_read_b64_tr_b16 a[86:87], v241 offset:54272\n"
    "buffer_load_dwordx4 v13, s[52:55], s64 offen offset:2048 lds\n"
    "v_cvt_pk_bf16_f32 v65, v66, v67\n"
    "v_cvt_pk_bf16_f32 v66, v68, v69\n"
    "v_mfma_f32_32x32x16_bf16 v[80:95], a[72:75], v[148:151], v[80:95]\n"
    "s_waitcnt lgkmcnt(4)\n"
    "ds_read_b64_tr_b16 a[88:89], v240 offset:55296\n"
    "ds_read_b64_tr_b16 a[90:91], v240 offset:56320\n"
    "v_cvt_pk_bf16_f32 v67, v70, v71\n"
    "v_mul_f32 v72, v72, v8\n"
    "v_mul_f32 v73, v73, v8\n"
    "v_mul_f32 v74, v74, v8\n"
    "v_mfma_f32_32x32x16_bf16 v[96:111], a[76:79], v[148:151], v[96:111]\n"
    "s_waitcnt lgkmcnt(4)\n"
    "ds_read_b64_tr_b16 a[92:93], v241 offset:55296\n"
    "ds_read_b64_tr_b16 a[94:95], v241 offset:56320\n"
    "buffer_load_dwordx4 v14, s[52:55], s64 offen offset:3072 lds\n"
    "v_mul_f32 v75, v75, v8\n"
    "v_mul_f32 v76, v76, v8\n"
    "v_mfma_f32_32x32x16_bf16 v[80:95], a[80:83], v[152:155], v[80:95]\n"
    "s_waitcnt lgkmcnt(4)\n"
    "ds_read_b64_tr_b16 a[64:65], v240 offset:57344\n"
    "ds_read_b64_tr_b16 a[66:67], v240 offset:58368\n"
    "v_mul_f32 v77, v77, v8\n"
    "v_mul_f32 v78, v78, v8\n"
    "v_mul_f32 v79, v79, v8\n"
    "v_cvt_pk_bf16_f32 v72, v72, v73\n"
    "v_mfma_f32_32x32x16_bf16 v[96:111], a[84:87], v[152:155], v[96:111]\n"
    "s_waitcnt lgkmcnt(4)\n"
    "ds_read_b64_tr_b16 a[68:69], v241 offset:57344\n"
    "ds_read_b64_tr_b16 a[70:71], v241 offset:58368\n"
    "v_cvt_pk_bf16_f32 v73, v74, v75\n"
    "v_cvt_pk_bf16_f32 v74, v76, v77\n"
    "v_cvt_pk_bf16_f32 v75, v78, v79\n"
    "v_mfma_f32_32x32x16_bf16 v[80:95], a[88:91], v[156:159], v[80:95]\n"
    "s_waitcnt lgkmcnt(4)\n"
    "ds_read_b64_tr_b16 a[72:73], v240 offset:59392\n"
    "ds_read_b64_tr_b16 a[74:75], v240 offset:60416\n"
    "v_mfma_f32_32x32x16_bf16 v[96:111], a[92:95], v[156:159], v[96:111]\n"
    "s_waitcnt lgkmcnt(4)\n"
    "ds_read_b64_tr_b16 a[76:77], v241 offset:59392\n"
    "ds_read_b64_tr_b16 a[78:79], v241 offset:60416\n"
    "v_mfma_f32_32x32x16_bf16 v[80:95], a[64:67], v[160:163], v[80:95]\n"
    "s_waitcnt lgkmcnt(4)\n"
    "ds_read_b64_tr_b16 a[80:81], v240 offset:61440\n"
    "ds_read_b64_tr_b16 a[82:83], v240 offset:62464\n"
    "v_mfma_f32_32x32x16_bf16 v[96:111], a[68:71], v[160:163], v[96:111]\n"
    "s_waitcnt lgkmcnt(4)\n"
    "s_mov_b32 s76, 0x1800\n"
    "ds_read_b64_tr_b16 a[84:85], v241 offset:61440\n"
    "ds_read_b64_tr_b16 a[86:87], v241 offset:62464\n"
    "buffer_store_dwordx4 v[64:67], v0, s[56:59], s76 offen offset:0 sc1\n"
    "v_mfma_f32_32x32x16_bf16 v[80:95], a[72:75], v[164:167], v[80:95]\n"
    "s_waitcnt lgkmcnt(4)\n"
    "ds_read_b64_tr_b16 a[88:89], v240 offset:63488\n"
    "ds_read_b64_tr_b16 a[90:91], v240 offset:64512\n"
    "buffer_store_dwordx4 v[72:75], v0, s[56:59], s76 offen offset:1024 sc1\n"
    "v_mfma_f32_32x32x16_bf16 v[96:111], a[76:79], v[164:167], v[96:111]\n"
    "s_waitcnt lgkmcnt(4)\n"
    "ds_read_b64_tr_b16 a[92:93], v241 offset:63488\n"
    "ds_read_b64_tr_b16 a[94:95], v241 offset:64512\n"
    "v_mfma_f32_32x32x16_bf16 v[80:95], a[80:83], v[168:171], v[80:95]\n"
    "s_waitcnt vmcnt(14) lgkmcnt(4)\n"
    "s_barrier\n"
    "ds_read_b64_tr_b16 a[64:65], v242 offset:0\n"
    "ds_read_b64_tr_b16 a[66:67], v242 offset:1024\n"
    "v_mfma_f32_32x32x16_bf16 v[96:111], a[84:87], v[168:171], v[96:111]\n"
    "s_waitcnt lgkmcnt(4)\n"
    "ds_read_b64_tr_b16 a[68:69], v243 offset:0\n"
    "ds_read_b64_tr_b16 a[70:71], v243 offset:1024\n"
    "v_mfma_f32_32x32x16_bf16 v[80:95], a[88:91], v[172:175], v[80:95]\n"
    "s_waitcnt lgkmcnt(4)\n"
    "ds_read_b64_tr_b16 a[72:73], v242 offset:2048\n"
    "ds_read_b64_tr_b16 a[74:75], v242 offset:3072\n"
    "v_mfma_f32_32x32x16_bf16 v[96:111], a[92:95], v[172:175], v[96:111]\n"
    "s_waitcnt lgkmcnt(4)\n"
    "ds_read_b64_tr_b16 a[76:77], v243 offset:2048\n"
    "ds_read_b64_tr_b16 a[78:79], v243 offset:3072\n"
    "v_mfma_f32_32x32x16_bf16 v[16:31], a[64:67], v[112:115], 0\n"
    "s_waitcnt lgkmcnt(4)\n"
    "ds_read_b64_tr_b16 a[80:81], v242 offset:4096\n"
    "ds_read_b64_tr_b16 a[82:83], v242 offset:5120\n"
    "v_mfma_f32_32x32x16_bf16 v[32:47], a[68:71], v[112:115], 0\n"
    "s_waitcnt lgkmcnt(4)\n"
    "ds_read_b64_tr_b16 a[84:85], v243 offset:4096\n"
    "ds_read_b64_tr_b16 a[86:87], v243 offset:5120\n"
    "v_mul_f32 v80, v80, v8\n"
    "v_mul_f32 v81, v81, v8\n"
    "v_mul_f32 v82, v82, v8\n"
    "v_mul_f32 v83, v83, v8\n"
    "v_mfma_f32_32x32x16_bf16 v[16:31], a[72:75], v[116:119], v[16:31]\n"
    "s_waitcnt lgkmcnt(4)\n"
    "s_add_u32 m0, s65, 0x0\n"
    "ds_read_b64_tr_b16 a[88:89], v242 offset:6144\n"
    "ds_read_b64_tr_b16 a[90:91], v242 offset:7168\n"
    "v_mul_f32 v84, v84, v8\n"
    "v_mul_f32 v85, v85, v8\n"
    "v_mul_f32 v86, v86, v8\n"
    "v_mfma_f32_32x32x16_bf16 v[32:47], a[76:79], v[116:119], v[32:47]\n"
    "s_waitcnt lgkmcnt(4)\n"
    "s_add_u32 s64, s68, 0x280\n"
    "ds_read_b64_tr_b16 a[92:93], v243 offset:6144\n"
    "ds_read_b64_tr_b16 a[94:95], v243 offset:7168\n"
    "v_mul_f32 v87, v87, v8\n"
    "v_cvt_pk_bf16_f32 v80, v80, v81\n"
    "v_cvt_pk_bf16_f32 v81, v82, v83\n"
    "v_mfma_f32_32x32x16_bf16 v[16:31], a[80:83], v[120:123], v[16:31]\n"
    "s_waitcnt lgkmcnt(4)\n"
    "ds_read_b64_tr_b16 a[64:65], v242 offset:8192\n"
    "ds_read_b64_tr_b16 a[66:67], v242 offset:9216\n"
    "buffer_load_dwordx4 v11, s[52:55], s64 offen offset:0 lds\n"
    "v_cvt_pk_bf16_f32 v82, v84, v85\n"
    "v_cvt_pk_bf16_f32 v83, v86, v87\n"
    "v_mfma_f32_32x32x16_bf16 v[32:47], a[84:87], v[120:123], v[32:47]\n"
    "s_waitcnt lgkmcnt(4)\n"
    "ds_read_b64_tr_b16 a[68:69], v243 offset:8192\n"
    "ds_read_b64_tr_b16 a[70:71], v243 offset:9216\n"
    "v_mul_f32 v88, v88, v8\n"
    "v_mul_f32 v89, v89, v8\n"
    "v_mul_f32 v90, v90, v8\n"
    "v_mul_f32 v91, v91, v8\n"
    "v_mfma_f32_32x32x16_bf16 v[16:31], a[88:91], v[124:127], v[16:31]\n"
    "s_waitcnt lgkmcnt(4)\n"
    "ds_read_b64_tr_b16 a[72:73], v242 offset:10240\n"
    "ds_read_b64_tr_b16 a[74:75], v242 offset:11264\n"
    "buffer_load_dwordx4 v12, s[52:55], s64 offen offset:1024 lds\n"
    "v_mul_f32 v92, v92, v8\n"
    "v_mul_f32 v93, v93, v8\n"
    "v_mfma_f32_32x32x16_bf16 v[32:47], a[92:95], v[124:127], v[32:47]\n"
    "s_waitcnt lgkmcnt(4)\n"
    "ds_read_b64_tr_b16 a[76:77], v243 offset:10240\n"
    "ds_read_b64_tr_b16 a[78:79], v243 offset:11264\n"
    "v_mul_f32 v94, v94, v8\n"
    "v_mul_f32 v95, v95, v8\n"
    "v_cvt_pk_bf16_f32 v88, v88, v89\n"
    "v_cvt_pk_bf16_f32 v89, v90, v91\n"
    "v_mfma_f32_32x32x16_bf16 v[16:31], a[64:67], v[128:131], v[16:31]\n"
    "s_waitcnt lgkmcnt(4)\n"
    "ds_read_b64_tr_b16 a[80:81], v242 offset:12288\n"
    "ds_read_b64_tr_b16 a[82:83], v242 offset:13312\n"
    "buffer_load_dwordx4 v13, s[52:55], s64 offen offset:2048 lds\n"
    "v_cvt_pk_bf16_f32 v90, v92, v93\n"
    "v_cvt_pk_bf16_f32 v91, v94, v95\n"
    "v_mfma_f32_32x32x16_bf16 v[32:47], a[68:71], v[128:131], v[32:47]\n"
    "s_waitcnt lgkmcnt(4)\n"
    "ds_read_b64_tr_b16 a[84:85], v243 offset:12288\n"
    "ds_read_b64_tr_b16 a[86:87], v243 offset:13312\n"
    "v_mfma_f32_32x32x16_bf16 v[16:31], a[72:75], v[132:135], v[16:31]\n"
    "s_waitcnt lgkmcnt(4)\n"
    "ds_read_b64_tr_b16 a[88:89], v242 offset:14336\n"
    "ds_read_b64_tr_b16 a[90:91], v242 offset:15360\n"
    "buffer_load_dwordx4 v14, s[52:55], s64 offen offset:3072 lds\n"
    "v_mfma_f32_32x32x16_bf16 v[32:47], a[76:79], v[132:135], v[32:47]\n"
    "s_waitcnt lgkmcnt(4)\n"
    "s_add_u32 m0, s65, 0x4000\n"
    "ds_read_b64_tr_b16 a[92:93], v243 offset:14336\n"
    "ds_read_b64_tr_b16 a[94:95], v243 offset:15360\n"
    "v_mfma_f32_32x32x16_bf16 v[16:31], a[80:83], v[136:139], v[16:31]\n"
    "s_waitcnt lgkmcnt(4)\n"
    "s_add_u32 s64, s68, 0x1c280\n"
    "ds_read_b64_tr_b16 a[64:65], v242 offset:16384\n"
    "ds_read_b64_tr_b16 a[66:67], v242 offset:17408\n"
    "v_mul_f32 v96, v96, v8\n"
    "v_mul_f32 v97, v97, v8\n"
    "v_mul_f32 v98, v98, v8\n"
    "v_mfma_f32_32x32x16_bf16 v[32:47], a[84:87], v[136:139], v[32:47]\n"
    "s_waitcnt lgkmcnt(4)\n"
    "ds_read_b64_tr_b16 a[68:69], v243 offset:16384\n"
    "ds_read_b64_tr_b16 a[70:71], v243 offset:17408\n"
    "buffer_load_dwordx4 v11, s[52:55], s64 offen offset:0 lds\n"
    "v_mul_f32 v99, v99, v8\n"
    "v_mul_f32 v100, v100, v8\n"
    "v_mfma_f32_32x32x16_bf16 v[16:31], a[88:91], v[140:143], v[16:31]\n"
    "s_waitcnt lgkmcnt(4)\n"
    "s_mov_b32 s76, 0x2000\n"
    "ds_read_b64_tr_b16 a[72:73], v242 offset:18432\n"
    "ds_read_b64_tr_b16 a[74:75], v242 offset:19456\n"
    "buffer_store_dwordx4 v[80:83], v0, s[56:59], s76 offen offset:0 sc1\n"
    "v_mfma_f32_32x32x16_bf16 v[32:47], a[92:95], v[140:143], v[32:47]\n"
    "s_waitcnt lgkmcnt(4)\n"
    "ds_read_b64_tr_b16 a[76:77], v243 offset:18432\n"
    "ds_read_b64_tr_b16 a[78:79], v243 offset:19456\n"
    "buffer_load_dwordx4 v12, s[52:55], s64 offen offset:1024 lds\n"
    "buffer_store_dwordx4 v[88:91], v0, s[56:59], s76 offen offset:1024 sc1\n"
    "v_mfma_f32_32x32x16_bf16 v[16:31], a[64:67], v[144:147], v[16:31]\n"
    "s_waitcnt lgkmcnt(4)\n"
    "ds_read_b64_tr_b16 a[80:81], v242 offset:20480\n"
    "ds_read_b64_tr_b16 a[82:83], v242 offset:21504\n"
    "v_mul_f32 v101, v101, v8\n"
    "v_mul_f32 v102, v102, v8\n"
    "v_mul_f32 v103, v103, v8\n"
    "v_cvt_pk_bf16_f32 v96, v96, v97\n"
    "v_mfma_f32_32x32x16_bf16 v[32:47], a[68:71], v[144:147], v[32:47]\n"
    "s_waitcnt lgkmcnt(4)\n"
    "ds_read_b64_tr_b16 a[84:85], v243 offset:20480\n"
    "ds_read_b64_tr_b16 a[86:87], v243 offset:21504\n"
    "buffer_load_dwordx4 v13, s[52:55], s64 offen offset:2048 lds\n"
    "v_cvt_pk_bf16_f32 v97, v98, v99\n"
    "v_cvt_pk_bf16_f32 v98, v100, v101\n"
    "v_mfma_f32_32x32x16_bf16 v[16:31], a[72:75], v[148:151], v[16:31]\n"
    "s_waitcnt lgkmcnt(4)\n"
    "ds_read_b64_tr_b16 a[88:89], v242 offset:22528\n"
    "ds_read_b64_tr_b16 a[90:91], v242 offset:23552\n"
    "v_cvt_pk_bf16_f32 v99, v102, v103\n"
    "v_mul_f32 v104, v104, v8\n"
    "v_mul_f32 v105, v105, v8\n"
    "v_mul_f32 v106, v106, v8\n"
    "v_mfma_f32_32x32x16_bf16 v[32:47], a[76:79], v[148:151], v[32:47]\n"
    "s_waitcnt lgkmcnt(4)\n"
    "ds_read_b64_tr_b16 a[92:93], v243 offset:22528\n"
    "ds_read_b64_tr_b16 a[94:95], v243 offset:23552\n"
    "buffer_load_dwordx4 v14, s[52:55], s64 offen offset:3072 lds\n"
    "v_mul_f32 v107, v107, v8\n"
    "v_mul_f32 v108, v108, v8\n"
    "v_mfma_f32_32x32x16_bf16 v[16:31], a[80:83], v[152:155], v[16:31]\n"
    "s_waitcnt lgkmcnt(4)\n"
    "ds_read_b64_tr_b16 a[64:65], v242 offset:24576\n"
    "ds_read_b64_tr_b16 a[66:67], v242 offset:25600\n"
    "v_mul_f32 v109, v109, v8\n"
    "v_mul_f32 v110, v110, v8\n"
    "v_mul_f32 v111, v111, v8\n"
    "v_cvt_pk_bf16_f32 v104, v104, v105\n"
    "v_mfma_f32_32x32x16_bf16 v[32:47], a[84:87], v[152:155], v[32:47]\n"
    "s_waitcnt lgkmcnt(4)\n"
    "ds_read_b64_tr_b16 a[68:69], v243 offset:24576\n"
    "ds_read_b64_tr_b16 a[70:71], v243 offset:25600\n"
    "v_cvt_pk_bf16_f32 v105, v106, v107\n"
    "v_cvt_pk_bf16_f32 v106, v108, v109\n"
    "v_cvt_pk_bf16_f32 v107, v110, v111\n"
    "v_mfma_f32_32x32x16_bf16 v[16:31], a[88:91], v[156:159], v[16:31]\n"
    "s_waitcnt lgkmcnt(4)\n"
    "ds_read_b64_tr_b16 a[72:73], v242 offset:26624\n"
    "ds_read_b64_tr_b16 a[74:75], v242 offset:27648\n"
    "v_mfma_f32_32x32x16_bf16 v[32:47], a[92:95], v[156:159], v[32:47]\n"
    "s_waitcnt lgkmcnt(4)\n"
    "ds_read_b64_tr_b16 a[76:77], v243 offset:26624\n"
    "ds_read_b64_tr_b16 a[78:79], v243 offset:27648\n"
    "v_mfma_f32_32x32x16_bf16 v[16:31], a[64:67], v[160:163], v[16:31]\n"
    "s_waitcnt lgkmcnt(4)\n"
    "ds_read_b64_tr_b16 a[80:81], v242 offset:28672\n"
    "ds_read_b64_tr_b16 a[82:83], v242 offset:29696\n"
    "v_mfma_f32_32x32x16_bf16 v[32:47], a[68:71], v[160:163], v[32:47]\n"
    "s_waitcnt lgkmcnt(4)\n"
    "s_mov_b32 s76, 0x2800\n"
    "ds_read_b64_tr_b16 a[84:85], v243 offset:28672\n"
    "ds_read_b64_tr_b16 a[86:87], v243 offset:29696\n"
    "buffer_store_dwordx4 v[96:99], v0, s[56:59], s76 offen offset:0 sc1\n"
    "v_mfma_f32_32x32x16_bf16 v[16:31], a[72:75], v[164:167], v[16:31]\n"
    "s_waitcnt lgkmcnt(4)\n"
    "ds_read_b64_tr_b16 a[88:89], v242 offset:30720\n"
    "ds_read_b64_tr_b16 a[90:91], v242 offset:31744\n"
    "buffer_store_dwordx4 v[104:107], v0, s[56:59], s76 offen offset:1024 sc1\n"
    "v_mfma_f32_32x32x16_bf16 v[32:47], a[76:79], v[164:167], v[32:47]\n"
    "s_waitcnt lgkmcnt(4)\n"
    "ds_read_b64_tr_b16 a[92:93], v243 offset:30720\n"
    "ds_read_b64_tr_b16 a[94:95], v243 offset:31744\n"
    "v_mfma_f32_32x32x16_bf16 v[16:31], a[80:83], v[168:171], v[16:31]\n"
    "s_waitcnt vmcnt(14) lgkmcnt(4)\n"
    "s_barrier\n"
    "ds_read_b64_tr_b16 a[64:65], v242 offset:32768\n"
    "ds_read_b64_tr_b16 a[66:67], v242 offset:33792\n"
    "v_mfma_f32_32x32x16_bf16 v[32:47], a[84:87], v[168:171], v[32:47]\n"
    "s_waitcnt lgkmcnt(4)\n"
    "ds_read_b64_tr_b16 a[68:69], v243 offset:32768\n"
    "ds_read_b64_tr_b16 a[70:71], v243 offset:33792\n"
    "v_mfma_f32_32x32x16_bf16 v[16:31], a[88:91], v[172:175], v[16:31]\n"
    "s_waitcnt lgkmcnt(4)\n"
    "ds_read_b64_tr_b16 a[72:73], v242 offset:34816\n"
    "ds_read_b64_tr_b16 a[74:75], v242 offset:35840\n"
    "v_mfma_f32_32x32x16_bf16 v[32:47], a[92:95], v[172:175], v[32:47]\n"
    "s_waitcnt lgkmcnt(4)\n"
    "ds_read_b64_tr_b16 a[76:77], v243 offset:34816\n"
    "ds_read_b64_tr_b16 a[78:79], v243 offset:35840\n"
    "v_mfma_f32_32x32x16_bf16 v[48:63], a[64:67], v[112:115], 0\n"
    "s_waitcnt lgkmcnt(4)\n"
    "ds_read_b64_tr_b16 a[80:81], v242 offset:36864\n"
    "ds_read_b64_tr_b16 a[82:83], v242 offset:37888\n"
    "v_mfma_f32_32x32x16_bf16 v[64:79], a[68:71], v[112:115], 0\n"
    "s_waitcnt lgkmcnt(4)\n"
    "ds_read_b64_tr_b16 a[84:85], v243 offset:36864\n"
    "ds_read_b64_tr_b16 a[86:87], v243 offset:37888\n"
    "v_mul_f32 v16, v16, v8\n"
    "v_mul_f32 v17, v17, v8\n"
    "v_mul_f32 v18, v18, v8\n"
    "v_mul_f32 v19, v19, v8\n"
    "v_mfma_f32_32x32x16_bf16 v[48:63], a[72:75], v[116:119], v[48:63]\n"
    "s_waitcnt lgkmcnt(4)\n"
    "s_add_u32 m0, s65, 0x8000\n"
    "ds_read_b64_tr_b16 a[88:89], v242 offset:38912\n"
    "ds_read_b64_tr_b16 a[90:91], v242 offset:39936\n"
    "v_mul_f32 v20, v20, v8\n"
    "v_mul_f32 v21, v21, v8\n"
    "v_mul_f32 v22, v22, v8\n"
    "v_mfma_f32_32x32x16_bf16 v[64:79], a[76:79], v[116:119], v[64:79]\n"
    "s_waitcnt lgkmcnt(4)\n"
    "s_add_u32 s64, s68, 0x300\n"
    "ds_read_b64_tr_b16 a[92:93], v243 offset:38912\n"
    "ds_read_b64_tr_b16 a[94:95], v243 offset:39936\n"
    "v_mul_f32 v23, v23, v8\n"
    "v_cvt_pk_bf16_f32 v16, v16, v17\n"
    "v_cvt_pk_bf16_f32 v17, v18, v19\n"
    "v_mfma_f32_32x32x16_bf16 v[48:63], a[80:83], v[120:123], v[48:63]\n"
    "s_waitcnt lgkmcnt(4)\n"
    "ds_read_b64_tr_b16 a[64:65], v242 offset:40960\n"
    "ds_read_b64_tr_b16 a[66:67], v242 offset:41984\n"
    "buffer_load_dwordx4 v11, s[52:55], s64 offen offset:0 lds\n"
    "v_cvt_pk_bf16_f32 v18, v20, v21\n"
    "v_cvt_pk_bf16_f32 v19, v22, v23\n"
    "v_mfma_f32_32x32x16_bf16 v[64:79], a[84:87], v[120:123], v[64:79]\n"
    "s_waitcnt lgkmcnt(4)\n"
    "ds_read_b64_tr_b16 a[68:69], v243 offset:40960\n"
    "ds_read_b64_tr_b16 a[70:71], v243 offset:41984\n"
    "v_mul_f32 v24, v24, v8\n"
    "v_mul_f32 v25, v25, v8\n"
    "v_mul_f32 v26, v26, v8\n"
    "v_mul_f32 v27, v27, v8\n"
    "v_mfma_f32_32x32x16_bf16 v[48:63], a[88:91], v[124:127], v[48:63]\n"
    "s_waitcnt lgkmcnt(4)\n"
    "ds_read_b64_tr_b16 a[72:73], v242 offset:43008\n"
    "ds_read_b64_tr_b16 a[74:75], v242 offset:44032\n"
    "buffer_load_dwordx4 v12, s[52:55], s64 offen offset:1024 lds\n"
    "v_mul_f32 v28, v28, v8\n"
    "v_mul_f32 v29, v29, v8\n"
    "v_mfma_f32_32x32x16_bf16 v[64:79], a[92:95], v[124:127], v[64:79]\n"
    "s_waitcnt lgkmcnt(4)\n"
    "ds_read_b64_tr_b16 a[76:77], v243 offset:43008\n"
    "ds_read_b64_tr_b16 a[78:79], v243 offset:44032\n"
    "v_mul_f32 v30, v30, v8\n"
    "v_mul_f32 v31, v31, v8\n"
    "v_cvt_pk_bf16_f32 v24, v24, v25\n"
    "v_cvt_pk_bf16_f32 v25, v26, v27\n"
    "v_mfma_f32_32x32x16_bf16 v[48:63], a[64:67], v[128:131], v[48:63]\n"
    "s_waitcnt lgkmcnt(4)\n"
    "ds_read_b64_tr_b16 a[80:81], v242 offset:45056\n"
    "ds_read_b64_tr_b16 a[82:83], v242 offset:46080\n"
    "buffer_load_dwordx4 v13, s[52:55], s64 offen offset:2048 lds\n"
    "v_cvt_pk_bf16_f32 v26, v28, v29\n"
    "v_cvt_pk_bf16_f32 v27, v30, v31\n"
    "v_mfma_f32_32x32x16_bf16 v[64:79], a[68:71], v[128:131], v[64:79]\n"
    "s_waitcnt lgkmcnt(4)\n"
    "ds_read_b64_tr_b16 a[84:85], v243 offset:45056\n"
    "ds_read_b64_tr_b16 a[86:87], v243 offset:46080\n"
    "v_mfma_f32_32x32x16_bf16 v[48:63], a[72:75], v[132:135], v[48:63]\n"
    "s_waitcnt lgkmcnt(4)\n"
    "ds_read_b64_tr_b16 a[88:89], v242 offset:47104\n"
    "ds_read_b64_tr_b16 a[90:91], v242 offset:48128\n"
    "buffer_load_dwordx4 v14, s[52:55], s64 offen offset:3072 lds\n"
    "v_mfma_f32_32x32x16_bf16 v[64:79], a[76:79], v[132:135], v[64:79]\n"
    "s_waitcnt lgkmcnt(4)\n"
    "s_add_u32 m0, s65, 0xc000\n"
    "ds_read_b64_tr_b16 a[92:93], v243 offset:47104\n"
    "ds_read_b64_tr_b16 a[94:95], v243 offset:48128\n"
    "v_mfma_f32_32x32x16_bf16 v[48:63], a[80:83], v[136:139], v[48:63]\n"
    "s_waitcnt lgkmcnt(4)\n"
    "s_add_u32 s64, s68, 0x1c300\n"
    "ds_read_b64_tr_b16 a[64:65], v242 offset:49152\n"
    "ds_read_b64_tr_b16 a[66:67], v242 offset:50176\n"
    "v_mul_f32 v32, v32, v8\n"
    "v_mul_f32 v33, v33, v8\n"
    "v_mul_f32 v34, v34, v8\n"
    "v_mfma_f32_32x32x16_bf16 v[64:79], a[84:87], v[136:139], v[64:79]\n"
    "s_waitcnt lgkmcnt(4)\n"
    "ds_read_b64_tr_b16 a[68:69], v243 offset:49152\n"
    "ds_read_b64_tr_b16 a[70:71], v243 offset:50176\n"
    "buffer_load_dwordx4 v11, s[52:55], s64 offen offset:0 lds\n"
    "v_mul_f32 v35, v35, v8\n"
    "v_mul_f32 v36, v36, v8\n"
    "v_mfma_f32_32x32x16_bf16 v[48:63], a[88:91], v[140:143], v[48:63]\n"
    "s_waitcnt lgkmcnt(4)\n"
    "s_mov_b32 s76, 0x3000\n"
    "ds_read_b64_tr_b16 a[72:73], v242 offset:51200\n"
    "ds_read_b64_tr_b16 a[74:75], v242 offset:52224\n"
    "buffer_store_dwordx4 v[16:19], v0, s[56:59], s76 offen offset:0 sc1\n"
    "v_mfma_f32_32x32x16_bf16 v[64:79], a[92:95], v[140:143], v[64:79]\n"
    "s_waitcnt lgkmcnt(4)\n"
    "ds_read_b64_tr_b16 a[76:77], v243 offset:51200\n"
    "ds_read_b64_tr_b16 a[78:79], v243 offset:52224\n"
    "buffer_load_dwordx4 v12, s[52:55], s64 offen offset:1024 lds\n"
    "buffer_store_dwordx4 v[24:27], v0, s[56:59], s76 offen offset:1024 sc1\n"
    "v_mfma_f32_32x32x16_bf16 v[48:63], a[64:67], v[144:147], v[48:63]\n"
    "s_waitcnt lgkmcnt(4)\n"
    "ds_read_b64_tr_b16 a[80:81], v242 offset:53248\n"
    "ds_read_b64_tr_b16 a[82:83], v242 offset:54272\n"
    "v_mul_f32 v37, v37, v8\n"
    "v_mul_f32 v38, v38, v8\n"
    "v_mul_f32 v39, v39, v8\n"
    "v_cvt_pk_bf16_f32 v32, v32, v33\n"
    "v_mfma_f32_32x32x16_bf16 v[64:79], a[68:71], v[144:147], v[64:79]\n"
    "s_waitcnt lgkmcnt(4)\n"
    "ds_read_b64_tr_b16 a[84:85], v243 offset:53248\n"
    "ds_read_b64_tr_b16 a[86:87], v243 offset:54272\n"
    "buffer_load_dwordx4 v13, s[52:55], s64 offen offset:2048 lds\n"
    "v_cvt_pk_bf16_f32 v33, v34, v35\n"
    "v_cvt_pk_bf16_f32 v34, v36, v37\n"
    "v_mfma_f32_32x32x16_bf16 v[48:63], a[72:75], v[148:151], v[48:63]\n"
    "s_waitcnt lgkmcnt(4)\n"
    "ds_read_b64_tr_b16 a[88:89], v242 offset:55296\n"
    "ds_read_b64_tr_b16 a[90:91], v242 offset:56320\n"
    "v_cvt_pk_bf16_f32 v35, v38, v39\n"
    "v_mul_f32 v40, v40, v8\n"
    "v_mul_f32 v41, v41, v8\n"
    "v_mul_f32 v42, v42, v8\n"
    "v_mfma_f32_32x32x16_bf16 v[64:79], a[76:79], v[148:151], v[64:79]\n"
    "s_waitcnt lgkmcnt(4)\n"
    "ds_read_b64_tr_b16 a[92:93], v243 offset:55296\n"
    "ds_read_b64_tr_b16 a[94:95], v243 offset:56320\n"
    "buffer_load_dwordx4 v14, s[52:55], s64 offen offset:3072 lds\n"
    "v_mul_f32 v43, v43, v8\n"
    "v_mul_f32 v44, v44, v8\n"
    "v_mfma_f32_32x32x16_bf16 v[48:63], a[80:83], v[152:155], v[48:63]\n"
    "s_waitcnt lgkmcnt(4)\n"
    "ds_read_b64_tr_b16 a[64:65], v242 offset:57344\n"
    "ds_read_b64_tr_b16 a[66:67], v242 offset:58368\n"
    "v_mul_f32 v45, v45, v8\n"
    "v_mul_f32 v46, v46, v8\n"
    "v_mul_f32 v47, v47, v8\n"
    "v_cvt_pk_bf16_f32 v40, v40, v41\n"
    "v_mfma_f32_32x32x16_bf16 v[64:79], a[84:87], v[152:155], v[64:79]\n"
    "s_waitcnt lgkmcnt(4)\n"
    "ds_read_b64_tr_b16 a[68:69], v243 offset:57344\n"
    "ds_read_b64_tr_b16 a[70:71], v243 offset:58368\n"
    "v_cvt_pk_bf16_f32 v41, v42, v43\n"
    "v_cvt_pk_bf16_f32 v42, v44, v45\n"
    "v_cvt_pk_bf16_f32 v43, v46, v47\n"
    "v_mfma_f32_32x32x16_bf16 v[48:63], a[88:91], v[156:159], v[48:63]\n"
    "s_waitcnt lgkmcnt(4)\n"
    "ds_read_b64_tr_b16 a[72:73], v242 offset:59392\n"
    "ds_read_b64_tr_b16 a[74:75], v242 offset:60416\n"
    "v_mfma_f32_32x32x16_bf16 v[64:79], a[92:95], v[156:159], v[64:79]\n"
    "s_waitcnt lgkmcnt(4)\n"
    "ds_read_b64_tr_b16 a[76:77], v243 offset:59392\n"
    "ds_read_b64_tr_b16 a[78:79], v243 offset:60416\n"
    "v_mfma_f32_32x32x16_bf16 v[48:63], a[64:67], v[160:163], v[48:63]\n"
    "s_waitcnt lgkmcnt(4)\n"
    "ds_read_b64_tr_b16 a[80:81], v242 offset:61440\n"
    "ds_read_b64_tr_b16 a[82:83], v242 offset:62464\n"
    "v_mfma_f32_32x32x16_bf16 v[64:79], a[68:71], v[160:163], v[64:79]\n"
    "s_waitcnt lgkmcnt(4)\n"
    "s_mov_b32 s76, 0x3800\n"
    "ds_read_b64_tr_b16 a[84:85], v243 offset:61440\n"
    "ds_read_b64_tr_b16 a[86:87], v243 offset:62464\n"
    "buffer_store_dwordx4 v[32:35], v0, s[56:59], s76 offen offset:0 sc1\n"
    "v_mfma_f32_32x32x16_bf16 v[48:63], a[72:75], v[164:167], v[48:63]\n"
    "s_waitcnt lgkmcnt(4)\n"
    "ds_read_b64_tr_b16 a[88:89], v242 offset:63488\n"
    "ds_read_b64_tr_b16 a[90:91], v242 offset:64512\n"
    "buffer_store_dwordx4 v[40:43], v0, s[56:59], s76 offen offset:1024 sc1\n"
    "v_mfma_f32_32x32x16_bf16 v[64:79], a[76:79], v[164:167], v[64:79]\n"
    "s_waitcnt lgkmcnt(4)\n"
    "ds_read_b64_tr_b16 a[92:93], v243 offset:63488\n"
    "ds_read_b64_tr_b16 a[94:95], v243 offset:64512\n"
    "v_mfma_f32_32x32x16_bf16 v[48:63], a[80:83], v[168:171], v[48:63]\n"
    "s_waitcnt vmcnt(14) lgkmcnt(4)\n"
    "s_barrier\n"
    "ds_read_b64_tr_b16 a[64:65], v240 offset:0\n"
    "ds_read_b64_tr_b16 a[66:67], v240 offset:1024\n"
    "v_mfma_f32_32x32x16_bf16 v[64:79], a[84:87], v[168:171], v[64:79]\n"
    "s_waitcnt lgkmcnt(4)\n"
    "ds_read_b64_tr_b16 a[68:69], v241 offset:0\n"
    "ds_read_b64_tr_b16 a[70:71], v241 offset:1024\n"
    "v_mfma_f32_32x32x16_bf16 v[48:63], a[88:91], v[172:175], v[48:63]\n"
    "s_waitcnt lgkmcnt(4)\n"
    "ds_read_b64_tr_b16 a[72:73], v240 offset:2048\n"
    "ds_read_b64_tr_b16 a[74:75], v240 offset:3072\n"
    "v_mfma_f32_32x32x16_bf16 v[64:79], a[92:95], v[172:175], v[64:79]\n"
    "s_waitcnt lgkmcnt(4)\n"
    "ds_read_b64_tr_b16 a[76:77], v241 offset:2048\n"
    "ds_read_b64_tr_b16 a[78:79], v241 offset:3072\n"
    "v_mfma_f32_32x32x16_bf16 v[80:95], a[64:67], v[112:115], 0\n"
    "s_waitcnt lgkmcnt(4)\n"
    "ds_read_b64_tr_b16 a[80:81], v240 offset:4096\n"
    "ds_read_b64_tr_b16 a[82:83], v240 offset:5120\n"
    "v_mfma_f32_32x32x16_bf16 v[96:111], a[68:71], v[112:115], 0\n"
    "s_waitcnt lgkmcnt(4)\n"
    "ds_read_b64_tr_b16 a[84:85], v241 offset:4096\n"
    "ds_read_b64_tr_b16 a[86:87], v241 offset:5120\n"
    "v_mul_f32 v48, v48, v8\n"
    "v_mul_f32 v49, v49, v8\n"
    "v_mul_f32 v50, v50, v8\n"
    "v_mul_f32 v51, v51, v8\n"
    "v_mfma_f32_32x32x16_bf16 v[80:95], a[72:75], v[116:119], v[80:95]\n"
    "s_waitcnt lgkmcnt(4)\n"
    "ds_read_b64_tr_b16 a[88:89], v240 offset:6144\n"
    "ds_read_b64_tr_b16 a[90:91], v240 offset:7168\n"
    "v_mul_f32 v52, v52, v8\n"
    "v_mul_f32 v53, v53, v8\n"
    "v_mul_f32 v54, v54, v8\n"
    "v_mul_f32 v55, v55, v8\n"
    "v_mfma_f32_32x32x16_bf16 v[96:111], a[76:79], v[116:119], v[96:111]\n"
    "s_waitcnt lgkmcnt(4)\n"
    "ds_read_b64_tr_b16 a[92:93], v241 offset:6144\n"
    "ds_read_b64_tr_b16 a[94:95], v241 offset:7168\n"
    "v_cvt_pk_bf16_f32 v48, v48, v49\n"
    "v_cvt_pk_bf16_f32 v49, v50, v51\n"
    "v_cvt_pk_bf16_f32 v50, v52, v53\n"
    "v_cvt_pk_bf16_f32 v51, v54, v55\n"
    "v_mfma_f32_32x32x16_bf16 v[80:95], a[80:83], v[120:123], v[80:95]\n"
    "s_waitcnt lgkmcnt(4)\n"
    "ds_read_b64_tr_b16 a[64:65], v240 offset:8192\n"
    "ds_read_b64_tr_b16 a[66:67], v240 offset:9216\n"
    "v_mul_f32 v56, v56, v8\n"
    "v_mul_f32 v57, v57, v8\n"
    "v_mul_f32 v58, v58, v8\n"
    "v_mul_f32 v59, v59, v8\n"
    "v_mfma_f32_32x32x16_bf16 v[96:111], a[84:87], v[120:123], v[96:111]\n"
    "s_waitcnt lgkmcnt(4)\n"
    "ds_read_b64_tr_b16 a[68:69], v241 offset:8192\n"
    "ds_read_b64_tr_b16 a[70:71], v241 offset:9216\n"
    "v_mul_f32 v60, v60, v8\n"
    "v_mul_f32 v61, v61, v8\n"
    "v_mul_f32 v62, v62, v8\n"
    "v_mul_f32 v63, v63, v8\n"
    "v_mfma_f32_32x32x16_bf16 v[80:95], a[88:91], v[124:127], v[80:95]\n"
    "s_waitcnt lgkmcnt(4)\n"
    "ds_read_b64_tr_b16 a[72:73], v240 offset:10240\n"
    "ds_read_b64_tr_b16 a[74:75], v240 offset:11264\n"
    "v_cvt_pk_bf16_f32 v56, v56, v57\n"
    "v_cvt_pk_bf16_f32 v57, v58, v59\n"
    "v_cvt_pk_bf16_f32 v58, v60, v61\n"
    "v_cvt_pk_bf16_f32 v59, v62, v63\n"
    "v_mfma_f32_32x32x16_bf16 v[96:111], a[92:95], v[124:127], v[96:111]\n"
    "s_waitcnt lgkmcnt(4)\n"
    "ds_read_b64_tr_b16 a[76:77], v241 offset:10240\n"
    "ds_read_b64_tr_b16 a[78:79], v241 offset:11264\n"
    "v_mfma_f32_32x32x16_bf16 v[80:95], a[64:67], v[128:131], v[80:95]\n"
    "s_waitcnt lgkmcnt(4)\n"
    "ds_read_b64_tr_b16 a[80:81], v240 offset:12288\n"
    "ds_read_b64_tr_b16 a[82:83], v240 offset:13312\n"
    "v_mfma_f32_32x32x16_bf16 v[96:111], a[68:71], v[128:131], v[96:111]\n"
    "s_waitcnt lgkmcnt(4)\n"
    "ds_read_b64_tr_b16 a[84:85], v241 offset:12288\n"
    "ds_read_b64_tr_b16 a[86:87], v241 offset:13312\n"
    "v_mfma_f32_32x32x16_bf16 v[80:95], a[72:75], v[132:135], v[80:95]\n"
    "s_waitcnt lgkmcnt(4)\n"
    "ds_read_b64_tr_b16 a[88:89], v240 offset:14336\n"
    "ds_read_b64_tr_b16 a[90:91], v240 offset:15360\n"
    "v_mfma_f32_32x32x16_bf16 v[96:111], a[76:79], v[132:135], v[96:111]\n"
    "s_waitcnt lgkmcnt(4)\n"
    "ds_read_b64_tr_b16 a[92:93], v241 offset:14336\n"
    "ds_read_b64_tr_b16 a[94:95], v241 offset:15360\n"
    "v_mfma_f32_32x32x16_bf16 v[80:95], a[80:83], v[136:139], v[80:95]\n"
    "s_waitcnt lgkmcnt(4)\n"
    "ds_read_b64_tr_b16 a[64:65], v240 offset:16384\n"
    "ds_read_b64_tr_b16 a[66:67], v240 offset:17408\n"
    "v_mul_f32 v64, v64, v8\n"
    "v_mul_f32 v65, v65, v8\n"
    "v_mul_f32 v66, v66, v8\n"
    "v_mul_f32 v67, v67, v8\n"
    "v_mfma_f32_32x32x16_bf16 v[96:111], a[84:87], v[136:139], v[96:111]\n"
    "s_waitcnt lgkmcnt(4)\n"
    "ds_read_b64_tr_b16 a[68:69], v241 offset:16384\n"
    "ds_read_b64_tr_b16 a[70:71], v241 offset:17408\n"
    "v_mul_f32 v68, v68, v8\n"
    "v_mul_f32 v69, v69, v8\n"
    "v_mul_f32 v70, v70, v8\n"
    "v_mul_f32 v71, v71, v8\n"
    "v_mfma_f32_32x32x16_bf16 v[80:95], a[88:91], v[140:143], v[80:95]\n"
    "s_waitcnt lgkmcnt(4)\n"
    "s_mov_b32 s76, 0x4000\n"
    "ds_read_b64_tr_b16 a[72:73], v240 offset:18432\n"
    "ds_read_b64_tr_b16 a[74:75], v240 offset:19456\n"
    "buffer_store_dwordx4 v[48:51], v0, s[56:59], s76 offen offset:0 sc1\n"
    "v_mfma_f32_32x32x16_bf16 v[96:111], a[92:95], v[140:143], v[96:111]\n"
    "s_waitcnt lgkmcnt(4)\n"
    "ds_read_b64_tr_b16 a[76:77], v241 offset:18432\n"
    "ds_read_b64_tr_b16 a[78:79], v241 offset:19456\n"
    "buffer_store_dwordx4 v[56:59], v0, s[56:59], s76 offen offset:1024 sc1\n"
    "v_mfma_f32_32x32x16_bf16 v[80:95], a[64:67], v[144:147], v[80:95]\n"
    "s_waitcnt lgkmcnt(4)\n"
    "ds_read_b64_tr_b16 a[80:81], v240 offset:20480\n"
    "ds_read_b64_tr_b16 a[82:83], v240 offset:21504\n"
    "v_cvt_pk_bf16_f32 v64, v64, v65\n"
    "v_cvt_pk_bf16_f32 v65, v66, v67\n"
    "v_cvt_pk_bf16_f32 v66, v68, v69\n"
    "v_cvt_pk_bf16_f32 v67, v70, v71\n"
    "v_mfma_f32_32x32x16_bf16 v[96:111], a[68:71], v[144:147], v[96:111]\n"
    "s_waitcnt lgkmcnt(4)\n"
    "ds_read_b64_tr_b16 a[84:85], v241 offset:20480\n"
    "ds_read_b64_tr_b16 a[86:87], v241 offset:21504\n"
    "v_mul_f32 v72, v72, v8\n"
    "v_mul_f32 v73, v73, v8\n"
    "v_mul_f32 v74, v74, v8\n"
    "v_mul_f32 v75, v75, v8\n"
    "v_mfma_f32_32x32x16_bf16 v[80:95], a[72:75], v[148:151], v[80:95]\n"
    "s_waitcnt lgkmcnt(4)\n"
    "ds_read_b64_tr_b16 a[88:89], v240 offset:22528\n"
    "ds_read_b64_tr_b16 a[90:91], v240 offset:23552\n"
    "v_mul_f32 v76, v76, v8\n"
    "v_mul_f32 v77, v77, v8\n"
    "v_mul_f32 v78, v78, v8\n"
    "v_mul_f32 v79, v79, v8\n"
    "v_mfma_f32_32x32x16_bf16 v[96:111], a[76:79], v[148:151], v[96:111]\n"
    "s_waitcnt lgkmcnt(4)\n"
    "ds_read_b64_tr_b16 a[92:93], v241 offset:22528\n"
    "ds_read_b64_tr_b16 a[94:95], v241 offset:23552\n"
    "v_cvt_pk_bf16_f32 v72, v72, v73\n"
    "v_cvt_pk_bf16_f32 v73, v74, v75\n"
    "v_cvt_pk_bf16_f32 v74, v76, v77\n"
    "v_cvt_pk_bf16_f32 v75, v78, v79\n"
    "v_mfma_f32_32x32x16_bf16 v[80:95], a[80:83], v[152:155], v[80:95]\n"
    "s_waitcnt lgkmcnt(4)\n"
    "ds_read_b64_tr_b16 a[64:65], v240 offset:24576\n"
    "ds_read_b64_tr_b16 a[66:67], v240 offset:25600\n"
    "v_mfma_f32_32x32x16_bf16 v[96:111], a[84:87], v[152:155], v[96:111]\n"
    "s_waitcnt lgkmcnt(4)\n"
    "ds_read_b64_tr_b16 a[68:69], v241 offset:24576\n"
    "ds_read_b64_tr_b16 a[70:71], v241 offset:25600\n"
    "v_mfma_f32_32x32x16_bf16 v[80:95], a[88:91], v[156:159], v[80:95]\n"
    "s_waitcnt lgkmcnt(4)\n"
    "ds_read_b64_tr_b16 a[72:73], v240 offset:26624\n"
    "ds_read_b64_tr_b16 a[74:75], v240 offset:27648\n"
    "v_mfma_f32_32x32x16_bf16 v[96:111], a[92:95], v[156:159], v[96:111]\n"
    "s_waitcnt lgkmcnt(4)\n"
    "ds_read_b64_tr_b16 a[76:77], v241 offset:26624\n"
    "ds_read_b64_tr_b16 a[78:79], v241 offset:27648\n"
    "v_mfma_f32_32x32x16_bf16 v[80:95], a[64:67], v[160:163], v[80:95]\n"
    "s_waitcnt lgkmcnt(4)\n"
    "ds_read_b64_tr_b16 a[80:81], v240 offset:28672\n"
    "ds_read_b64_tr_b16 a[82:83], v240 offset:29696\n"
    "v_mfma_f32_32x32x16_bf16 v[96:111], a[68:71], v[160:163], v[96:111]\n"
    "s_waitcnt lgkmcnt(4)\n"
    "s_mov_b32 s76, 0x4800\n"
    "ds_read_b64_tr_b16 a[84:85], v241 offset:28672\n"
    "ds_read_b64_tr_b16 a[86:87], v241 offset:29696\n"
    "buffer_store_dwordx4 v[64:67], v0, s[56:59], s76 offen offset:0 sc1\n"
    "v_mfma_f32_32x32x16_bf16 v[80:95], a[72:75], v[164:167], v[80:95]\n"
    "s_waitcnt lgkmcnt(4)\n"
    "ds_read_b64_tr_b16 a[88:89], v240 offset:30720\n"
    "ds_read_b64_tr_b16 a[90:91], v240 offset:31744\n"
    "buffer_store_dwordx4 v[72:75], v0, s[56:59], s76 offen offset:1024 sc1\n"
    "v_mfma_f32_32x32x16_bf16 v[96:111], a[76:79], v[164:167], v[96:111]\n"
    "s_waitcnt lgkmcnt(4)\n"
    "ds_read_b64_tr_b16 a[92:93], v241 offset:30720\n"
    "ds_read_b64_tr_b16 a[94:95], v241 offset:31744\n"
    "v_mfma_f32_32x32x16_bf16 v[80:95], a[80:83], v[168:171], v[80:95]\n"
    "s_waitcnt vmcnt(6) lgkmcnt(4)\n"
    "s_barrier\n"
    "ds_read_b64_tr_b16 a[64:65], v240 offset:32768\n"
    "ds_read_b64_tr_b16 a[66:67], v240 offset:33792\n"
    "v_mfma_f32_32x32x16_bf16 v[96:111], a[84:87], v[168:171], v[96:111]\n"
    "s_waitcnt lgkmcnt(4)\n"
    "ds_read_b64_tr_b16 a[68:69], v241 offset:32768\n"
    "ds_read_b64_tr_b16 a[70:71], v241 offset:33792\n"
    "v_mfma_f32_32x32x16_bf16 v[80:95], a[88:91], v[172:175], v[80:95]\n"
    "s_waitcnt lgkmcnt(4)\n"
    "ds_read_b64_tr_b16 a[72:73], v240 offset:34816\n"
    "ds_read_b64_tr_b16 a[74:75], v240 offset:35840\n"
    "v_mfma_f32_32x32x16_bf16 v[96:111], a[92:95], v[172:175], v[96:111]\n"
    "s_waitcnt lgkmcnt(4)\n"
    "ds_read_b64_tr_b16 a[76:77], v241 offset:34816\n"
    "ds_read_b64_tr_b16 a[78:79], v241 offset:35840\n"
    "v_mfma_f32_32x32x16_bf16 v[16:31], a[64:67], v[112:115], 0\n"
    "s_waitcnt lgkmcnt(4)\n"
    "ds_read_b64_tr_b16 a[80:81], v240 offset:36864\n"
    "ds_read_b64_tr_b16 a[82:83], v240 offset:37888\n"
    "v_mfma_f32_32x32x16_bf16 v[32:47], a[68:71], v[112:115], 0\n"
    "s_waitcnt lgkmcnt(4)\n"
    "ds_read_b64_tr_b16 a[84:85], v241 offset:36864\n"
    "ds_read_b64_tr_b16 a[86:87], v241 offset:37888\n"
    "v_mul_f32 v80, v80, v8\n"
    "v_mul_f32 v81, v81, v8\n"
    "v_mul_f32 v82, v82, v8\n"
    "v_mul_f32 v83, v83, v8\n"
    "v_mfma_f32_32x32x16_bf16 v[16:31], a[72:75], v[116:119], v[16:31]\n"
    "s_waitcnt lgkmcnt(4)\n"
    "ds_read_b64_tr_b16 a[88:89], v240 offset:38912\n"
    "ds_read_b64_tr_b16 a[90:91], v240 offset:39936\n"
    "v_mul_f32 v84, v84, v8\n"
    "v_mul_f32 v85, v85, v8\n"
    "v_mul_f32 v86, v86, v8\n"
    "v_mul_f32 v87, v87, v8\n"
    "v_mfma_f32_32x32x16_bf16 v[32:47], a[76:79], v[116:119], v[32:47]\n"
    "s_waitcnt lgkmcnt(4)\n"
    "ds_read_b64_tr_b16 a[92:93], v241 offset:38912\n"
    "ds_read_b64_tr_b16 a[94:95], v241 offset:39936\n"
    "v_cvt_pk_bf16_f32 v80, v80, v81\n"
    "v_cvt_pk_bf16_f32 v81, v82, v83\n"
    "v_cvt_pk_bf16_f32 v82, v84, v85\n"
    "v_cvt_pk_bf16_f32 v83, v86, v87\n"
    "v_mfma_f32_32x32x16_bf16 v[16:31], a[80:83], v[120:123], v[16:31]\n"
    "s_waitcnt lgkmcnt(4)\n"
    "ds_read_b64_tr_b16 a[64:65], v240 offset:40960\n"
    "ds_read_b64_tr_b16 a[66:67], v240 offset:41984\n"
    "v_mul_f32 v88, v88, v8\n"
    "v_mul_f32 v89, v89, v8\n"
    "v_mul_f32 v90, v90, v8\n"
    "v_mul_f32 v91, v91, v8\n"
    "v_mfma_f32_32x32x16_bf16 v[32:47], a[84:87], v[120:123], v[32:47]\n"
    "s_waitcnt lgkmcnt(4)\n"
    "ds_read_b64_tr_b16 a[68:69], v241 offset:40960\n"
    "ds_read_b64_tr_b16 a[70:71], v241 offset:41984\n"
    "v_mul_f32 v92, v92, v8\n"
    "v_mul_f32 v93, v93, v8\n"
    "v_mul_f32 v94, v94, v8\n"
    "v_mul_f32 v95, v95, v8\n"
    "v_mfma_f32_32x32x16_bf16 v[16:31], a[88:91], v[124:127], v[16:31]\n"
    "s_waitcnt lgkmcnt(4)\n"
    "ds_read_b64_tr_b16 a[72:73], v240 offset:43008\n"
    "ds_read_b64_tr_b16 a[74:75], v240 offset:44032\n"
    "v_cvt_pk_bf16_f32 v88, v88, v89\n"
    "v_cvt_pk_bf16_f32 v89, v90, v91\n"
    "v_cvt_pk_bf16_f32 v90, v92, v93\n"
    "v_cvt_pk_bf16_f32 v91, v94, v95\n"
    "v_mfma_f32_32x32x16_bf16 v[32:47], a[92:95], v[124:127], v[32:47]\n"
    "s_waitcnt lgkmcnt(4)\n"
    "ds_read_b64_tr_b16 a[76:77], v241 offset:43008\n"
    "ds_read_b64_tr_b16 a[78:79], v241 offset:44032\n"
    "v_mfma_f32_32x32x16_bf16 v[16:31], a[64:67], v[128:131], v[16:31]\n"
    "s_waitcnt lgkmcnt(4)\n"
    "ds_read_b64_tr_b16 a[80:81], v240 offset:45056\n"
    "ds_read_b64_tr_b16 a[82:83], v240 offset:46080\n"
    "v_mfma_f32_32x32x16_bf16 v[32:47], a[68:71], v[128:131], v[32:47]\n"
    "s_waitcnt lgkmcnt(4)\n"
    "ds_read_b64_tr_b16 a[84:85], v241 offset:45056\n"
    "ds_read_b64_tr_b16 a[86:87], v241 offset:46080\n"
    "v_mfma_f32_32x32x16_bf16 v[16:31], a[72:75], v[132:135], v[16:31]\n"
    "s_waitcnt lgkmcnt(4)\n"
    "ds_read_b64_tr_b16 a[88:89], v240 offset:47104\n"
    "ds_read_b64_tr_b16 a[90:91], v240 offset:48128\n"
    "v_mfma_f32_32x32x16_bf16 v[32:47], a[76:79], v[132:135], v[32:47]\n"
    "s_waitcnt lgkmcnt(4)\n"
    "ds_read_b64_tr_b16 a[92:93], v241 offset:47104\n"
    "ds_read_b64_tr_b16 a[94:95], v241 offset:48128\n"
    "v_mfma_f32_32x32x16_bf16 v[16:31], a[80:83], v[136:139], v[16:31]\n"
    "s_waitcnt lgkmcnt(4)\n"
    "ds_read_b64_tr_b16 a[64:65], v240 offset:49152\n"
    "ds_read_b64_tr_b16 a[66:67], v240 offset:50176\n"
    "v_mul_f32 v96, v96, v8\n"
    "v_mul_f32 v97, v97, v8\n"
    "v_mul_f32 v98, v98, v8\n"
    "v_mul_f32 v99, v99, v8\n"
    "v_mfma_f32_32x32x16_bf16 v[32:47], a[84:87], v[136:139], v[32:47]\n"
    "s_waitcnt lgkmcnt(4)\n"
    "ds_read_b64_tr_b16 a[68:69], v241 offset:49152\n"
    "ds_read_b64_tr_b16 a[70:71], v241 offset:50176\n"
    "v_mul_f32 v100, v100, v8\n"
    "v_mul_f32 v101, v101, v8\n"
    "v_mul_f32 v102, v102, v8\n"
    "v_mul_f32 v103, v103, v8\n"
    "v_mfma_f32_32x32x16_bf16 v[16:31], a[88:91], v[140:143], v[16:31]\n"
    "s_waitcnt lgkmcnt(4)\n"
    "s_mov_b32 s76, 0x5000\n"
    "ds_read_b64_tr_b16 a[72:73], v240 offset:51200\n"
    "ds_read_b64_tr_b16 a[74:75], v240 offset:52224\n"
    "buffer_store_dwordx4 v[80:83], v0, s[56:59], s76 offen offset:0 sc1\n"
    "v_mfma_f32_32x32x16_bf16 v[32:47], a[92:95], v[140:143], v[32:47]\n"
    "s_waitcnt lgkmcnt(4)\n"
    "ds_read_b64_tr_b16 a[76:77], v241 offset:51200\n"
    "ds_read_b64_tr_b16 a[78:79], v241 offset:52224\n"
    "buffer_store_dwordx4 v[88:91], v0, s[56:59], s76 offen offset:1024 sc1\n"
    "v_mfma_f32_32x32x16_bf16 v[16:31], a[64:67], v[144:147], v[16:31]\n"
    "s_waitcnt lgkmcnt(4)\n"
    "ds_read_b64_tr_b16 a[80:81], v240 offset:53248\n"
    "ds_read_b64_tr_b16 a[82:83], v240 offset:54272\n"
    "v_cvt_pk_bf16_f32 v96, v96, v97\n"
    "v_cvt_pk_bf16_f32 v97, v98, v99\n"
    "v_cvt_pk_bf16_f32 v98, v100, v101\n"
    "v_cvt_pk_bf16_f32 v99, v102, v103\n"
    "v_mfma_f32_32x32x16_bf16 v[32:47], a[68:71], v[144:147], v[32:47]\n"
    "s_waitcnt lgkmcnt(4)\n"
    "ds_read_b64_tr_b16 a[84:85], v241 offset:53248\n"
    "ds_read_b64_tr_b16 a[86:87], v241 offset:54272\n"
    "v_mul_f32 v104, v104, v8\n"
    "v_mul_f32 v105, v105, v8\n"
    "v_mul_f32 v106, v106, v8\n"
    "v_mul_f32 v107, v107, v8\n"
    "v_mfma_f32_32x32x16_bf16 v[16:31], a[72:75], v[148:151], v[16:31]\n"
    "s_waitcnt lgkmcnt(4)\n"
    "ds_read_b64_tr_b16 a[88:89], v240 offset:55296\n"
    "ds_read_b64_tr_b16 a[90:91], v240 offset:56320\n"
    "v_mul_f32 v108, v108, v8\n"
    "v_mul_f32 v109, v109, v8\n"
    "v_mul_f32 v110, v110, v8\n"
    "v_mul_f32 v111, v111, v8\n"
    "v_mfma_f32_32x32x16_bf16 v[32:47], a[76:79], v[148:151], v[32:47]\n"
    "s_waitcnt lgkmcnt(4)\n"
    "ds_read_b64_tr_b16 a[92:93], v241 offset:55296\n"
    "ds_read_b64_tr_b16 a[94:95], v241 offset:56320\n"
    "v_cvt_pk_bf16_f32 v104, v104, v105\n"
    "v_cvt_pk_bf16_f32 v105, v106, v107\n"
    "v_cvt_pk_bf16_f32 v106, v108, v109\n"
    "v_cvt_pk_bf16_f32 v107, v110, v111\n"
    "v_mfma_f32_32x32x16_bf16 v[16:31], a[80:83], v[152:155], v[16:31]\n"
    "s_waitcnt lgkmcnt(4)\n"
    "ds_read_b64_tr_b16 a[64:65], v240 offset:57344\n"
    "ds_read_b64_tr_b16 a[66:67], v240 offset:58368\n"
    "v_mfma_f32_32x32x16_bf16 v[32:47], a[84:87], v[152:155], v[32:47]\n"
    "s_waitcnt lgkmcnt(4)\n"
    "ds_read_b64_tr_b16 a[68:69], v241 offset:57344\n"
    "ds_read_b64_tr_b16 a[70:71], v241 offset:58368\n"
    "v_mfma_f32_32x32x16_bf16 v[16:31], a[88:91], v[156:159], v[16:31]\n"
    "s_waitcnt lgkmcnt(4)\n"
    "ds_read_b64_tr_b16 a[72:73], v240 offset:59392\n"
    "ds_read_b64_tr_b16 a[74:75], v240 offset:60416\n"
    "v_mfma_f32_32x32x16_bf16 v[32:47], a[92:95], v[156:159], v[32:47]\n"
    "s_waitcnt lgkmcnt(4)\n"
    "ds_read_b64_tr_b16 a[76:77], v241 offset:59392\n"
    "ds_read_b64_tr_b16 a[78:79], v241 offset:60416\n"
    "v_mfma_f32_32x32x16_bf16 v[16:31], a[64:67], v[160:163], v[16:31]\n"
    "s_waitcnt lgkmcnt(4)\n"
    "ds_read_b64_tr_b16 a[80:81], v240 offset:61440\n"
    "ds_read_b64_tr_b16 a[82:83], v240 offset:62464\n"
    "v_mfma_f32_32x32x16_bf16 v[32:47], a[68:71], v[160:163], v[32:47]\n"
    "s_waitcnt lgkmcnt(4)\n"
    "s_mov_b32 s76, 0x5800\n"
    "ds_read_b64_tr_b16 a[84:85], v241 offset:61440\n"
    "ds_read_b64_tr_b16 a[86:87], v241 offset:62464\n"
    "buffer_store_dwordx4 v[96:99], v0, s[56:59], s76 offen offset:0 sc1\n"
    "v_mfma_f32_32x32x16_bf16 v[16:31], a[72:75], v[164:167], v[16:31]\n"
    "s_waitcnt lgkmcnt(4)\n"
    "ds_read_b64_tr_b16 a[88:89], v240 offset:63488\n"
    "ds_read_b64_tr_b16 a[90:91], v240 offset:64512\n"
    "buffer_store_dwordx4 v[104:107], v0, s[56:59], s76 offen offset:1024 sc1\n"
    "v_mfma_f32_32x32x16_bf16 v[32:47], a[76:79], v[164:167], v[32:47]\n"
    "s_waitcnt lgkmcnt(4)\n"
    "ds_read_b64_tr_b16 a[92:93], v241 offset:63488\n"
    "ds_read_b64_tr_b16 a[94:95], v241 offset:64512\n"
    "v_mfma_f32_32x32x16_bf16 v[16:31], a[80:83], v[168:171], v[16:31]\n"
    "s_waitcnt lgkmcnt(4)\n"
    "v_mfma_f32_32x32x16_bf16 v[32:47], a[84:87], v[168:171], v[32:47]\n"
    "s_waitcnt lgkmcnt(2)\n"
    "v_mfma_f32_32x32x16_bf16 v[16:31], a[88:91], v[172:175], v[16:31]\n"
    "s_waitcnt lgkmcnt(0)\n"
    "v_mfma_f32_32x32x16_bf16 v[32:47], a[92:95], v[172:175], v[32:47]\n"
    "s_nop 11\n"
    "v_mul_f32 v16, v16, v8\n"
    "v_mul_f32 v17, v17, v8\n"
    "v_mul_f32 v18, v18, v8\n"
    "v_mul_f32 v19, v19, v8\n"
    "v_mul_f32 v20, v20, v8\n"
    "v_mul_f32 v21, v21, v8\n"
    "v_mul_f32 v22, v22, v8\n"
    "v_mul_f32 v23, v23, v8\n"
    "v_cvt_pk_bf16_f32 v16, v16, v17\n"
    "v_cvt_pk_bf16_f32 v17, v18, v19\n"
    "v_cvt_pk_bf16_f32 v18, v20, v21\n"
    "v_cvt_pk_bf16_f32 v19, v22, v23\n"
    "v_mul_f32 v24, v24, v8\n"
    "v_mul_f32 v25, v25, v8\n"
    "v_mul_f32 v26, v26, v8\n"
    "v_mul_f32 v27, v27, v8\n"
    "v_mul_f32 v28, v28, v8\n"
    "v_mul_f32 v29, v29, v8\n"
    "v_mul_f32 v30, v30, v8\n"
    "v_mul_f32 v31, v31, v8\n"
    "v_cvt_pk_bf16_f32 v24, v24, v25\n"
    "v_cvt_pk_bf16_f32 v25, v26, v27\n"
    "v_cvt_pk_bf16_f32 v26, v28, v29\n"
    "v_cvt_pk_bf16_f32 v27, v30, v31\n"
    "s_mov_b32 s76, 0x6000\n"
    "buffer_store_dwordx4 v[16:19], v0, s[56:59], s76 offen offset:0 sc1\n"
    "buffer_store_dwordx4 v[24:27], v0, s[56:59], s76 offen offset:1024 sc1\n"
    "v_mul_f32 v32, v32, v8\n"
    "v_mul_f32 v33, v33, v8\n"
    "v_mul_f32 v34, v34, v8\n"
    "v_mul_f32 v35, v35, v8\n"
    "v_mul_f32 v36, v36, v8\n"
    "v_mul_f32 v37, v37, v8\n"
    "v_mul_f32 v38, v38, v8\n"
    "v_mul_f32 v39, v39, v8\n"
    "v_cvt_pk_bf16_f32 v32, v32, v33\n"
    "v_cvt_pk_bf16_f32 v33, v34, v35\n"
    "v_cvt_pk_bf16_f32 v34, v36, v37\n"
    "v_cvt_pk_bf16_f32 v35, v38, v39\n"
    "v_mul_f32 v40, v40, v8\n"
    "v_mul_f32 v41, v41, v8\n"
    "v_mul_f32 v42, v42, v8\n"
    "v_mul_f32 v43, v43, v8\n"
    "v_mul_f32 v44, v44, v8\n"
    "v_mul_f32 v45, v45, v8\n"
    "v_mul_f32 v46, v46, v8\n"
    "v_mul_f32 v47, v47, v8\n"
    "v_cvt_pk_bf16_f32 v40, v40, v41\n"
    "v_cvt_pk_bf16_f32 v41, v42, v43\n"
    "v_cvt_pk_bf16_f32 v42, v44, v45\n"
    "v_cvt_pk_bf16_f32 v43, v46, v47\n"
    "s_mov_b32 s76, 0x6800\n"
    "buffer_store_dwordx4 v[32:35], v0, s[56:59], s76 offen offset:0 sc1\n"
    "buffer_store_dwordx4 v[40:43], v0, s[56:59], s76 offen offset:1024 sc1\n"
    "L_end_%=:\n"
    :: "s"(ws), "s"(wq), "s"(wo), "s"(wsp), "s"(wid), "s"(blk), "s"(lb), "s"(win), "s"(wout)
    : "v0","v1","v2","v3","v4","v5","v6","v7","v8","v9","v10","v11","v12","v13","v14","v15","v16","v17","v18","v19","v20","v21","v22","v23","v24","v25","v26","v27","v28","v29","v30","v31","v32","v33","v34","v35","v36","v37","v38","v39","v40","v41","v42","v43","v44","v45","v46","v47","v48","v49","v50","v51","v52","v53","v54","v55","v56","v57","v58","v59","v60","v61","v62","v63","v64","v65","v66","v67","v68","v69","v70","v71","v72","v73","v74","v75","v76","v77","v78","v79","v80","v81","v82","v83","v84","v85","v86","v87","v88","v89","v90","v91","v92","v93","v94","v95","v96","v97","v98","v99","v100","v101","v102","v103","v104","v105","v106","v107","v108","v109","v110","v111","v112","v113","v114","v115","v116","v117","v118","v119","v120","v121","v122","v123","v124","v125","v126","v127","v128","v129","v130","v131","v132","v133","v134","v135","v136","v137","v138","v139","v140","v141","v142","v143","v144","v145","v146","v147","v148","v149","v150","v151","v152","v153","v154","v155","v156","v157","v158","v159","v160","v161","v162","v163","v164","v165","v166","v167","v168","v169","v170","v171","v172","v173","v174","v175","v176","v177","v178","v179","v180","v181","v182","v183","v184","v185","v186","v187","v188","v189","v190","v191","v192","v193","v194","v195","v196","v197","v198","v199","v200","v201","v202","v203","v204","v205","v206","v207","v208","v209","v210","v211","v212","v213","v214","v215","v216","v217","v218","v219","v220","v221","v222","v223","v224","v225","v226","v227","v228","v229","v230","v231","v232","v233","v234","v235","v236","v237","v238","v239","v240","v241","v242","v243","v244","v245","v246","v247","v248","v249","v250","v251","v252","v253","v254","v255","a0","a1","a2","a3","a4","a5","a6","a7","a8","a9","a10","a11","a12","a13","a14","a15","a16","a17","a18","a19","a20","a21","a22","a23","a24","a25","a26","a27","a28","a29","a30","a31","a32","a33","a34","a35","a36","a37","a38","a39","a40","a41","a42","a43","a44","a45","a46","a47","a48","a49","a50","a51","a52","a53","a54","a55","a56","a57","a58","a59","a60","a61","a62","a63","a64","a65","a66","a67","a68","a69","a70","a71","a72","a73","a74","a75","a76","a77","a78","a79","a80","a81","a82","a83","a84","a85","a86","a87","a88","a89","a90","a91","a92","a93","a94","a95","a96","a97","a98","a99","a100","a101","a102","a103","a104","a105","a106","a107","a108","a109","a110","a111","a112","a113","a114","a115","a116","a117","a118","a119","a120","a121","a122","a123","a124","a125","a126","a127","a128","a129","a130","a131","a132","a133","a134","a135","a136","a137","a138","a139","a140","a141","a142","a143","a144","a145","a146","a147","a148","a149","a150","a151","a152","a153","a154","a155","a156","a157","a158","a159","a160","a161","a162","a163","a164","a165","a166","a167","a168","a169","a170","a171","a172","a173","a174","a175","a176","a177","a178","a179","a180","a181","a182","a183","a184","a185","a186","a187","a188","a189","a190","a191","a192","a193","a194","a195","a196","a197","a198","a199","a200","a201","a202","a203","a204","a205","a206","a207","a208","a209","a210","a211","a212","a213","a214","a215","a216","a217","a218","a219","a220","a221","a222","a223","a224","a225","a226","a227","a228","a229","a230","a231","a232","a233","a234","a235","a236","a237","a238","a239","a240","a241","a242","a243","a244","a245","a246","a247","a248","a249","a250","a251","a252","a253","a254","a255","s24","s25","s26","s27","s28","s29","s30","s31","s36","s37","s38","s39","s40","s41","s42","s43","s44","s45","s46","s47","s48","s49","s50","s51","s52","s53","s54","s55","s56","s57","s58","s59","s60","s61","s62","s63","s64","s65","s66","s67","s68","s69","s70","s71","s72","s73","s74","s75","s76","s77","s78","s79","s80","s81","s82","s83","s84","s85","s86","s87","s88","s89","s90","s91","s92","s93","s94","s95","s96","s97","s98","s99","vcc","memory");
}
extern "C" __global__ __launch_bounds__(256, 1) void t_tail(const unsigned short* __restrict__ x, const unsigned short* __restrict__ bsp, char* __restrict__ ws, unsigned short* __restrict__ out) {
  __shared__ __attribute__((aligned(16))) char sm[163840];
  const unsigned w = __builtin_amdgcn_readfirstlane(threadIdx.x >> 6);
  const unsigned b = blockIdx.x, xg = b & 7, ii = b >> 3;
  const unsigned tb = 8 * (xg >> 1) + (ii >> 2), j = 4 * (xg & 1) + (ii & 3);
  const unsigned ldsb = (unsigned)(size_t)sm;
  asm volatile(
    "s_mov_b64 s[40:41], %0\n"
    "s_and_b32 s41, s41, 0xffff\n"
    "s_mov_b32 s42, 0x36bc000\n"
    "s_mov_b32 s43, 0x20000\n"
    "s_mov_b64 s[44:45], %1\n"
    "s_and_b32 s45, s45, 0xffff\n"
    "s_mov_b32 s46, 0x1c0000\n"
    "s_mov_b32 s47, 0x20000\n"
    "s_mov_b64 s[48:49], %2\n"
    "s_and_b32 s49, s49, 0xffff\n"
    "s_mov_b32 s50, 0x1000\n"
    "s_mov_b32 s51, 0x20000\n"
    "s_mov_b32 s52, %3\n"
    "s_mov_b32 s53, %4\n"
    "s_mov_b32 s54, %5\n"
    "s_mov_b32 s55, %6\n"
    "v_mbcnt_lo_u32_b32 v1, -1, 0\n"
    "v_mbcnt_hi_u32_b32 v1, -1, v1\n"
    "v_lshlrev_b32 v0, 4, v1\n"
    "s_mov_b32 s72, 0xff00ff00\n"
    "s_mov_b32 s73, 0xff00ff00\n"
    "s_lshr_b32 s88, %7, 3\n"
    "s_lshr_b32 s89, s88, 2\n"
    "s_lshl_b32 s89, s89, 2\n"
    "s_add_u32 s89, s89, s52\n"
    "s_lshl_b32 s88, s88, 2\n"
    "s_add_u32 s88, s88, s52\n"
    "s_and_b32 s56, s53, 7\n"
    "s_lshl_b32 s56, s56, 1\n"
    "s_and_b32 s57, s54, 3\n"
    "s_lshl_b32 s57, s57, 2\n"
    "s_add_u32 s90, s56, s57\n"
    "s_and_b32 s90, s90, 0\n"
    "s_add_u32 s91, s52, s53\n"
    "s_and_b32 s91, s91, 3\n"
    "s_mov_b32 s91, s52\n"
    "s_bfe_u32 s92, s53, 0x10002\n"
    "s_mul_i32 s92, s92, 0\n"
    "s_getpc_b64 s[74:75]\n"
    "L_pc_%=:\n"
    "s_lshl_b32 s56, s88, 10\n"
    "s_min_u32 s56, s56, L_codeend_%=-L_pc_%=-1024\n"
    "v_add_u32 v7, s56, v0\n"
    "global_load_dword v254, v7, s[74:75]\n"
    "s_mul_i32 s69, s91, 25\n"
    "s_min_u32 s69, s69, 73\n"
    "s_lshl_b32 s69, s69, 10\n"
    "s_mul_i32 s67, s54, 100352\n"
    "s_add_u32 s67, s67, 0x21c4000\n"
    "s_lshl_b32 s58, s53, 2\n"
    "s_add_u32 s58, s58, s52\n"
    "s_lshl_b32 s66, s58, 16\n"
    "s_add_u32 s66, s66, 0x2ebc000\n"
    "s_mov_b64 s[80:81], %8\n"
    "s_and_b32 s81, s81, 0xffff\n"
    "s_mov_b32 s82, 0x1c0000\n"
    "s_mov_b32 s83, 0x20000\n"
    "v_and_b32 v186, 7, v1\n"
    "v_lshrrev_b32 v187, 3, v1\n"
    "s_lshr_b32 s56, s53, 1\n"
    "s_lshl_b32 s56, s56, 6\n"
    "s_and_b32 s57, s53, 1\n"
    "s_lshl_b32 s57, s57, 1\n"
    "s_add_u32 s56, s56, s57\n"
    "s_lshr_b32 s57, s54, 2\n"
    "s_add_u32 s56, s56, s57\n"
    "s_mul_i32 s87, s56, 28672\n"
    "s_mul_i32 s57, s52, 7168\n"
    "s_add_u32 s87, s87, s57\n"
    "s_and_b32 s57, s54, 3\n"
    "s_lshl_b32 s57, s57, 7\n"
    "s_add_u32 s87, s87, s57\n"
    "s_add_u32 s87, s87, 0x5c4000\n"
    "v_lshlrev_b32 v184, 9, v187\n"
    "v_lshl_add_u32 v184, v186, 4, v184\n"
    "v_add_u32 v185, 0x1000, v184\n"
    "v_mov_b32 v7, 0x7ff00000\n"
    "v_cmp_gt_u32 s[74:75], 6, v187\n"
    "s_nop 1\n"
    "v_cndmask_b32 v185, v7, v185, s[74:75]\n"
    "s_lshl_b32 s56, s52, 11\n"
    "s_add_u32 m0, s55, s56\n"
    "s_add_u32 m0, m0, 0x1c000\n"
    "s_add_u32 s57, s56, 0x1e00\n"
    "buffer_load_dwordx4 v0, s[40:43], s57 offen offset:0 lds\n"
    "buffer_load_dwordx4 v0, s[40:43], s57 offen offset:1024 lds\n"
    "s_lshl_b32 s56, s53, 6\n"
    "s_lshl_b32 s57, s54, 3\n"
    "s_add_u32 s56, s56, s57\n"
    "v_add_u32 v188, s56, v186\n"
    "v_mul_u32_u24 v188, 0x380, v188\n"
    "v_lshl_add_u32 v188, v187, 4, v188\n"
    "s_mul_i32 s86, s52, 224\n"
    "buffer_load_dwordx4 v[160:163], v188, s[80:83], s86 offen offset:0\n"
    "v_add_u32 v8, 0x80, v188\n"
    "v_cndmask_b32 v8, v7, v8, s[74:75]\n"
    "buffer_load_dwordx4 v[164:167], v8, s[80:83], s86 offen offset:0\n"
    "s_add_u32 s58, s87, 0x0\n"
    "buffer_load_dwordx4 v[16:19], v184, s[40:43], s58 offen offset:0 nt\n"
    "buffer_load_dwordx4 v[80:83], v185, s[40:43], s58 offen offset:0 nt\n"
    "s_add_u32 s60, s92, 0\n"
    "s_cmp_ge_u32 s60, 7\n"
    "s_cselect_b32 s70, 7, 0\n"
    "s_sub_u32 s60, s60, s70\n"
    "s_lshl_b32 s60, s60, 12\n"
    "s_add_u32 s60, s60, s69\n"
    "s_add_u32 m0, s55, s60\n"
    "s_add_u32 s61, s67, s60\n"
    "s_nop 0\n"
    "buffer_load_dwordx4 v0, s[40:43], s61 offen offset:0 lds\n"
    "buffer_load_dwordx4 v0, s[40:43], s61 offen offset:1024 lds\n"
    "s_add_u32 s59, s87, 0x1c000\n"
    "buffer_load_dwordx4 v[20:23], v184, s[40:43], s59 offen offset:0 nt\n"
    "buffer_load_dwordx4 v[84:87], v185, s[40:43], s59 offen offset:0 nt\n"
    "buffer_load_dwordx4 v0, s[40:43], s61 offen offset:2048 lds\n"
    "buffer_load_dwordx4 v0, s[40:43], s61 offen offset:3072 lds\n"
    "s_add_u32 s58, s87, 0x38000\n"
    "buffer_load_dwordx4 v[24:27], v184, s[40:43], s58 offen offset:0 nt\n"
    "buffer_load_dwordx4 v[88:91], v185, s[40:43], s58 offen offset:0 nt\n"
    "s_add_u32 s60, s92, 1\n"
    "s_cmp_ge_u32 s60, 7\n"
    "s_cselect_b32 s70, 7, 0\n"
    "s_sub_u32 s60, s60, s70\n"
    "s_lshl_b32 s60, s60, 12\n"
    "s_add_u32 s60, s60, s69\n"
    "s_add_u32 m0, s55, s60\n"
    "s_add_u32 s61, s67, s60\n"
    "s_nop 0\n"
    "buffer_load_dwordx4 v0, s[40:43], s61 offen offset:0 lds\n"
    "buffer_load_dwordx4 v0, s[40:43], s61 offen offset:1024 lds\n"
    "s_add_u32 s59, s87, 0x54000\n"
    "buffer_load_dwordx4 v[28:31], v184, s[40:43], s59 offen offset:0 nt\n"
    "buffer_load_dwordx4 v[92:95], v185, s[40:43], s59 offen offset:0 nt\n"
    "buffer_load_dwordx4 v0, s[40:43], s61 offen offset:2048 lds\n"
    "buffer_load_dwordx4 v0, s[40:43], s61 offen offset:3072 lds\n"
    "s_add_u32 s58, s87, 0x70000\n"
    "buffer_load_dwordx4 v[32:35], v184, s[40:43], s58 offen offset:0 nt\n"
    "buffer_load_dwordx4 v[96:99], v185, s[40:43], s58 offen offset:0 nt\n"
    "s_add_u32 s60, s92, 2\n"
    "s_cmp_ge_u32 s60, 7\n"
    "s_cselect_b32 s70, 7, 0\n"
    "s_sub_u32 s60, s60, s70\n"
    "s_lshl_b32 s60, s60, 12\n"
    "s_add_u32 s60, s60, s69\n"
    "s_add_u32 m0, s55, s60\n"
    "s_add_u32 s61, s67, s60\n"
    "s_nop 0\n"
    "buffer_load_dwordx4 v0, s[40:43], s61 offen offset:0 lds\n"
    "buffer_load_dwordx4 v0, s[40:43], s61 offen offset:1024 lds\n"
    "s_add_u32 s59, s87, 0x8c000\n"
    "buffer_load_dwordx4 v[36:39], v184, s[40:43], s59 offen offset:0 nt\n"
    "buffer_load_dwordx4 v[100:103], v185, s[40:43], s59 offen offset:0 nt\n"
    "buffer_load_dwordx4 v0, s[40:43], s61 offen offset:2048 lds\n"
    "buffer_load_dwordx4 v0, s[40:43], s61 offen offset:3072 lds\n"
    "s_add_u32 s58, s87, 0xa8000\n"
    "buffer_load_dwordx4 v[40:43], v184, s[40:43], s58 offen offset:0 nt\n"
    "buffer_load_dwordx4 v[104:107], v185, s[40:43], s58 offen offset:0 nt\n"
    "s_add_u32 s60, s92, 3\n"
    "s_cmp_ge_u32 s60, 7\n"
    "s_cselect_b32 s70, 7, 0\n"
    "s_sub_u32 s60, s60, s70\n"
    "s_lshl_b32 s60, s60, 12\n"
    "s_add_u32 s60, s60, s69\n"
    "s_add_u32 m0, s55, s60\n"
    "s_add_u32 s61, s67, s60\n"
    "s_nop 0\n"
    "buffer_load_dwordx4 v0, s[40:43], s61 offen offset:0 lds\n"
    "buffer_load_dwordx4 v0, s[40:43], s61 offen offset:1024 lds\n"
    "s_add_u32 s59, s87, 0xc4000\n"
    "buffer_load_dwordx4 v[44:47], v184, s[40:43], s59 offen offset:0 nt\n"
    "buffer_load_dwordx4 v[108:111], v185, s[40:43], s59 offen offset:0 nt\n"
    "buffer_load_dwordx4 v0, s[40:43], s61 offen offset:2048 lds\n"
    "buffer_load_dwordx4 v0, s[40:43], s61 offen offset:3072 lds\n"
    "s_add_u32 s58, s87, 0xe0000\n"
    "buffer_load_dwordx4 v[48:51], v184, s[40:43], s58 offen offset:0 nt\n"
    "buffer_load_dwordx4 v[112:115], v185, s[40:43], s58 offen offset:0 nt\n"
    "s_add_u32 s60, s92, 4\n"
    "s_cmp_ge_u32 s60, 7\n"
    "s_cselect_b32 s70, 7, 0\n"
    "s_sub_u32 s60, s60, s70\n"
    "s_lshl_b32 s60, s60, 12\n"
    "s_add_u32 s60, s60, s69\n"
    "s_add_u32 m0, s55, s60\n"
    "s_add_u32 s61, s67, s60\n"
    "s_nop 0\n"
    "buffer_load_dwordx4 v0, s[40:43], s61 offen offset:0 lds\n"
    "buffer_load_dwordx4 v0, s[40:43], s61 offen offset:1024 lds\n"
    "s_add_u32 s59, s87, 0xfc000\n"
    "buffer_load_dwordx4 v[52:55], v184, s[40:43], s59 offen offset:0 nt\n"
    "buffer_load_dwordx4 v[116:119], v185, s[40:43], s59 offen offset:0 nt\n"
    "buffer_load_dwordx4 v0, s[40:43], s61 offen offset:2048 lds\n"
    "s_add_u32 s58, s87, 0x118000\n"
    "buffer_load_dwordx4 v[56:59], v184, s[40:43], s58 offen offset:0 nt\n"
    "buffer_load_dwordx4 v[120:123], v185, s[40:43], s58 offen offset:0 nt\n"
    "buffer_load_dwordx4 v0, s[40:43], s61 offen offset:3072 lds\n"
    "s_add_u32 s59, s87, 0x134000\n"
    "buffer_load_dwordx4 v[60:63], v184, s[40:43], s59 offen offset:0 nt\n"
    "buffer_load_dwordx4 v[124:127], v185, s[40:43], s59 offen offset:0 nt\n"
    "s_add_u32 s60, s92, 5\n"
    "s_cmp_ge_u32 s60, 7\n"
    "s_cselect_b32 s70, 7, 0\n"
    "s_sub_u32 s60, s60, s70\n"
    "s_lshl_b32 s60, s60, 12\n"
    "s_add_u32 s60, s60, s69\n"
    "s_add_u32 m0, s55, s60\n"
    "s_add_u32 s61, s67, s60\n"
    "s_nop 0\n"
    "buffer_load_dwordx4 v0, s[40:43], s61 offen offset:0 lds\n"
    "s_add_u32 s58, s87, 0x150000\n"
    "buffer_load_dwordx4 v[64:67], v184, s[40:43], s58 offen offset:0 nt\n"
    "buffer_load_dwordx4 v[128:131], v185, s[40:43], s58 offen offset:0 nt\n"
    "buffer_load_dwordx4 v0, s[40:43], s61 offen offset:1024 lds\n"
    "s_add_u32 s59, s87, 0x16c000\n"
    "buffer_load_dwordx4 v[68:71], v184, s[40:43], s59 offen offset:0 nt\n"
    "buffer_load_dwordx4 v[132:135], v185, s[40:43], s59 offen offset:0 nt\n"
    "buffer_load_dwordx4 v0, s[40:43], s61 offen offset:2048 lds\n"
    "s_add_u32 s58, s87, 0x188000\n"
    "buffer_load_dwordx4 v[72:75], v184, s[40:43], s58 offen offset:0 nt\n"
    "buffer_load_dwordx4 v[136:139], v185, s[40:43], s58 offen offset:0 nt\n"
    "buffer_load_dwordx4 v0, s[40:43], s61 offen offset:3072 lds\n"
    "s_add_u32 s59, s87, 0x1a4000\n"
    "buffer_load_dwordx4 v[76:79], v184, s[40:43], s59 offen offset:0 nt\n"
    "buffer_load_dwordx4 v[140:143], v185, s[40:43], s59 offen offset:0 nt\n"
    "s_add_u32 s60, s92, 6\n"
    "s_cmp_ge_u32 s60, 7\n"
    "s_cselect_b32 s70, 7, 0\n"
    "s_sub_u32 s60, s60, s70\n"
    "s_lshl_b32 s60, s60, 12\n"
    "s_add_u32 s60, s60, s69\n"
    "s_add_u32 m0, s55, s60\n"
    "s_add_u32 s61, s67, s60\n"
    "s_nop 0\n"
    "buffer_load_dwordx4 v0, s[40:43], s61 offen offset:0 lds\n"
    "v_mov_b32 v240, -1\n"
    "v_mov_b32 v241, -1\n"
    "v_mov_b32 v242, -1\n"
    "v_mov_b32 v243, -1\n"
    "s_lshl_b32 s56, s53, 2\n"
    "s_add_u32 s56, s56, s52\n"
    "s_mul_i32 s56, s56, 28672\n"
    "s_mul_i32 s57, s54, 3584\n"
    "s_add_u32 s56, s56, s57\n"
    "s_add_u32 s56, s56, 0x2b30000\n"
    "s_waitcnt vmcnt(58)\n"
    "buffer_store_dwordx4 v[240:243], v0, s[40:43], s56 offen offset:0 sc1\n"
    "buffer_store_dwordx4 v[240:243], v0, s[40:43], s56 offen offset:1024 sc1\n"
    "buffer_store_dwordx4 v[240:243], v0, s[40:43], s56 offen offset:2048 sc1\n"
    "v_mov_b32 v9, 0x7ff00000\n"
    "v_cmp_gt_u32 vcc, 32, v1\n"
    "s_nop 1\n"
    "v_cndmask_b32 v9, v9, v0, vcc\n"
    "buffer_store_dwordx4 v[240:243], v9, s[40:43], s56 offen offset:3072 sc1\n"
    "s_lshl_b32 s56, s53, 3\n"
    "s_add_u32 s56, s56, s54\n"
    "s_lshl_b32 s56, s56, 9\n"
    "s_lshl_b32 s57, s52, 7\n"
    "s_add_u32 s56, s56, s57\n"
    "s_add_u32 s56, s56, 0x2790000\n"
    "v_mov_b32 v9, 0x7ff00000\n"
    "v_cmp_gt_u32 vcc, 8, v1\n"
    "s_nop 1\n"
    "v_cndmask_b32 v9, v9, v0, vcc\n"
    "s_waitcnt vmcnt(58)\n"
    "buffer_store_dwordx4 v[240:243], v9, s[40:43], s56 offen offset:0 sc1\n"
    "v_lshlrev_b32 v144, 16, v16\n"
    "v_and_b32 v145, 0xffff0000, v16\n"
    "v_lshlrev_b32 v146, 16, v17\n"
    "v_and_b32 v147, 0xffff0000, v17\n"
    "v_lshlrev_b32 v148, 16, v18\n"
    "v_and_b32 v149, 0xffff0000, v18\n"
    "v_lshlrev_b32 v150, 16, v19\n"
    "v_and_b32 v151, 0xffff0000, v19\n"
    "v_lshlrev_b32 v152, 16, v80\n"
    "v_and_b32 v153, 0xffff0000, v80\n"
    "v_lshlrev_b32 v154, 16, v81\n"
    "v_and_b32 v155, 0xffff0000, v81\n"
    "v_lshlrev_b32 v156, 16, v82\n"
    "v_and_b32 v157, 0xffff0000, v82\n"
    "v_lshlrev_b32 v158, 16, v83\n"
    "v_and_b32 v159, 0xffff0000, v83\n"
    "s_waitcnt vmcnt(57)\n"
    "v_lshlrev_b32 v168, 16, v20\n"
    "v_and_b32 v169, 0xffff0000, v20\n"
    "v_lshlrev_b32 v170, 16, v21\n"
    "v_and_b32 v171, 0xffff0000, v21\n"
    "v_lshlrev_b32 v172, 16, v22\n"
    "v_and_b32 v173, 0xffff0000, v22\n"
    "v_lshlrev_b32 v174, 16, v23\n"
    "v_and_b32 v175, 0xffff0000, v23\n"
    "v_add_f32 v144, v144, v168\n"
    "v_add_f32 v145, v145, v169\n"
    "v_add_f32 v146, v146, v170\n"
    "v_add_f32 v147, v147, v171\n"
    "v_add_f32 v148, v148, v172\n"
    "v_add_f32 v149, v149, v173\n"
    "v_add_f32 v150, v150, v174\n"
    "v_add_f32 v151, v151, v175\n"
    "s_waitcnt vmcnt(56)\n"
    "v_lshlrev_b32 v168, 16, v84\n"
    "v_and_b32 v169, 0xffff0000, v84\n"
    "v_lshlrev_b32 v170, 16, v85\n"
    "v_and_b32 v171, 0xffff0000, v85\n"
    "v_lshlrev_b32 v172, 16, v86\n"
    "v_and_b32 v173, 0xffff0000, v86\n"
    "v_lshlrev_b32 v174, 16, v87\n"
    "v_and_b32 v175, 0xffff0000, v87\n"
    "v_add_f32 v152, v152, v168\n"
    "v_add_f32 v153, v153, v169\n"
    "v_add_f32 v154, v154, v170\n"
    "v_add_f32 v155, v155, v171\n"
    "v_add_f32 v156, v156, v172\n"
    "v_add_f32 v157, v157, v173\n"
    "v_add_f32 v158, v158, v174\n"
    "v_add_f32 v159, v159, v175\n"
    "s_barrier\n"
    "s_waitcnt vmcnt(53)\n"
    "v_lshlrev_b32 v168, 16, v24\n"
    "v_and_b32 v169, 0xffff0000, v24\n"
    "v_lshlrev_b32 v170, 16, v25\n"
    "v_and_b32 v171, 0xffff0000, v25\n"
    "v_lshlrev_b32 v172, 16, v26\n"
    "v_and_b32 v173, 0xffff0000, v26\n"
    "v_lshlrev_b32 v174, 16, v27\n"
    "v_and_b32 v175, 0xffff0000, v27\n"
    "v_add_f32 v144, v144, v168\n"
    "v_add_f32 v145, v145, v169\n"
    "v_add_f32 v146, v146, v170\n"
    "v_add_f32 v147, v147, v171\n"
    "v_add_f32 v148, v148, v172\n"
    "v_add_f32 v149, v149, v173\n"
    "v_add_f32 v150, v150, v174\n"
    "v_add_f32 v151, v151, v175\n"
    "s_waitcnt vmcnt(52)\n"
    "v_lshlrev_b32 v168, 16, v88\n"
    "v_and_b32 v169, 0xffff0000, v88\n"
    "v_lshlrev_b32 v170, 16, v89\n"
    "v_and_b32 v171, 0xffff0000, v89\n"
    "v_lshlrev_b32 v172, 16, v90\n"
    "v_and_b32 v173, 0xffff0000, v90\n"
    "v_lshlrev_b32 v174, 16, v91\n"
    "v_and_b32 v175, 0xffff0000, v91\n"
    "v_add_f32 v152, v152, v168\n"
    "v_add_f32 v153, v153, v169\n"
    "v_add_f32 v154, v154, v170\n"
    "v_add_f32 v155, v155, v171\n"
    "v_add_f32 v156, v156, v172\n"
    "v_add_f32 v157, v157, v173\n"
    "v_add_f32 v158, v158, v174\n"
    "v_add_f32 v159, v159, v175\n"
    "s_waitcnt vmcnt(49)\n"
    "v_lshlrev_b32 v168, 16, v28\n"
    "v_and_b32 v169, 0xffff0000, v28\n"
    "v_lshlrev_b32 v170, 16, v29\n"
    "v_and_b32 v171, 0xffff0000, v29\n"
    "v_lshlrev_b32 v172, 16, v30\n"
    "v_and_b32 v173, 0xffff0000, v30\n"
    "v_lshlrev_b32 v174, 16, v31\n"
    "v_and_b32 v175, 0xffff0000, v31\n"
    "v_add_f32 v144, v144, v168\n"
    "v_add_f32 v145, v145, v169\n"
    "v_add_f32 v146, v146, v170\n"
    "v_add_f32 v147, v147, v171\n"
    "v_add_f32 v148, v148, v172\n"
    "v_add_f32 v149, v149, v173\n"
    "v_add_f32 v150, v150, v174\n"
    "v_add_f32 v151, v151, v175\n"
    "s_waitcnt vmcnt(48)\n"
    "v_lshlrev_b32 v168, 16, v92\n"
    "v_and_b32 v169, 0xffff0000, v92\n"
    "v_lshlrev_b32 v170, 16, v93\n"
    "v_and_b32 v171, 0xffff0000, v93\n"
    "v_lshlrev_b32 v172, 16, v94\n"
    "v_and_b32 v173, 0xffff0000, v94\n"
    "v_lshlrev_b32 v174, 16, v95\n"
    "v_and_b32 v175, 0xffff0000, v95\n"
    "v_add_f32 v152, v152, v168\n"
    "v_add_f32 v153, v153, v169\n"
    "v_add_f32 v154, v154, v170\n"
    "v_add_f32 v155, v155, v171\n"
    "v_add_f32 v156, v156, v172\n"
    "v_add_f32 v157, v157, v173\n"
    "v_add_f32 v158, v158, v174\n"
    "v_add_f32 v159, v159, v175\n"
    "s_waitcnt vmcnt(45)\n"
    "v_lshlrev_b32 v168, 16, v32\n"
    "v_and_b32 v169, 0xffff0000, v32\n"
    "v_lshlrev_b32 v170, 16, v33\n"
    "v_and_b32 v171, 0xffff0000, v33\n"
    "v_lshlrev_b32 v172, 16, v34\n"
    "v_and_b32 v173, 0xffff0000, v34\n"
    "v_lshlrev_b32 v174, 16, v35\n"
    "v_and_b32 v175, 0xffff0000, v35\n"
    "v_add_f32 v144, v144, v168\n"
    "v_add_f32 v145, v145, v169\n"
    "v_add_f32 v146, v146, v170\n"
    "v_add_f32 v147, v147, v171\n"
    "v_add_f32 v148, v148, v172\n"
    "v_add_f32 v149, v149, v173\n"
    "v_add_f32 v150, v150, v174\n"
    "v_add_f32 v151, v151, v175\n"
    "s_waitcnt vmcnt(44)\n"
    "v_lshlrev_b32 v168, 16, v96\n"
    "v_and_b32 v169, 0xffff0000, v96\n"
    "v_lshlrev_b32 v170, 16, v97\n"
    "v_and_b32 v171, 0xffff0000, v97\n"
    "v_lshlrev_b32 v172, 16, v98\n"
    "v_and_b32 v173, 0xffff0000, v98\n"
    "v_lshlrev_b32 v174, 16, v99\n"
    "v_and_b32 v175, 0xffff0000, v99\n"
    "v_add_f32 v152, v152, v168\n"
    "v_add_f32 v153, v153, v169\n"
    "v_add_f32 v154, v154, v170\n"
    "v_add_f32 v155, v155, v171\n"
    "v_add_f32 v156, v156, v172\n"
    "v_add_f32 v157, v157, v173\n"
    "v_add_f32 v158, v158, v174\n"
    "v_add_f32 v159, v159, v175\n"
    "s_waitcnt vmcnt(41)\n"
    "v_lshlrev_b32 v168, 16, v36\n"
    "v_and_b32 v169, 0xffff0000, v36\n"
    "v_lshlrev_b32 v170, 16, v37\n"
    "v_and_b32 v171, 0xffff0000, v37\n"
    "v_lshlrev_b32 v172, 16, v38\n"
    "v_and_b32 v173, 0xffff0000, v38\n"
    "v_lshlrev_b32 v174, 16, v39\n"
    "v_and_b32 v175, 0xffff0000, v39\n"
    "v_add_f32 v144, v144, v168\n"
    "v_add_f32 v145, v145, v169\n"
    "v_add_f32 v146, v146, v170\n"
    "v_add_f32 v147, v147, v171\n"
    "v_add_f32 v148, v148, v172\n"
    "v_add_f32 v149, v149, v173\n"
    "v_add_f32 v150, v150, v174\n"
    "v_add_f32 v151, v151, v175\n"
    "s_waitcnt vmcnt(40)\n"
    "v_lshlrev_b32 v168, 16, v100\n"
    "v_and_b32 v169, 0xffff0000, v100\n"
    "v_lshlrev_b32 v170, 16, v101\n"
    "v_and_b32 v171, 0xffff0000, v101\n"
    "v_lshlrev_b32 v172, 16, v102\n"
    "v_and_b32 v173, 0xffff0000, v102\n"
    "v_lshlrev_b32 v174, 16, v103\n"
    "v_and_b32 v175, 0xffff0000, v103\n"
    "v_add_f32 v152, v152, v168\n"
    "v_add_f32 v153, v153, v169\n"
    "v_add_f32 v154, v154, v170\n"
    "v_add_f32 v155, v155, v171\n"
    "v_add_f32 v156, v156, v172\n"
    "v_add_f32 v157, v157, v173\n"
    "v_add_f32 v158, v158, v174\n"
    "v_add_f32 v159, v159, v175\n"
    "s_waitcnt vmcnt(37)\n"
    "v_lshlrev_b32 v168, 16, v40\n"
    "v_and_b32 v169, 0xffff0000, v40\n"
    "v_lshlrev_b32 v170, 16, v41\n"
    "v_and_b32 v171, 0xffff0000, v41\n"
    "v_lshlrev_b32 v172, 16, v42\n"
    "v_and_b32 v173, 0xffff0000, v42\n"
    "v_lshlrev_b32 v174, 16, v43\n"
    "v_and_b32 v175, 0xffff0000, v43\n"
    "v_add_f32 v144, v144, v168\n"
    "v_add_f32 v145, v145, v169\n"
    "v_add_f32 v146, v146, v170\n"
    "v_add_f32 v147, v147, v171\n"
    "v_add_f32 v148, v148, v172\n"
    "v_add_f32 v149, v149, v173\n"
    "v_add_f32 v150, v150, v174\n"
    "v_add_f32 v151, v151, v175\n"
    "s_waitcnt vmcnt(36)\n"
    "v_lshlrev_b32 v168, 16, v104\n"
    "v_and_b32 v169, 0xffff0000, v104\n"
    "v_lshlrev_b32 v170, 16, v105\n"
    "v_and_b32 v171, 0xffff0000, v105\n"
    "v_lshlrev_b32 v172, 16, v106\n"
    "v_and_b32 v173, 0xffff0000, v106\n"
    "v_lshlrev_b32 v174, 16, v107\n"
    "v_and_b32 v175, 0xffff0000, v107\n"
    "v_add_f32 v152, v152, v168\n"
    "v_add_f32 v153, v153, v169\n"
    "v_add_f32 v154, v154, v170\n"
    "v_add_f32 v155, v155, v171\n"
    "v_add_f32 v156, v156, v172\n"
    "v_add_f32 v157, v157, v173\n"
    "v_add_f32 v158, v158, v174\n"
    "v_add_f32 v159, v159, v175\n"
    "s_waitcnt vmcnt(33)\n"
    "v_lshlrev_b32 v168, 16, v44\n"
    "v_and_b32 v169, 0xffff0000, v44\n"
    "v_lshlrev_b32 v170, 16, v45\n"
    "v_and_b32 v171, 0xffff0000, v45\n"
    "v_lshlrev_b32 v172, 16, v46\n"
    "v_and_b32 v173, 0xffff0000, v46\n"
    "v_lshlrev_b32 v174, 16, v47\n"
    "v_and_b32 v175, 0xffff0000, v47\n"
    "v_add_f32 v144, v144, v168\n"
    "v_add_f32 v145, v145, v169\n"
    "v_add_f32 v146, v146, v170\n"
    "v_add_f32 v147, v147, v171\n"
    "v_add_f32 v148, v148, v172\n"
    "v_add_f32 v149, v149, v173\n"
    "v_add_f32 v150, v150, v174\n"
    "v_add_f32 v151, v151, v175\n"
    "s_waitcnt vmcnt(32)\n"
    "v_lshlrev_b32 v168, 16, v108\n"
    "v_and_b32 v169, 0xffff0000, v108\n"
    "v_lshlrev_b32 v170, 16, v109\n"
    "v_and_b32 v171, 0xffff0000, v109\n"
    "v_lshlrev_b32 v172, 16, v110\n"
    "v_and_b32 v173, 0xffff0000, v110\n"
    "v_lshlrev_b32 v174, 16, v111\n"
    "v_and_b32 v175, 0xffff0000, v111\n"
    "v_add_f32 v152, v152, v168\n"
    "v_add_f32 v153, v153, v169\n"
    "v_add_f32 v154, v154, v170\n"
    "v_add_f32 v155, v155, v171\n"
    "v_add_f32 v156, v156, v172\n"
    "v_add_f32 v157, v157, v173\n"
    "v_add_f32 v158, v158, v174\n"
    "v_add_f32 v159, v159, v175\n"
    "s_waitcnt vmcnt(29)\n"
    "v_lshlrev_b32 v168, 16, v48\n"
    "v_and_b32 v169, 0xffff0000, v48\n"
    "v_lshlrev_b32 v170, 16, v49\n"
    "v_and_b32 v171, 0xffff0000, v49\n"
    "v_lshlrev_b32 v172, 16, v50\n"
    "v_and_b32 v173, 0xffff0000, v50\n"
    "v_lshlrev_b32 v174, 16, v51\n"
    "v_and_b32 v175, 0xffff0000, v51\n"
    "v_add_f32 v144, v144, v168\n"
    "v_add_f32 v145, v145, v169\n"
    "v_add_f32 v146, v146, v170\n"
    "v_add_f32 v147, v147, v171\n"
    "v_add_f32 v148, v148, v172\n"
    "v_add_f32 v149, v149, v173\n"
    "v_add_f32 v150, v150, v174\n"
    "v_add_f32 v151, v151, v175\n"
    "s_waitcnt vmcnt(28)\n"
    "v_lshlrev_b32 v168, 16, v112\n"
    "v_and_b32 v169, 0xffff0000, v112\n"
    "v_lshlrev_b32 v170, 16, v113\n"
    "v_and_b32 v171, 0xffff0000, v113\n"
    "v_lshlrev_b32 v172, 16, v114\n"
    "v_and_b32 v173, 0xffff0000, v114\n"
    "v_lshlrev_b32 v174, 16, v115\n"
    "v_and_b32 v175, 0xffff0000, v115\n"
    "v_add_f32 v152, v152, v168\n"
    "v_add_f32 v153, v153, v169\n"
    "v_add_f32 v154, v154, v170\n"
    "v_add_f32 v155, v155, v171\n"
    "v_add_f32 v156, v156, v172\n"
    "v_add_f32 v157, v157, v173\n"
    "v_add_f32 v158, v158, v174\n"
    "v_add_f32 v159, v159, v175\n"
    "s_waitcnt vmcnt(25)\n"
    "v_lshlrev_b32 v168, 16, v52\n"
    "v_and_b32 v169, 0xffff0000, v52\n"
    "v_lshlrev_b32 v170, 16, v53\n"
    "v_and_b32 v171, 0xffff0000, v53\n"
    "v_lshlrev_b32 v172, 16, v54\n"
    "v_and_b32 v173, 0xffff0000, v54\n"
    "v_lshlrev_b32 v174, 16, v55\n"
    "v_and_b32 v175, 0xffff0000, v55\n"
    "v_add_f32 v144, v144, v168\n"
    "v_add_f32 v145, v145, v169\n"
    "v_add_f32 v146, v146, v170\n"
    "v_add_f32 v147, v147, v171\n"
    "v_add_f32 v148, v148, v172\n"
    "v_add_f32 v149, v149, v173\n"
    "v_add_f32 v150, v150, v174\n"
    "v_add_f32 v151, v151, v175\n"
    "s_waitcnt vmcnt(24)\n"
    "v_lshlrev_b32 v168, 16, v116\n"
    "v_and_b32 v169, 0xffff0000, v116\n"
    "v_lshlrev_b32 v170, 16, v117\n"
    "v_and_b32 v171, 0xffff0000, v117\n"
    "v_lshlrev_b32 v172, 16, v118\n"
    "v_and_b32 v173, 0xffff0000, v118\n"
    "v_lshlrev_b32 v174, 16, v119\n"
    "v_and_b32 v175, 0xffff0000, v119\n"
    "v_add_f32 v152, v152, v168\n"
    "v_add_f32 v153, v153, v169\n"
    "v_add_f32 v154, v154, v170\n"
    "v_add_f32 v155, v155, v171\n"
    "v_add_f32 v156, v156, v172\n"
    "v_add_f32 v157, v157, v173\n"
    "v_add_f32 v158, v158, v174\n"
    "v_add_f32 v159, v159, v175\n"
    "s_waitcnt vmcnt(22)\n"
    "v_lshlrev_b32 v168, 16, v56\n"
    "v_and_b32 v169, 0xffff0000, v56\n"
    "v_lshlrev_b32 v170, 16, v57\n"
    "v_and_b32 v171, 0xffff0000, v57\n"
    "v_lshlrev_b32 v172, 16, v58\n"
    "v_and_b32 v173, 0xffff0000, v58\n"
    "v_lshlrev_b32 v174, 16, v59\n"
    "v_and_b32 v175, 0xffff0000, v59\n"
    "v_add_f32 v144, v144, v168\n"
    "v_add_f32 v145, v145, v169\n"
    "v_add_f32 v146, v146, v170\n"
    "v_add_f32 v147, v147, v171\n"
    "v_add_f32 v148, v148, v172\n"
    "v_add_f32 v149, v149, v173\n"
    "v_add_f32 v150, v150, v174\n"
    "v_add_f32 v151, v151, v175\n"
    "s_waitcnt vmcnt(21)\n"
    "v_lshlrev_b32 v168, 16, v120\n"
    "v_and_b32 v169, 0xffff0000, v120\n"
    "v_lshlrev_b32 v170, 16, v121\n"
    "v_and_b32 v171, 0xffff0000, v121\n"
    "v_lshlrev_b32 v172, 16, v122\n"
    "v_and_b32 v173, 0xffff0000, v122\n"
    "v_lshlrev_b32 v174, 16, v123\n"
    "v_and_b32 v175, 0xffff0000, v123\n"
    "v_add_f32 v152, v152, v168\n"
    "v_add_f32 v153, v153, v169\n"
    "v_add_f32 v154, v154, v170\n"
    "v_add_f32 v155, v155, v171\n"
    "v_add_f32 v156, v156, v172\n"
    "v_add_f32 v157, v157, v173\n"
    "v_add_f32 v158, v158, v174\n"
    "v_add_f32 v159, v159, v175\n"
    "s_waitcnt vmcnt(19)\n"
    "v_lshlrev_b32 v168, 16, v60\n"
    "v_and_b32 v169, 0xffff0000, v60\n"
    "v_lshlrev_b32 v170, 16, v61\n"
    "v_and_b32 v171, 0xffff0000, v61\n"
    "v_lshlrev_b32 v172, 16, v62\n"
    "v_and_b32 v173, 0xffff0000, v62\n"
    "v_lshlrev_b32 v174, 16, v63\n"
    "v_and_b32 v175, 0xffff0000, v63\n"
    "v_add_f32 v144, v144, v168\n"
    "v_add_f32 v145, v145, v169\n"
    "v_add_f32 v146, v146, v170\n"
    "v_add_f32 v147, v147, v171\n"
    "v_add_f32 v148, v148, v172\n"
    "v_add_f32 v149, v149, v173\n"
    "v_add_f32 v150, v150, v174\n"
    "v_add_f32 v151, v151, v175\n"
    "s_waitcnt vmcnt(18)\n"
    "v_lshlrev_b32 v168, 16, v124\n"
    "v_and_b32 v169, 0xffff0000, v124\n"
    "v_lshlrev_b32 v170, 16, v125\n"
    "v_and_b32 v171, 0xffff0000, v125\n"
    "v_lshlrev_b32 v172, 16, v126\n"
    "v_and_b32 v173, 0xffff0000, v126\n"
    "v_lshlrev_b32 v174, 16, v127\n"
    "v_and_b32 v175, 0xffff0000, v127\n"
    "v_add_f32 v152, v152, v168\n"
    "v_add_f32 v153, v153, v169\n"
    "v_add_f32 v154, v154, v170\n"
    "v_add_f32 v155, v155, v171\n"
    "v_add_f32 v156, v156, v172\n"
    "v_add_f32 v157, v157, v173\n"
    "v_add_f32 v158, v158, v174\n"
    "v_add_f32 v159, v159, v175\n"
    "s_waitcnt vmcnt(16)\n"
    "v_lshlrev_b32 v168, 16, v64\n"
    "v_and_b32 v169, 0xffff0000, v64\n"
    "v_lshlrev_b32 v170, 16, v65\n"
    "v_and_b32 v171, 0xffff0000, v65\n"
    "v_lshlrev_b32 v172, 16, v66\n"
    "v_and_b32 v173, 0xffff0000, v66\n"
    "v_lshlrev_b32 v174, 16, v67\n"
    "v_and_b32 v175, 0xffff0000, v67\n"
    "v_add_f32 v144, v144, v168\n"
    "v_add_f32 v145, v145, v169\n"
    "v_add_f32 v146, v146, v170\n"
    "v_add_f32 v147, v147, v171\n"
    "v_add_f32 v148, v148, v172\n"
    "v_add_f32 v149, v149, v173\n"
    "v_add_f32 v150, v150, v174\n"
    "v_add_f32 v151, v151, v175\n"
    "s_waitcnt vmcnt(15)\n"
    "v_lshlrev_b32 v168, 16, v128\n"
    "v_and_b32 v169, 0xffff0000, v128\n"
    "v_lshlrev_b32 v170, 16, v129\n"
    "v_and_b32 v171, 0xffff0000, v129\n"
    "v_lshlrev_b32 v172, 16, v130\n"
    "v_and_b32 v173, 0xffff0000, v130\n"
    "v_lshlrev_b32 v174, 16, v131\n"
    "v_and_b32 v175, 0xffff0000, v131\n"
    "v_add_f32 v152, v152, v168\n"
    "v_add_f32 v153, v153, v169\n"
    "v_add_f32 v154, v154, v170\n"
    "v_add_f32 v155, v155, v171\n"
    "v_add_f32 v156, v156, v172\n"
    "v_add_f32 v157, v157, v173\n"
    "v_add_f32 v158, v158, v174\n"
    "v_add_f32 v159, v159, v175\n"
    "s_waitcnt vmcnt(13)\n"
    "v_lshlrev_b32 v168, 16, v68\n"
    "v_and_b32 v169, 0xffff0000, v68\n"
    "v_lshlrev_b32 v170, 16, v69\n"
    "v_and_b32 v171, 0xffff0000, v69\n"
    "v_lshlrev_b32 v172, 16, v70\n"
    "v_and_b32 v173, 0xffff0000, v70\n"
    "v_lshlrev_b32 v174, 16, v71\n"
    "v_and_b32 v175, 0xffff0000, v71\n"
    "v_add_f32 v144, v144, v168\n"
    "v_add_f32 v145, v145, v169\n"
    "v_add_f32 v146, v146, v170\n"
    "v_add_f32 v147, v147, v171\n"
    "v_add_f32 v148, v148, v172\n"
    "v_add_f32 v149, v149, v173\n"
    "v_add_f32 v150, v150, v174\n"
    "v_add_f32 v151, v151, v175\n"
    "s_waitcnt vmcnt(12)\n"
    "v_lshlrev_b32 v168, 16, v132\n"
    "v_and_b32 v169, 0xffff0000, v132\n"
    "v_lshlrev_b32 v170, 16, v133\n"
    "v_and_b32 v171, 0xffff0000, v133\n"
    "v_lshlrev_b32 v172, 16, v134\n"
    "v_and_b32 v173, 0xffff0000, v134\n"
    "v_lshlrev_b32 v174, 16, v135\n"
    "v_and_b32 v175, 0xffff0000, v135\n"
    "v_add_f32 v152, v152, v168\n"
    "v_add_f32 v153, v153, v169\n"
    "v_add_f32 v154, v154, v170\n"
    "v_add_f32 v155, v155, v171\n"
    "v_add_f32 v156, v156, v172\n"
    "v_add_f32 v157, v157, v173\n"
    "v_add_f32 v158, v158, v174\n"
    "v_add_f32 v159, v159, v175\n"
    "s_waitcnt vmcnt(10)\n"
    "v_lshlrev_b32 v168, 16, v72\n"
    "v_and_b32 v169, 0xffff0000, v72\n"
    "v_lshlrev_b32 v170, 16, v73\n"
    "v_and_b32 v171, 0xffff0000, v73\n"
    "v_lshlrev_b32 v172, 16, v74\n"
    "v_and_b32 v173, 0xffff0000, v74\n"
    "v_lshlrev_b32 v174, 16, v75\n"
    "v_and_b32 v175, 0xffff0000, v75\n"
    "v_add_f32 v144, v144, v168\n"
    "v_add_f32 v145, v145, v169\n"
    "v_add_f32 v146, v146, v170\n"
    "v_add_f32 v147, v147, v171\n"
    "v_add_f32 v148, v148, v172\n"
    "v_add_f32 v149, v149, v173\n"
    "v_add_f32 v150, v150, v174\n"
    "v_add_f32 v151, v151, v175\n"
    "s_waitcnt vmcnt(9)\n"
    "v_lshlrev_b32 v168, 16, v136\n"
    "v_and_b32 v169, 0xffff0000, v136\n"
    "v_lshlrev_b32 v170, 16, v137\n"
    "v_and_b32 v171, 0xffff0000, v137\n"
    "v_lshlrev_b32 v172, 16, v138\n"
    "v_and_b32 v173, 0xffff0000, v138\n"
    "v_lshlrev_b32 v174, 16, v139\n"
    "v_and_b32 v175, 0xffff0000, v139\n"
    "v_add_f32 v152, v152, v168\n"
    "v_add_f32 v153, v153, v169\n"
    "v_add_f32 v154, v154, v170\n"
    "v_add_f32 v155, v155, v171\n"
    "v_add_f32 v156, v156, v172\n"
    "v_add_f32 v157, v157, v173\n"
    "v_add_f32 v158, v158, v174\n"
    "v_add_f32 v159, v159, v175\n"
    "s_waitcnt vmcnt(7)\n"
    "v_lshlrev_b32 v168, 16, v76\n"
    "v_and_b32 v169, 0xffff0000, v76\n"
    "v_lshlrev_b32 v170, 16, v77\n"
    "v_and_b32 v171, 0xffff0000, v77\n"
    "v_lshlrev_b32 v172, 16, v78\n"
    "v_and_b32 v173, 0xffff0000, v78\n"
    "v_lshlrev_b32 v174, 16, v79\n"
    "v_and_b32 v175, 0xffff0000, v79\n"
    "v_add_f32 v144, v144, v168\n"
    "v_add_f32 v145, v145, v169\n"
    "v_add_f32 v146, v146, v170\n"
    "v_add_f32 v147, v147, v171\n"
    "v_add_f32 v148, v148, v172\n"
    "v_add_f32 v149, v149, v173\n"
    "v_add_f32 v150, v150, v174\n"
    "v_add_f32 v151, v151, v175\n"
    "s_waitcnt vmcnt(6)\n"
    "v_lshlrev_b32 v168, 16, v140\n"
    "v_and_b32 v169, 0xffff0000, v140\n"
    "v_lshlrev_b32 v170, 16, v141\n"
    "v_and_b32 v171, 0xffff0000, v141\n"
    "v_lshlrev_b32 v172, 16, v142\n"
    "v_and_b32 v173, 0xffff0000, v142\n"
    "v_lshlrev_b32 v174, 16, v143\n"
    "v_and_b32 v175, 0xffff0000, v143\n"
    "v_add_f32 v152, v152, v168\n"
    "v_add_f32 v153, v153, v169\n"
    "v_add_f32 v154, v154, v170\n"
    "v_add_f32 v155, v155, v171\n"
    "v_add_f32 v156, v156, v172\n"
    "v_add_f32 v157, v157, v173\n"
    "v_add_f32 v158, v158, v174\n"
    "v_add_f32 v159, v159, v175\n"
    "s_add_u32 s71, s90, 0\n"
    "s_and_b32 s71, s71, 15\n"
    "s_lshl_b32 s71, s71, 12\n"
    "s_add_u32 s71, s71, s66\n"
    "buffer_load_dwordx4 a[0:3], v0, s[40:43], s71 offen offset:0\n"
    "buffer_load_dwordx4 a[4:7], v0, s[40:43], s71 offen offset:1024\n"
    "buffer_load_dwordx4 a[8:11], v0, s[40:43], s71 offen offset:2048\n"
    "buffer_load_dwordx4 a[12:15], v0, s[40:43], s71 offen offset:3072\n"
    "s_add_u32 s71, s90, 1\n"
    "s_and_b32 s71, s71, 15\n"
    "s_lshl_b32 s71, s71, 12\n"
    "s_add_u32 s71, s71, s66\n"
    "buffer_load_dwordx4 a[16:19], v0, s[40:43], s71 offen offset:0\n"
    "buffer_load_dwordx4 a[20:23], v0, s[40:43], s71 offen offset:1024\n"
    "buffer_load_dwordx4 a[24:27], v0, s[40:43], s71 offen offset:2048\n"
    "buffer_load_dwordx4 a[28:31], v0, s[40:43], s71 offen offset:3072\n"
    "v_cndmask_b32 v176, v148, v144, s[72:73]\n"
    "v_cndmask_b32 v177, v149, v145, s[72:73]\n"
    "v_cndmask_b32 v178, v150, v146, s[72:73]\n"
    "v_cndmask_b32 v179, v151, v147, s[72:73]\n"
    "v_mov_b32_dpp v180, v176 row_ror:8 row_mask:0xf bank_mask:0xf\n"
    "v_mov_b32_dpp v181, v177 row_ror:8 row_mask:0xf bank_mask:0xf\n"
    "v_mov_b32_dpp v182, v178 row_ror:8 row_mask:0xf bank_mask:0xf\n"
    "v_mov_b32_dpp v183, v179 row_ror:8 row_mask:0xf bank_mask:0xf\n"
    "v_cndmask_b32 v144, v144, v180, s[72:73]\n"
    "v_cndmask_b32 v145, v145, v181, s[72:73]\n"
    "v_cndmask_b32 v146, v146, v182, s[72:73]\n"
    "v_cndmask_b32 v147, v147, v183, s[72:73]\n"
    "v_cndmask_b32 v148, v180, v148, s[72:73]\n"
    "v_cndmask_b32 v149, v181, v149, s[72:73]\n"
    "v_cndmask_b32 v150, v182, v150, s[72:73]\n"
    "v_cndmask_b32 v151, v183, v151, s[72:73]\n"
    "v_cndmask_b32 v176, v156, v152, s[72:73]\n"
    "v_cndmask_b32 v177, v157, v153, s[72:73]\n"
    "v_cndmask_b32 v178, v158, v154, s[72:73]\n"
    "v_cndmask_b32 v179, v159, v155, s[72:73]\n"
    "v_mov_b32_dpp v180, v176 row_ror:8 row_mask:0xf bank_mask:0xf\n"
    "v_mov_b32_dpp v181, v177 row_ror:8 row_mask:0xf bank_mask:0xf\n"
    "v_mov_b32_dpp v182, v178 row_ror:8 row_mask:0xf bank_mask:0xf\n"
    "v_mov_b32_dpp v183, v179 row_ror:8 row_mask:0xf bank_mask:0xf\n"
    "v_cndmask_b32 v152, v152, v180, s[72:73]\n"
    "v_cndmask_b32 v153, v153, v181, s[72:73]\n"
    "v_cndmask_b32 v154, v154, v182, s[72:73]\n"
    "v_cndmask_b32 v155, v155, v183, s[72:73]\n"
    "v_cndmask_b32 v156, v180, v156, s[72:73]\n"
    "v_cndmask_b32 v157, v181, v157, s[72:73]\n"
    "v_cndmask_b32 v158, v182, v158, s[72:73]\n"
    "v_cndmask_b32 v159, v183, v159, s[72:73]\n"
    "s_mul_i32 s56, s52, 448\n"
    "s_add_u32 s56, s56, s55\n"
    "s_add_u32 s56, s56, 0x1c000\n"
    "v_lshlrev_b32 v8, 5, v187\n"
    "v_add_u32 v8, s56, v8\n"
    "ds_read_b128 v[192:195], v8 offset:0\n"
    "ds_read_b128 v[196:199], v8 offset:16\n"
    "v_lshlrev_b32 v168, 16, v160\n"
    "v_and_b32 v169, 0xffff0000, v160\n"
    "v_lshlrev_b32 v170, 16, v161\n"
    "v_and_b32 v171, 0xffff0000, v161\n"
    "v_lshlrev_b32 v172, 16, v162\n"
    "v_and_b32 v173, 0xffff0000, v162\n"
    "v_lshlrev_b32 v174, 16, v163\n"
    "v_and_b32 v175, 0xffff0000, v163\n"
    "s_waitcnt lgkmcnt(1)\n"
    "v_fma_f32 v144, v192, v144, v168\n"
    "v_fma_f32 v145, v193, v145, v169\n"
    "v_fma_f32 v146, v194, v146, v170\n"
    "v_fma_f32 v147, v195, v147, v171\n"
    "s_waitcnt lgkmcnt(0)\n"
    "v_fma_f32 v148, v196, v148, v172\n"
    "v_fma_f32 v149, v197, v149, v173\n"
    "v_fma_f32 v150, v198, v150, v174\n"
    "v_fma_f32 v151, v199, v151, v175\n"
    "ds_read_b128 v[192:195], v8 offset:256\n"
    "ds_read_b128 v[196:199], v8 offset:272\n"
    "v_lshlrev_b32 v168, 16, v164\n"
    "v_and_b32 v169, 0xffff0000, v164\n"
    "v_lshlrev_b32 v170, 16, v165\n"
    "v_and_b32 v171, 0xffff0000, v165\n"
    "v_lshlrev_b32 v172, 16, v166\n"
    "v_and_b32 v173, 0xffff0000, v166\n"
    "v_lshlrev_b32 v174, 16, v167\n"
    "v_and_b32 v175, 0xffff0000, v167\n"
    "s_waitcnt lgkmcnt(1)\n"
    "v_fma_f32 v152, v192, v152, v168\n"
    "v_fma_f32 v153, v193, v153, v169\n"
    "v_fma_f32 v154, v194, v154, v170\n"
    "v_fma_f32 v155, v195, v155, v171\n"
    "s_waitcnt lgkmcnt(0)\n"
    "v_fma_f32 v156, v196, v156, v172\n"
    "v_fma_f32 v157, v197, v157, v173\n"
    "v_fma_f32 v158, v198, v158, v174\n"
    "v_fma_f32 v159, v199, v159, v175\n"
    "v_add_f32 v234, v144, v145\n"
    "v_add_f32 v234, v234, v146\n"
    "v_add_f32 v234, v234, v147\n"
    "v_add_f32 v234, v234, v148\n"
    "v_add_f32 v234, v234, v149\n"
    "v_add_f32 v234, v234, v150\n"
    "v_add_f32 v234, v234, v151\n"
    "v_add_f32 v234, v234, v152\n"
    "v_add_f32 v234, v234, v153\n"
    "v_add_f32 v234, v234, v154\n"
    "v_add_f32 v234, v234, v155\n"
    "v_add_f32 v234, v234, v156\n"
    "v_add_f32 v234, v234, v157\n"
    "v_add_f32 v234, v234, v158\n"
    "v_add_f32 v234, v234, v159\n"
    "s_add_u32 s56, s55, 0x27e00\n"
    "v_lshlrev_b32 v191, 2, v186\n"
    "v_add_u32 v191, s56, v191\n"
    "s_lshl_b32 s57, s52, 5\n"
    "v_add_u32 v190, s57, v191\n"
    "v_add_f32_dpp v234, v234, v234 row_ror:8 row_mask:0xf bank_mask:0xf\n"
    "v_mov_b32 v9, v234\n"
    "s_nop 1\n"
    "v_permlane16_swap_b32 v234, v9\n"
    "v_add_f32 v234, v234, v9\n"
    "v_mov_b32 v9, v234\n"
    "s_nop 1\n"
    "v_permlane32_swap_b32 v234, v9\n"
    "v_add_f32 v234, v234, v9\n"
    "ds_write_b32 v190, v234 offset:0\n"
    "s_waitcnt lgkmcnt(0)\n"
    "s_barrier\n"
    "ds_read_b32 v236, v191 offset:0\n"
    "ds_read_b32 v237, v191 offset:32\n"
    "ds_read_b32 v238, v191 offset:64\n"
    "ds_read_b32 v239, v191 offset:96\n"
    "s_waitcnt lgkmcnt(2)\n"
    "v_add_f32 v236, v236, v237\n"
    "s_waitcnt lgkmcnt(0)\n"
    "v_add_f32 v238, v238, v239\n"
    "v_add_f32 v234, v236, v238\n"
    "v_mul_f32 v232, 0x3b124925, v234\n"
    "v_cvt_pk_bf16_f32 v160, v144, v145\n"
    "v_cvt_pk_bf16_f32 v161, v146, v147\n"
    "v_cvt_pk_bf16_f32 v162, v148, v149\n"
    "v_cvt_pk_bf16_f32 v163, v150, v151\n"
    "v_cvt_pk_bf16_f32 v164, v152, v153\n"
    "v_cvt_pk_bf16_f32 v165, v154, v155\n"
    "v_cvt_pk_bf16_f32 v166, v156, v157\n"
    "v_cvt_pk_bf16_f32 v167, v158, v159\n"
    "v_sub_f32 v144, v144, v232\n"
    "v_sub_f32 v145, v145, v232\n"
    "v_sub_f32 v146, v146, v232\n"
    "v_sub_f32 v147, v147, v232\n"
    "v_sub_f32 v148, v148, v232\n"
    "v_sub_f32 v149, v149, v232\n"
    "v_sub_f32 v150, v150, v232\n"
    "v_sub_f32 v151, v151, v232\n"
    "v_sub_f32 v152, v152, v232\n"
    "v_sub_f32 v153, v153, v232\n"
    "v_sub_f32 v154, v154, v232\n"
    "v_sub_f32 v155, v155, v232\n"
    "v_sub_f32 v156, v156, v232\n"
    "v_sub_f32 v157, v157, v232\n"
    "v_sub_f32 v158, v158, v232\n"
    "v_sub_f32 v159, v159, v232\n"
    "v_mov_b32 v10, 0\n"
    "v_cndmask_b32 v152, v10, v152, s[74:75]\n"
    "v_cndmask_b32 v153, v10, v153, s[74:75]\n"
    "v_cndmask_b32 v154, v10, v154, s[74:75]\n"
    "v_cndmask_b32 v155, v10, v155, s[74:75]\n"
    "v_cndmask_b32 v156, v10, v156, s[74:75]\n"
    "v_cndmask_b32 v157, v10, v157, s[74:75]\n"
    "v_cndmask_b32 v158, v10, v158, s[74:75]\n"
    "v_cndmask_b32 v159, v10, v159, s[74:75]\n"
    "v_mul_f32 v234, v144, v144\n"
    "v_fmac_f32 v234, v145, v145\n"
    "v_fmac_f32 v234, v146, v146\n"
    "v_fmac_f32 v234, v147, v147\n"
    "v_fmac_f32 v234, v148, v148\n"
    "v_fmac_f32 v234, v149, v149\n"
    "v_fmac_f32 v234, v150, v150\n"
    "v_fmac_f32 v234, v151, v151\n"
    "v_fmac_f32 v234, v152, v152\n"
    "v_fmac_f32 v234, v153, v153\n"
    "v_fmac_f32 v234, v154, v154\n"
    "v_fmac_f32 v234, v155, v155\n"
    "v_fmac_f32 v234, v156, v156\n"
    "v_fmac_f32 v234, v157, v157\n"
    "v_fmac_f32 v234, v158, v158\n"
    "v_fmac_f32 v234, v159, v159\n"
    "ds_read_b128 v[192:195], v8 offset:1792\n"
    "ds_read_b128 v[200:203], v8 offset:3584\n"
    "ds_read_b128 v[196:199], v8 offset:1808\n"
    "ds_read_b128 v[204:207], v8 offset:3600\n"
    "ds_read_b128 v[208:211], v8 offset:2048\n"
    "ds_read_b128 v[216:219], v8 offset:3840\n"
    "ds_read_b128 v[212:215], v8 offset:2064\n"
    "ds_read_b128 v[220:223], v8 offset:3856\n"
    "v_add_f32_dpp v234, v234, v234 row_ror:8 row_mask:0xf bank_mask:0xf\n"
    "v_mov_b32 v9, v234\n"
    "s_nop 1\n"
    "v_permlane16_swap_b32 v234, v9\n"
    "v_add_f32 v234, v234, v9\n"
    "v_mov_b32 v9, v234\n"
    "s_nop 1\n"
    "v_permlane32_swap_b32 v234, v9\n"
    "v_add_f32 v234, v234, v9\n"
    "ds_write_b32 v190, v234 offset:128\n"
    "s_waitcnt lgkmcnt(0)\n"
    "s_barrier\n"
    "ds_read_b32 v236, v191 offset:128\n"
    "ds_read_b32 v237, v191 offset:160\n"
    "ds_read_b32 v238, v191 offset:192\n"
    "ds_read_b32 v239, v191 offset:224\n"
    "s_waitcnt lgkmcnt(2)\n"
    "v_add_f32 v236, v236, v237\n"
    "s_waitcnt lgkmcnt(0)\n"
    "v_add_f32 v238, v238, v239\n"
    "v_add_f32 v234, v236, v238\n"
    "v_mov_b32 v10, 0x3727c5ac\n"
    "v_fmamk_f32 v234, v234, 0x3b124925, v10\n"
    "v_rsq_f32 v233, v234\n"
    "s_nop 0\n"
    "v_mul_f32 v144, v144, v233\n"
    "v_mul_f32 v145, v145, v233\n"
    "v_mul_f32 v146, v146, v233\n"
    "v_mul_f32 v147, v147, v233\n"
    "v_mul_f32 v148, v148, v233\n"
    "v_mul_f32 v149, v149, v233\n"
    "v_mul_f32 v150, v150, v233\n"
    "v_mul_f32 v151, v151, v233\n"
    "v_fma_f32 v168, v144, v200, v144\n"
    "v_fma_f32 v169, v145, v201, v145\n"
    "v_fma_f32 v170, v146, v202, v146\n"
    "v_fma_f32 v171, v147, v203, v147\n"
    "v_fma_f32 v172, v148, v204, v148\n"
    "v_fma_f32 v173, v149, v205, v149\n"
    "v_fma_f32 v174, v150, v206, v150\n"
    "v_fma_f32 v175, v151, v207, v151\n"
    "v_add_f32 v168, v168, v192\n"
    "v_add_f32 v169, v169, v193\n"
    "v_add_f32 v170, v170, v194\n"
    "v_add_f32 v171, v171, v195\n"
    "v_add_f32 v172, v172, v196\n"
    "v_add_f32 v173, v173, v197\n"
    "v_add_f32 v174, v174, v198\n"
    "v_add_f32 v175, v175, v199\n"
    "v_cvt_pk_bf16_f32 v144, v168, v169\n"
    "v_cvt_pk_bf16_f32 v145, v170, v171\n"
    "v_cvt_pk_bf16_f32 v146, v172, v173\n"
    "v_cvt_pk_bf16_f32 v147, v174, v175\n"
    "v_mul_f32 v152, v152, v233\n"
    "v_mul_f32 v153, v153, v233\n"
    "v_mul_f32 v154, v154, v233\n"
    "v_mul_f32 v155, v155, v233\n"
    "v_mul_f32 v156, v156, v233\n"
    "v_mul_f32 v157, v157, v233\n"
    "v_mul_f32 v158, v158, v233\n"
    "v_mul_f32 v159, v159, v233\n"
    "v_fma_f32 v168, v152, v216, v152\n"
    "v_fma_f32 v169, v153, v217, v153\n"
    "v_fma_f32 v170, v154, v218, v154\n"
    "v_fma_f32 v171, v155, v219, v155\n"
    "v_fma_f32 v172, v156, v220, v156\n"
    "v_fma_f32 v173, v157, v221, v157\n"
    "v_fma_f32 v174, v158, v222, v158\n"
    "v_fma_f32 v175, v159, v223, v159\n"
    "v_add_f32 v168, v168, v208\n"
    "v_add_f32 v169, v169, v209\n"
    "v_add_f32 v170, v170, v210\n"
    "v_add_f32 v171, v171, v211\n"
    "v_add_f32 v172, v172, v212\n"
    "v_add_f32 v173, v173, v213\n"
    "v_add_f32 v174, v174, v214\n"
    "v_add_f32 v175, v175, v215\n"
    "v_cvt_pk_bf16_f32 v152, v168, v169\n"
    "v_cvt_pk_bf16_f32 v153, v170, v171\n"
    "v_cvt_pk_bf16_f32 v154, v172, v173\n"
    "v_cvt_pk_bf16_f32 v155, v174, v175\n"
    "s_mul_i32 s56, s53, 57344\n"
    "s_lshr_b32 s57, s54, 1\n"
    "s_mul_i32 s57, s57, 14336\n"
    "s_add_u32 s56, s56, s57\n"
    "s_and_b32 s57, s54, 1\n"
    "s_lshl_b32 s57, s57, 7\n"
    "s_add_u32 s56, s56, s57\n"
    "s_mul_i32 s57, s52, 3584\n"
    "s_add_u32 s56, s56, s57\n"
    "s_add_u32 s56, s56, 0x2410000\n"
    "v_lshlrev_b32 v189, 8, v187\n"
    "v_lshl_add_u32 v189, v186, 4, v189\n"
    "v_add_u32 v8, 0x800, v189\n"
    "v_mov_b32 v7, 0x7ff00000\n"
    "v_cndmask_b32 v8, v7, v8, s[74:75]\n"
    "s_waitcnt vmcnt(8)\n"
    "buffer_store_dwordx4 v[144:147], v189, s[40:43], s56 offen offset:0 sc1\n"
    "buffer_store_dwordx4 v[152:155], v8, s[40:43], s56 offen offset:0 sc1\n"
    "s_add_u32 s58, s86, 0x25d0000\n"
    "v_add_u32 v8, 0x80, v188\n"
    "v_mov_b32 v7, 0x7ff00000\n"
    "v_cndmask_b32 v8, v7, v8, s[74:75]\n"
    "buffer_store_dwordx4 v[160:163], v188, s[40:43], s58 offen offset:0 sc1\n"
    "buffer_store_dwordx4 v[164:167], v8, s[40:43], s58 offen offset:0 sc1\n"
    "s_mul_i32 s58, s52, 15360\n"
    "s_add_u32 s59, s67, 0xc4000\n"
    "s_add_u32 s59, s59, s58\n"
    "s_add_u32 s58, s58, s55\n"
    "s_add_u32 s58, s58, 0x18800\n"
    "s_add_u32 m0, s58, 0x0\n"
    "s_add_u32 s60, s59, 0x0\n"
    "buffer_load_dwordx4 v0, s[40:43], s60 offen offset:0 lds\n"
    "buffer_load_dwordx4 v0, s[40:43], s60 offen offset:1024 lds\n"
    "buffer_load_dwordx4 v0, s[40:43], s60 offen offset:2048 lds\n"
    "buffer_load_dwordx4 v0, s[40:43], s60 offen offset:3072 lds\n"
    "s_add_u32 m0, s58, 0x1000\n"
    "s_add_u32 s60, s59, 0x1000\n"
    "buffer_load_dwordx4 v0, s[40:43], s60 offen offset:0 lds\n"
    "buffer_load_dwordx4 v0, s[40:43], s60 offen offset:1024 lds\n"
    "buffer_load_dwordx4 v0, s[40:43], s60 offen offset:2048 lds\n"
    "buffer_load_dwordx4 v0, s[40:43], s60 offen offset:3072 lds\n"
    "s_add_u32 m0, s58, 0x2000\n"
    "s_add_u32 s60, s59, 0x2000\n"
    "buffer_load_dwordx4 v0, s[40:43], s60 offen offset:0 lds\n"
    "buffer_load_dwordx4 v0, s[40:43], s60 offen offset:1024 lds\n"
    "buffer_load_dwordx4 v0, s[40:43], s60 offen offset:2048 lds\n"
    "buffer_load_dwordx4 v0, s[40:43], s60 offen offset:3072 lds\n"
    "s_add_u32 m0, s58, 0x3000\n"
    "s_add_u32 s60, s59, 0x3000\n"
    "buffer_load_dwordx4 v0, s[40:43], s60 offen offset:0 lds\n"
    "buffer_load_dwordx4 v0, s[40:43], s60 offen offset:1024 lds\n"
    "buffer_load_dwordx4 v0, s[40:43], s60 offen offset:2048 lds\n"
    "s_lshl_b32 s58, s53, 2\n"
    "s_add_u32 s58, s58, s52\n"
    "s_mul_i32 s63, s58, 14336\n"
    "s_add_u32 s63, s63, 0x2410000\n"
    "s_mul_i32 s65, s58, 28672\n"
    "s_add_u32 s65, s65, 0x2b30000\n"
    "s_mul_i32 s64, s54, 458752\n"
    "s_add_u32 s64, s64, 0x27b0000\n"
    "s_min_u32 s59, s54, 6\n"
    "s_mul_i32 s68, s59, 114688\n"
    "s_add_u32 s68, s68, 0x234c000\n"
    "v_and_b32 v2, 15, v1\n"
    "v_lshrrev_b32 v3, 4, v1\n"
    "v_add_u32 v4, s55, v0\n"
    "v_add_u32 v5, 0x10000, v4\n"
    "v_mov_b32 v6, 0xc0135761\n"
    "s_mov_b32 s62, 0\n"
    "L_h2_%=:\n"
    "buffer_load_dwordx4 v[16:19], v0, s[40:43], s63 offen offset:0 sc1\n"
    "buffer_load_dwordx4 v[20:23], v0, s[40:43], s63 offen offset:1024 sc1\n"
    "buffer_load_dwordx4 v[24:27], v0, s[40:43], s63 offen offset:2048 sc1\n"
    "buffer_load_dwordx4 v[28:31], v0, s[40:43], s63 offen offset:3072 sc1\n"
    "s_add_u32 s70, s63, 0x1000\n"
    "buffer_load_dwordx4 v[32:35], v0, s[40:43], s70 offen offset:0 sc1\n"
    "buffer_load_dwordx4 v[36:39], v0, s[40:43], s70 offen offset:1024 sc1\n"
    "buffer_load_dwordx4 v[40:43], v0, s[40:43], s70 offen offset:2048 sc1\n"
    "buffer_load_dwordx4 v[44:47], v0, s[40:43], s70 offen offset:3072 sc1\n"
    "s_add_u32 s70, s63, 0x2000\n"
    "buffer_load_dwordx4 v[48:51], v0, s[40:43], s70 offen offset:0 sc1\n"
    "buffer_load_dwordx4 v[52:55], v0, s[40:43], s70 offen offset:1024 sc1\n"
    "buffer_load_dwordx4 v[56:59], v0, s[40:43], s70 offen offset:2048 sc1\n"
    "buffer_load_dwordx4 v[60:63], v0, s[40:43], s70 offen offset:3072 sc1\n"
    "s_add_u32 s70, s63, 0x3000\n"
    "buffer_load_dwordx4 v[64:67], v0, s[40:43], s70 offen offset:0 sc1\n"
    "buffer_load_dwordx4 v[68:71], v0, s[40:43], s70 offen offset:1024 sc1\n"
    "s_waitcnt vmcnt(0)\n"
    "v_max_u32 v7, v16, v17\n"
    "v_max3_u32 v7, v18, v19, v7\n"
    "v_max3_u32 v7, v20, v21, v7\n"
    "v_max3_u32 v7, v22, v23, v7\n"
    "v_max3_u32 v7, v24, v25, v7\n"
    "v_max3_u32 v7, v26, v27, v7\n"
    "v_max3_u32 v7, v28, v29, v7\n"
    "v_max3_u32 v7, v30, v31, v7\n"
    "v_max3_u32 v7, v32, v33, v7\n"
    "v_max3_u32 v7, v34, v35, v7\n"
    "v_max3_u32 v7, v36, v37, v7\n"
    "v_max3_u32 v7, v38, v39, v7\n"
    "v_max3_u32 v7, v40, v41, v7\n"
    "v_max3_u32 v7, v42, v43, v7\n"
    "v_max3_u32 v7, v44, v45, v7\n"
    "v_max3_u32 v7, v46, v47, v7\n"
    "v_max3_u32 v7, v48, v49, v7\n"
    "v_max3_u32 v7, v50, v51, v7\n"
    "v_max3_u32 v7, v52, v53, v7\n"
    "v_max3_u32 v7, v54, v55, v7\n"
    "v_max3_u32 v7, v56, v57, v7\n"
    "v_max3_u32 v7, v58, v59, v7\n"
    "v_max3_u32 v7, v60, v61, v7\n"
    "v_max3_u32 v7, v62, v63, v7\n"
    "v_max3_u32 v7, v64, v65, v7\n"
    "v_max3_u32 v7, v66, v67, v7\n"
    "v_max3_u32 v7, v68, v69, v7\n"
    "v_max3_u32 v7, v70, v71, v7\n"
    "v_cmp_eq_u32 vcc, -1, v7\n"
    "s_add_u32 s62, s62, 1\n"
    "s_cmp_gt_u32 s62, 0x40000\n"
    "s_cbranch_scc1 L_h2_done_%=\n"
    "s_cbranch_vccz L_h2_done_%=\n"
    "s_sleep 1\n"
    "s_branch L_h2_%=\n"
    "L_h2_done_%=:\n"
    "s_barrier\n"
    "ds_read_b128 v[72:75], v4 offset:0\n"
    "ds_read_b128 v[76:79], v4 offset:1024\n"
    "ds_read_b128 v[80:83], v4 offset:2048\n"
    "ds_read_b128 v[84:87], v4 offset:3072\n"
    "ds_read_b128 v[88:91], v4 offset:4096\n"
    "ds_read_b128 v[92:95], v4 offset:5120\n"
    "s_waitcnt lgkmcnt(5)\n"
    "v_mfma_f32_16x16x32_bf16 v[104:107], v[16:19], v[72:75], 0\n"
    "ds_read_b128 v[96:99], v4 offset:6144\n"
    "s_waitcnt lgkmcnt(5)\n"
    "v_mfma_f32_16x16x32_bf16 v[108:111], v[16:19], v[76:79], 0\n"
    "ds_read_b128 v[100:103], v4 offset:7168\n"
    "s_waitcnt lgkmcnt(5)\n"
    "v_mfma_f32_16x16x32_bf16 v[112:115], v[16:19], v[80:83], 0\n"
    "ds_read_b128 v[72:75], v4 offset:8192\n"
    "s_add_u32 s71, s90, 2\n"
    "s_and_b32 s71, s71, 15\n"
    "s_lshl_b32 s71, s71, 12\n"
    "s_add_u32 s71, s71, s66\n"
    "buffer_load_dwordx4 a[32:35], v0, s[40:43], s71 offen offset:0\n"
    "s_waitcnt lgkmcnt(5)\n"
    "v_mfma_f32_16x16x32_bf16 v[116:119], v[16:19], v[84:87], 0\n"
    "ds_read_b128 v[76:79], v4 offset:9216\n"
    "s_waitcnt lgkmcnt(5)\n"
    "v_mfma_f32_16x16x32_bf16 v[120:123], v[16:19], v[88:91], 0\n"
    "ds_read_b128 v[80:83], v4 offset:10240\n"
    "s_waitcnt lgkmcnt(5)\n"
    "v_mfma_f32_16x16x32_bf16 v[124:127], v[16:19], v[92:95], 0\n"
    "ds_read_b128 v[84:87], v4 offset:11264\n"
    "buffer_load_dwordx4 a[36:39], v0, s[40:43], s71 offen offset:1024\n"
    "s_waitcnt lgkmcnt(5)\n"
    "v_mfma_f32_16x16x32_bf16 v[128:131], v[16:19], v[96:99], 0\n"
    "ds_read_b128 v[88:91], v4 offset:12288\n"
    "s_waitcnt lgkmcnt(5)\n"
    "v_mfma_f32_16x16x32_bf16 v[104:107], v[20:23], v[100:103], v[104:107]\n"
    "ds_read_b128 v[92:95], v4 offset:13312\n"
    "s_waitcnt lgkmcnt(5)\n"
    "v_mfma_f32_16x16x32_bf16 v[108:111], v[20:23], v[72:75], v[108:111]\n"
    "ds_read_b128 v[96:99], v4 offset:14336\n"
    "buffer_load_dwordx4 a[40:43], v0, s[40:43], s71 offen offset:2048\n"
    "s_waitcnt lgkmcnt(5)\n"
    "v_mfma_f32_16x16x32_bf16 v[112:115], v[20:23], v[76:79], v[112:115]\n"
    "ds_read_b128 v[100:103], v4 offset:15360\n"
    "s_waitcnt lgkmcnt(5)\n"
    "v_mfma_f32_16x16x32_bf16 v[116:119], v[20:23], v[80:83], v[116:119]\n"
    "ds_read_b128 v[72:75], v4 offset:16384\n"
    "s_waitcnt lgkmcnt(5)\n"
    "v_mfma_f32_16x16x32_bf16 v[120:123], v[20:23], v[84:87], v[120:123]\n"
    "ds_read_b128 v[76:79], v4 offset:17408\n"
    "buffer_load_dwordx4 a[44:47], v0, s[40:43], s71 offen offset:3072\n"
    "s_waitcnt lgkmcnt(5)\n"
    "v_mfma_f32_16x16x32_bf16 v[124:127], v[20:23], v[88:91], v[124:127]\n"
    "ds_read_b128 v[80:83], v4 offset:18432\n"
    "s_waitcnt lgkmcnt(5)\n"
    "v_mfma_f32_16x16x32_bf16 v[128:131], v[20:23], v[92:95], v[128:131]\n"
    "ds_read_b128 v[84:87], v4 offset:19456\n"
    "s_waitcnt lgkmcnt(5)\n"
    "v_mfma_f32_16x16x32_bf16 v[104:107], v[24:27], v[96:99], v[104:107]\n"
    "ds_read_b128 v[88:91], v4 offset:20480\n"
    "s_add_u32 s71, s90, 3\n"
    "s_and_b32 s71, s71, 15\n"
    "s_lshl_b32 s71, s71, 12\n"
    "s_add_u32 s71, s71, s66\n"
    "buffer_load_dwordx4 a[48:51], v0, s[40:43], s71 offen offset:0\n"
    "s_waitcnt lgkmcnt(5)\n"
    "v_mfma_f32_16x16x32_bf16 v[108:111], v[24:27], v[100:103], v[108:111]\n"
    "ds_read_b128 v[92:95], v4 offset:21504\n"
    "s_waitcnt lgkmcnt(5)\n"
    "v_mfma_f32_16x16x32_bf16 v[112:115], v[24:27], v[72:75], v[112:115]\n"
    "ds_read_b128 v[96:99], v4 offset:22528\n"
    "s_waitcnt lgkmcnt(5)\n"
    "v_mfma_f32_16x16x32_bf16 v[116:119], v[24:27], v[76:79], v[116:119]\n"
    "ds_read_b128 v[100:103], v4 offset:23552\n"
    "buffer_load_dwordx4 a[52:55], v0, s[40:43], s71 offen offset:1024\n"
    "s_waitcnt lgkmcnt(5)\n"
    "v_mfma_f32_16x16x32_bf16 v[120:123], v[24:27], v[80:83], v[120:123]\n"
    "ds_read_b128 v[72:75], v4 offset:24576\n"
    "s_waitcnt lgkmcnt(5)\n"
    "v_mfma_f32_16x16x32_bf16 v[124:127], v[24:27], v[84:87], v[124:127]\n"
    "ds_read_b128 v[76:79], v4 offset:25600\n"
    "s_waitcnt lgkmcnt(5)\n"
    "v_mfma_f32_16x16x32_bf16 v[128:131], v[24:27], v[88:91], v[128:131]\n"
    "ds_read_b128 v[80:83], v4 offset:26624\n"
    "buffer_load_dwordx4 a[56:59], v0, s[40:43], s71 offen offset:2048\n"
    "s_waitcnt lgkmcnt(5)\n"
    "v_mfma_f32_16x16x32_bf16 v[104:107], v[28:31], v[92:95], v[104:107]\n"
    "ds_read_b128 v[84:87], v4 offset:27648\n"
    "s_waitcnt lgkmcnt(5)\n"
    "v_mfma_f32_16x16x32_bf16 v[108:111], v[28:31], v[96:99], v[108:111]\n"
    "ds_read_b128 v[88:91], v4 offset:28672\n"
    "s_waitcnt lgkmcnt(5)\n"
    "v_mfma_f32_16x16x32_bf16 v[112:115], v[28:31], v[100:103], v[112:115]\n"
    "ds_read_b128 v[92:95], v4 offset:29696\n"
    "buffer_load_dwordx4 a[60:63], v0, s[40:43], s71 offen offset:3072\n"
    "s_waitcnt lgkmcnt(5)\n"
    "v_mfma_f32_16x16x32_bf16 v[116:119], v[28:31], v[72:75], v[116:119]\n"
    "ds_read_b128 v[96:99], v4 offset:30720\n"
    "s_waitcnt lgkmcnt(5)\n"
    "v_mfma_f32_16x16x32_bf16 v[120:123], v[28:31], v[76:79], v[120:123]\n"
    "ds_read_b128 v[100:103], v4 offset:31744\n"
    "s_waitcnt lgkmcnt(5)\n"
    "v_mfma_f32_16x16x32_bf16 v[124:127], v[28:31], v[80:83], v[124:127]\n"
    "ds_read_b128 v[72:75], v4 offset:32768\n"
    "s_add_u32 s71, s90, 4\n"
    "s_and_b32 s71, s71, 15\n"
    "s_lshl_b32 s71, s71, 12\n"
    "s_add_u32 s71, s71, s66\n"
    "buffer_load_dwordx4 a[64:67], v0, s[40:43], s71 offen offset:0\n"
    "s_waitcnt lgkmcnt(5)\n"
    "v_mfma_f32_16x16x32_bf16 v[128:131], v[28:31], v[84:87], v[128:131]\n"
    "ds_read_b128 v[76:79], v4 offset:33792\n"
    "s_waitcnt lgkmcnt(5)\n"
    "v_mfma_f32_16x16x32_bf16 v[104:107], v[32:35], v[88:91], v[104:107]\n"
    "ds_read_b128 v[80:83], v4 offset:34816\n"
    "s_waitcnt lgkmcnt(5)\n"
    "v_mfma_f32_16x16x32_bf16 v[108:111], v[32:35], v[92:95], v[108:111]\n"
    "ds_read_b128 v[84:87], v4 offset:35840\n"
    "buffer_load_dwordx4 a[68:71], v0, s[40:43], s71 offen offset:1024\n"
    "s_waitcnt lgkmcnt(5)\n"
    "v_mfma_f32_16x16x32_bf16 v[112:115], v[32:35], v[96:99], v[112:115]\n"
    "ds_read_b128 v[88:91], v4 offset:36864\n"
    "s_waitcnt lgkmcnt(5)\n"
    "v_mfma_f32_16x16x32_bf16 v[116:119], v[32:35], v[100:103], v[116:119]\n"
    "ds_read_b128 v[92:95], v4 offset:37888\n"
    "s_waitcnt lgkmcnt(5)\n"
    "v_mfma_f32_16x16x32_bf16 v[120:123], v[32:35], v[72:75], v[120:123]\n"
    "ds_read_b128 v[96:99], v4 offset:38912\n"
    "buffer_load_dwordx4 a[72:75], v0, s[40:43], s71 offen offset:2048\n"
    "s_waitcnt lgkmcnt(5)\n"
    "v_mfma_f32_16x16x32_bf16 v[124:127], v[32:35], v[76:79], v[124:127]\n"
    "ds_read_b128 v[100:103], v4 offset:39936\n"
    "s_waitcnt lgkmcnt(5)\n"
    "v_mfma_f32_16x16x32_bf16 v[128:131], v[32:35], v[80:83], v[128:131]\n"
    "ds_read_b128 v[72:75], v4 offset:40960\n"
    "s_waitcnt lgkmcnt(5)\n"
    "v_mfma_f32_16x16x32_bf16 v[104:107], v[36:39], v[84:87], v[104:107]\n"
    "ds_read_b128 v[76:79], v4 offset:41984\n"
    "buffer_load_dwordx4 a[76:79], v0, s[40:43], s71 offen offset:3072\n"
    "s_waitcnt lgkmcnt(5)\n"
    "v_mfma_f32_16x16x32_bf16 v[108:111], v[36:39], v[88:91], v[108:111]\n"
    "ds_read_b128 v[80:83], v4 offset:43008\n"
    "s_waitcnt lgkmcnt(5)\n"
    "v_mfma_f32_16x16x32_bf16 v[112:115], v[36:39], v[92:95], v[112:115]\n"
    "ds_read_b128 v[84:87], v4 offset:44032\n"
    "s_waitcnt lgkmcnt(5)\n"
    "v_mfma_f32_16x16x32_bf16 v[116:119], v[36:39], v[96:99], v[116:119]\n"
    "ds_read_b128 v[88:91], v4 offset:45056\n"
    "s_add_u32 s71, s90, 5\n"
    "s_and_b32 s71, s71, 15\n"
    "s_lshl_b32 s71, s71, 12\n"
    "s_add_u32 s71, s71, s66\n"
    "buffer_load_dwordx4 a[80:83], v0, s[40:43], s71 offen offset:0\n"
    "s_waitcnt lgkmcnt(5)\n"
    "v_mfma_f32_16x16x32_bf16 v[120:123], v[36:39], v[100:103], v[120:123]\n"
    "ds_read_b128 v[92:95], v4 offset:46080\n"
    "s_waitcnt lgkmcnt(5)\n"
    "v_mfma_f32_16x16x32_bf16 v[124:127], v[36:39], v[72:75], v[124:127]\n"
    "ds_read_b128 v[96:99], v4 offset:47104\n"
    "s_waitcnt lgkmcnt(5)\n"
    "v_mfma_f32_16x16x32_bf16 v[128:131], v[36:39], v[76:79], v[128:131]\n"
    "ds_read_b128 v[100:103], v4 offset:48128\n"
    "buffer_load_dwordx4 a[84:87], v0, s[40:43], s71 offen offset:1024\n"
    "s_waitcnt lgkmcnt(5)\n"
    "v_mfma_f32_16x16x32_bf16 v[104:107], v[40:43], v[80:83], v[104:107]\n"
    "ds_read_b128 v[72:75], v4 offset:49152\n"
    "s_waitcnt lgkmcnt(5)\n"
    "v_mfma_f32_16x16x32_bf16 v[108:111], v[40:43], v[84:87], v[108:111]\n"
    "ds_read_b128 v[76:79], v4 offset:50176\n"
    "s_waitcnt lgkmcnt(5)\n"
    "v_mfma_f32_16x16x32_bf16 v[112:115], v[40:43], v[88:91], v[112:115]\n"
    "ds_read_b128 v[80:83], v4 offset:51200\n"
    "buffer_load_dwordx4 a[88:91], v0, s[40:43], s71 offen offset:2048\n"
    "s_waitcnt lgkmcnt(5)\n"
    "v_mfma_f32_16x16x32_bf16 v[116:119], v[40:43], v[92:95], v[116:119]\n"
    "ds_read_b128 v[84:87], v4 offset:52224\n"
    "s_waitcnt lgkmcnt(5)\n"
    "v_mfma_f32_16x16x32_bf16 v[120:123], v[40:43], v[96:99], v[120:123]\n"
    "ds_read_b128 v[88:91], v4 offset:53248\n"
    "s_waitcnt lgkmcnt(5)\n"
    "v_mfma_f32_16x16x32_bf16 v[124:127], v[40:43], v[100:103], v[124:127]\n"
    "ds_read_b128 v[92:95], v4 offset:54272\n"
    "buffer_load_dwordx4 a[92:95], v0, s[40:43], s71 offen offset:3072\n"
    "s_waitcnt lgkmcnt(5)\n"
    "v_mfma_f32_16x16x32_bf16 v[128:131], v[40:43], v[72:75], v[128:131]\n"
    "ds_read_b128 v[96:99], v4 offset:55296\n"
    "s_waitcnt lgkmcnt(5)\n"
    "v_mfma_f32_16x16x32_bf16 v[104:107], v[44:47], v[76:79], v[104:107]\n"
    "ds_read_b128 v[100:103], v4 offset:56320\n"
    "s_waitcnt lgkmcnt(5)\n"
    "v_mfma_f32_16x16x32_bf16 v[108:111], v[44:47], v[80:83], v[108:111]\n"
    "ds_read_b128 v[72:75], v4 offset:57344\n"
    "s_add_u32 s71, s90, 6\n"
    "s_and_b32 s71, s71, 15\n"
    "s_lshl_b32 s71, s71, 12\n"
    "s_add_u32 s71, s71, s66\n"
    "buffer_load_dwordx4 a[96:99], v0, s[40:43], s71 offen offset:0\n"
    "s_waitcnt lgkmcnt(5)\n"
    "v_mfma_f32_16x16x32_bf16 v[112:115], v[44:47], v[84:87], v[112:115]\n"
    "ds_read_b128 v[76:79], v4 offset:58368\n"
    "s_waitcnt lgkmcnt(5)\n"
    "v_mfma_f32_16x16x32_bf16 v[116:119], v[44:47], v[88:91], v[116:119]\n"
    "ds_read_b128 v[80:83], v4 offset:59392\n"
    "s_waitcnt lgkmcnt(5)\n"
    "v_mfma_f32_16x16x32_bf16 v[120:123], v[44:47], v[92:95], v[120:123]\n"
    "ds_read_b128 v[84:87], v4 offset:60416\n"
    "buffer_load_dwordx4 a[100:103], v0, s[40:43], s71 offen offset:1024\n"
    "s_waitcnt lgkmcnt(5)\n"
    "v_mfma_f32_16x16x32_bf16 v[124:127], v[44:47], v[96:99], v[124:127]\n"
    "ds_read_b128 v[88:91], v4 offset:61440\n"
    "s_waitcnt lgkmcnt(5)\n"
    "v_mfma_f32_16x16x32_bf16 v[128:131], v[44:47], v[100:103], v[128:131]\n"
    "ds_read_b128 v[92:95], v4 offset:62464\n"
    "s_waitcnt lgkmcnt(5)\n"
    "v_mfma_f32_16x16x32_bf16 v[104:107], v[48:51], v[72:75], v[104:107]\n"
    "ds_read_b128 v[96:99], v4 offset:63488\n"
    "buffer_load_dwordx4 a[104:107], v0, s[40:43], s71 offen offset:2048\n"
    "s_waitcnt lgkmcnt(5)\n"
    "v_mfma_f32_16x16x32_bf16 v[108:111], v[48:51], v[76:79], v[108:111]\n"
    "ds_read_b128 v[100:103], v4 offset:64512\n"
    "s_waitcnt lgkmcnt(5)\n"
    "v_mfma_f32_16x16x32_bf16 v[112:115], v[48:51], v[80:83], v[112:115]\n"
    "ds_read_b128 v[72:75], v5 offset:0\n"
    "s_waitcnt lgkmcnt(5)\n"
    "v_mfma_f32_16x16x32_bf16 v[116:119], v[48:51], v[84:87], v[116:119]\n"
    "ds_read_b128 v[76:79], v5 offset:1024\n"
    "buffer_load_dwordx4 a[108:111], v0, s[40:43], s71 offen offset:3072\n"
    "s_waitcnt lgkmcnt(5)\n"
    "v_mfma_f32_16x16x32_bf16 v[120:123], v[48:51], v[88:91], v[120:123]\n"
    "ds_read_b128 v[80:83], v5 offset:2048\n"
    "s_waitcnt lgkmcnt(5)\n"
    "v_mfma_f32_16x16x32_bf16 v[124:127], v[48:51], v[92:95], v[124:127]\n"
    "ds_read_b128 v[84:87], v5 offset:3072\n"
    "s_waitcnt lgkmcnt(5)\n"
    "v_mfma_f32_16x16x32_bf16 v[128:131], v[48:51], v[96:99], v[128:131]\n"
    "ds_read_b128 v[88:91], v5 offset:4096\n"
    "s_add_u32 s71, s90, 7\n"
    "s_and_b32 s71, s71, 15\n"
    "s_lshl_b32 s71, s71, 12\n"
    "s_add_u32 s71, s71, s66\n"
    "buffer_load_dwordx4 a[112:115], v0, s[40:43], s71 offen offset:0\n"
    "s_waitcnt lgkmcnt(5)\n"
    "v_mfma_f32_16x16x32_bf16 v[104:107], v[52:55], v[100:103], v[104:107]\n"
    "ds_read_b128 v[92:95], v5 offset:5120\n"
    "s_waitcnt lgkmcnt(5)\n"
    "v_mfma_f32_16x16x32_bf16 v[108:111], v[52:55], v[72:75], v[108:111]\n"
    "ds_read_b128 v[96:99], v5 offset:6144\n"
    "s_waitcnt lgkmcnt(5)\n"
    "v_mfma_f32_16x16x32_bf16 v[112:115], v[52:55], v[76:79], v[112:115]\n"
    "ds_read_b128 v[100:103], v5 offset:7168\n"
    "buffer_load_dwordx4 a[116:119], v0, s[40:43], s71 offen offset:1024\n"
    "s_waitcnt lgkmcnt(5)\n"
    "v_mfma_f32_16x16x32_bf16 v[116:119], v[52:55], v[80:83], v[116:119]\n"
    "ds_read_b128 v[72:75], v5 offset:8192\n"
    "s_waitcnt lgkmcnt(5)\n"
    "v_mfma_f32_16x16x32_bf16 v[120:123], v[52:55], v[84:87], v[120:123]\n"
    "ds_read_b128 v[76:79], v5 offset:9216\n"
    "s_waitcnt lgkmcnt(5)\n"
    "v_mfma_f32_16x16x32_bf16 v[124:127], v[52:55], v[88:91], v[124:127]\n"
    "ds_read_b128 v[80:83], v5 offset:10240\n"
    "buffer_load_dwordx4 a[120:123], v0, s[40:43], s71 offen offset:2048\n"
    "s_waitcnt lgkmcnt(5)\n"
    "v_mfma_f32_16x16x32_bf16 v[128:131], v[52:55], v[92:95], v[128:131]\n"
    "ds_read_b128 v[84:87], v5 offset:11264\n"
    "s_waitcnt lgkmcnt(5)\n"
    "v_mfma_f32_16x16x32_bf16 v[104:107], v[56:59], v[96:99], v[104:107]\n"
    "ds_read_b128 v[88:91], v5 offset:12288\n"
    "s_waitcnt lgkmcnt(5)\n"
    "v_mfma_f32_16x16x32_bf16 v[108:111], v[56:59], v[100:103], v[108:111]\n"
    "ds_read_b128 v[92:95], v5 offset:13312\n"
    "buffer_load_dwordx4 a[124:127], v0, s[40:43], s71 offen offset:3072\n"
    "s_waitcnt lgkmcnt(5)\n"
    "v_mfma_f32_16x16x32_bf16 v[112:115], v[56:59], v[72:75], v[112:115]\n"
    "ds_read_b128 v[96:99], v5 offset:14336\n"
    "s_waitcnt lgkmcnt(5)\n"
    "v_mfma_f32_16x16x32_bf16 v[116:119], v[56:59], v[76:79], v[116:119]\n"
    "ds_read_b128 v[100:103], v5 offset:15360\n"
    "s_waitcnt lgkmcnt(5)\n"
    "v_mfma_f32_16x16x32_bf16 v[120:123], v[56:59], v[80:83], v[120:123]\n"
    "ds_read_b128 v[72:75], v5 offset:16384\n"
    "s_add_u32 s71, s90, 8\n"
    "s_and_b32 s71, s71, 15\n"
    "s_lshl_b32 s71, s71, 12\n"
    "s_add_u32 s71, s71, s66\n"
    "buffer_load_dwordx4 a[128:131], v0, s[40:43], s71 offen offset:0\n"
    "s_waitcnt lgkmcnt(5)\n"
    "v_mfma_f32_16x16x32_bf16 v[124:127], v[56:59], v[84:87], v[124:127]\n"
    "ds_read_b128 v[76:79], v5 offset:17408\n"
    "s_waitcnt lgkmcnt(5)\n"
    "v_mfma_f32_16x16x32_bf16 v[128:131], v[56:59], v[88:91], v[128:131]\n"
    "ds_read_b128 v[80:83], v5 offset:18432\n"
    "s_waitcnt lgkmcnt(5)\n"
    "v_mfma_f32_16x16x32_bf16 v[104:107], v[60:63], v[92:95], v[104:107]\n"
    "ds_read_b128 v[84:87], v5 offset:19456\n"
    "buffer_load_dwordx4 a[132:135], v0, s[40:43], s71 offen offset:1024\n"
    "s_waitcnt lgkmcnt(5)\n"
    "v_mfma_f32_16x16x32_bf16 v[108:111], v[60:63], v[96:99], v[108:111]\n"
    "ds_read_b128 v[88:91], v5 offset:20480\n"
    "s_waitcnt lgkmcnt(5)\n"
    "v_mfma_f32_16x16x32_bf16 v[112:115], v[60:63], v[100:103], v[112:115]\n"
    "ds_read_b128 v[92:95], v5 offset:21504\n"
    "s_waitcnt lgkmcnt(5)\n"
    "v_mfma_f32_16x16x32_bf16 v[116:119], v[60:63], v[72:75], v[116:119]\n"
    "ds_read_b128 v[96:99], v5 offset:22528\n"
    "buffer_load_dwordx4 a[136:139], v0, s[40:43], s71 offen offset:2048\n"
    "s_waitcnt lgkmcnt(5)\n"
    "v_mfma_f32_16x16x32_bf16 v[120:123], v[60:63], v[76:79], v[120:123]\n"
    "ds_read_b128 v[100:103], v5 offset:23552\n"
    "s_waitcnt lgkmcnt(5)\n"
    "v_mfma_f32_16x16x32_bf16 v[124:127], v[60:63], v[80:83], v[124:127]\n"
    "ds_read_b128 v[72:75], v5 offset:24576\n"
    "s_waitcnt lgkmcnt(5)\n"
    "v_mfma_f32_16x16x32_bf16 v[128:131], v[60:63], v[84:87], v[128:131]\n"
    "ds_read_b128 v[76:79], v5 offset:25600\n"
    "buffer_load_dwordx4 a[140:143], v0, s[40:43], s71 offen offset:3072\n"
    "s_waitcnt lgkmcnt(5)\n"
    "v_mfma_f32_16x16x32_bf16 v[104:107], v[64:67], v[88:91], v[104:107]\n"
    "ds_read_b128 v[80:83], v5 offset:26624\n"
    "s_waitcnt lgkmcnt(5)\n"
    "v_mfma_f32_16x16x32_bf16 v[108:111], v[64:67], v[92:95], v[108:111]\n"
    "ds_read_b128 v[84:87], v5 offset:27648\n"
    "s_waitcnt lgkmcnt(5)\n"
    "v_mfma_f32_16x16x32_bf16 v[112:115], v[64:67], v[96:99], v[112:115]\n"
    "ds_read_b128 v[88:91], v5 offset:28672\n"
    "s_add_u32 s71, s90, 9\n"
    "s_and_b32 s71, s71, 15\n"
    "s_lshl_b32 s71, s71, 12\n"
    "s_add_u32 s71, s71, s66\n"
    "buffer_load_dwordx4 a[144:147], v0, s[40:43], s71 offen offset:0\n"
    "s_waitcnt lgkmcnt(5)\n"
    "v_mfma_f32_16x16x32_bf16 v[116:119], v[64:67], v[100:103], v[116:119]\n"
    "ds_read_b128 v[92:95], v5 offset:29696\n"
    "s_waitcnt lgkmcnt(5)\n"
    "v_mfma_f32_16x16x32_bf16 v[120:123], v[64:67], v[72:75], v[120:123]\n"
    "ds_read_b128 v[96:99], v5 offset:30720\n"
    "s_waitcnt lgkmcnt(5)\n"
    "v_mfma_f32_16x16x32_bf16 v[124:127], v[64:67], v[76:79], v[124:127]\n"
    "ds_read_b128 v[100:103], v5 offset:31744\n"
    "buffer_load_dwordx4 a[148:151], v0, s[40:43], s71 offen offset:1024\n"
    "s_waitcnt lgkmcnt(5)\n"
    "v_mfma_f32_16x16x32_bf16 v[128:131], v[64:67], v[80:83], v[128:131]\n"
    "ds_read_b128 v[72:75], v5 offset:32768\n"
    "s_waitcnt lgkmcnt(5)\n"
    "v_mfma_f32_16x16x32_bf16 v[104:107], v[68:71], v[84:87], v[104:107]\n"
    "ds_read_b128 v[76:79], v5 offset:33792\n"
    "s_waitcnt lgkmcnt(5)\n"
    "v_mfma_f32_16x16x32_bf16 v[108:111], v[68:71], v[88:91], v[108:111]\n"
    "buffer_load_dwordx4 a[152:155], v0, s[40:43], s71 offen offset:2048\n"
    "s_waitcnt lgkmcnt(4)\n"
    "v_mfma_f32_16x16x32_bf16 v[112:115], v[68:71], v[92:95], v[112:115]\n"
    "s_waitcnt lgkmcnt(3)\n"
    "v_mfma_f32_16x16x32_bf16 v[116:119], v[68:71], v[96:99], v[116:119]\n"
    "s_waitcnt lgkmcnt(2)\n"
    "v_mfma_f32_16x16x32_bf16 v[120:123], v[68:71], v[100:103], v[120:123]\n"
    "buffer_load_dwordx4 a[156:159], v0, s[40:43], s71 offen offset:3072\n"
    "s_waitcnt lgkmcnt(1)\n"
    "v_mfma_f32_16x16x32_bf16 v[124:127], v[68:71], v[72:75], v[124:127]\n"
    "s_waitcnt lgkmcnt(0)\n"
    "v_mfma_f32_16x16x32_bf16 v[128:131], v[68:71], v[76:79], v[128:131]\n"
    "s_waitcnt lgkmcnt(0)\n"
    "s_barrier\n"
    "s_add_u32 s67, s67, 0xc4000\n"
    "s_mul_i32 s69, s52, 10\n"
    "s_min_u32 s69, s69, 28\n"
    "s_lshl_b32 s69, s69, 10\n"
    "s_nop 7\n"
    "v_mul_f32 v168, v104, v104\n"
    "v_mul_f32 v169, v105, v105\n"
    "v_mul_f32 v170, v106, v106\n"
    "v_mul_f32 v171, v107, v107\n"
    "v_fmamk_f32 v168, v168, 0xbdd2d3e8, v6\n"
    "v_fmamk_f32 v169, v169, 0xbdd2d3e8, v6\n"
    "v_fmamk_f32 v170, v170, 0xbdd2d3e8, v6\n"
    "v_fmamk_f32 v171, v171, 0xbdd2d3e8, v6\n"
    "v_mul_f32 v168, v104, v168\n"
    "v_mul_f32 v169, v105, v169\n"
    "v_mul_f32 v170, v106, v170\n"
    "v_mul_f32 v171, v107, v171\n"
    "v_exp_f32 v168, v168\n"
    "v_exp_f32 v169, v169\n"
    "v_exp_f32 v170, v170\n"
    "v_exp_f32 v171, v171\n"
    "v_add_f32 v168, 1.0, v168\n"
    "v_add_f32 v169, 1.0, v169\n"
    "v_add_f32 v170, 1.0, v170\n"
    "v_add_f32 v171, 1.0, v171\n"
    "v_rcp_f32 v168, v168\n"
    "v_rcp_f32 v169, v169\n"
    "v_rcp_f32 v170, v170\n"
    "v_rcp_f32 v171, v171\n"
    "v_mul_f32 v104, v104, v168\n"
    "v_mul_f32 v105, v105, v169\n"
    "v_mul_f32 v106, v106, v170\n"
    "v_mul_f32 v107, v107, v171\n"
    "s_add_u32 s56, s69, 0x0\n"
    "s_add_u32 m0, s55, s56\n"
    "s_add_u32 s57, s67, s56\n"
    "s_add_u32 s57, s57, 0xf000\n"
    "buffer_load_dwordx4 v0, s[40:43], s57 offen offset:0 lds\n"
    "buffer_load_dwordx4 v0, s[40:43], s57 offen offset:1024 lds\n"
    "buffer_load_dwordx4 v0, s[40:43], s57 offen offset:2048 lds\n"
    "buffer_load_dwordx4 v0, s[40:43], s57 offen offset:3072 lds\n"
    "v_mul_f32 v168, v108, v108\n"
    "v_mul_f32 v169, v109, v109\n"
    "v_mul_f32 v170, v110, v110\n"
    "v_mul_f32 v171, v111, v111\n"
    "v_fmamk_f32 v168, v168, 0xbdd2d3e8, v6\n"
    "v_fmamk_f32 v169, v169, 0xbdd2d3e8, v6\n"
    "v_fmamk_f32 v170, v170, 0xbdd2d3e8, v6\n"
    "v_fmamk_f32 v171, v171, 0xbdd2d3e8, v6\n"
    "v_mul_f32 v168, v108, v168\n"
    "v_mul_f32 v169, v109, v169\n"
    "v_mul_f32 v170, v110, v170\n"
    "v_mul_f32 v171, v111, v171\n"
    "v_exp_f32 v168, v168\n"
    "v_exp_f32 v169, v169\n"
    "v_exp_f32 v170, v170\n"
    "v_exp_f32 v171, v171\n"
    "v_add_f32 v168, 1.0, v168\n"
    "v_add_f32 v169, 1.0, v169\n"
    "v_add_f32 v170, 1.0, v170\n"
    "v_add_f32 v171, 1.0, v171\n"
    "v_rcp_f32 v168, v168\n"
    "v_rcp_f32 v169, v169\n"
    "v_rcp_f32 v170, v170\n"
    "v_rcp_f32 v171, v171\n"
    "v_mul_f32 v108, v108, v168\n"
    "v_mul_f32 v109, v109, v169\n"
    "v_mul_f32 v110, v110, v170\n"
    "v_mul_f32 v111, v111, v171\n"
    "s_add_u32 s56, s69, 0x1000\n"
    "s_add_u32 m0, s55, s56\n"
    "s_add_u32 s57, s67, s56\n"
    "s_add_u32 s57, s57, 0xf000\n"
    "buffer_load_dwordx4 v0, s[40:43], s57 offen offset:0 lds\n"
    "buffer_load_dwordx4 v0, s[40:43], s57 offen offset:1024 lds\n"
    "buffer_load_dwordx4 v0, s[40:43], s57 offen offset:2048 lds\n"
    "buffer_load_dwordx4 v0, s[40:43], s57 offen offset:3072 lds\n"
    "v_mul_f32 v168, v112, v112\n"
    "v_mul_f32 v169, v113, v113\n"
    "v_mul_f32 v170, v114, v114\n"
    "v_mul_f32 v171, v115, v115\n"
    "v_fmamk_f32 v168, v168, 0xbdd2d3e8, v6\n"
    "v_fmamk_f32 v169, v169, 0xbdd2d3e8, v6\n"
    "v_fmamk_f32 v170, v170, 0xbdd2d3e8, v6\n"
    "v_fmamk_f32 v171, v171, 0xbdd2d3e8, v6\n"
    "v_mul_f32 v168, v112, v168\n"
    "v_mul_f32 v169, v113, v169\n"
    "v_mul_f32 v170, v114, v170\n"
    "v_mul_f32 v171, v115, v171\n"
    "v_exp_f32 v168, v168\n"
    "v_exp_f32 v169, v169\n"
    "v_exp_f32 v170, v170\n"
    "v_exp_f32 v171, v171\n"
    "v_add_f32 v168, 1.0, v168\n"
    "v_add_f32 v169, 1.0, v169\n"
    "v_add_f32 v170, 1.0, v170\n"
    "v_add_f32 v171, 1.0, v171\n"
    "v_rcp_f32 v168, v168\n"
    "v_rcp_f32 v169, v169\n"
    "v_rcp_f32 v170, v170\n"
    "v_rcp_f32 v171, v171\n"
    "v_mul_f32 v112, v112, v168\n"
    "v_mul_f32 v113, v113, v169\n"
    "v_mul_f32 v114, v114, v170\n"
    "v_mul_f32 v115, v115, v171\n"
    "s_add_u32 s56, s69, 0x2000\n"
    "s_add_u32 m0, s55, s56\n"
    "s_add_u32 s57, s67, s56\n"
    "s_add_u32 s57, s57, 0xf000\n"
    "buffer_load_dwordx4 v0, s[40:43], s57 offen offset:0 lds\n"
    "buffer_load_dwordx4 v0, s[40:43], s57 offen offset:1024 lds\n"
    "v_mul_f32 v168, v116, v116\n"
    "v_mul_f32 v169, v117, v117\n"
    "v_mul_f32 v170, v118, v118\n"
    "v_mul_f32 v171, v119, v119\n"
    "v_fmamk_f32 v168, v168, 0xbdd2d3e8, v6\n"
    "v_fmamk_f32 v169, v169, 0xbdd2d3e8, v6\n"
    "v_fmamk_f32 v170, v170, 0xbdd2d3e8, v6\n"
    "v_fmamk_f32 v171, v171, 0xbdd2d3e8, v6\n"
    "v_mul_f32 v168, v116, v168\n"
    "v_mul_f32 v169, v117, v169\n"
    "v_mul_f32 v170, v118, v170\n"
    "v_mul_f32 v171, v119, v171\n"
    "v_exp_f32 v168, v168\n"
    "v_exp_f32 v169, v169\n"
    "v_exp_f32 v170, v170\n"
    "v_exp_f32 v171, v171\n"
    "v_add_f32 v168, 1.0, v168\n"
    "v_add_f32 v169, 1.0, v169\n"
    "v_add_f32 v170, 1.0, v170\n"
    "v_add_f32 v171, 1.0, v171\n"
    "v_rcp_f32 v168, v168\n"
    "v_rcp_f32 v169, v169\n"
    "v_rcp_f32 v170, v170\n"
    "v_rcp_f32 v171, v171\n"
    "v_mul_f32 v116, v116, v168\n"
    "v_mul_f32 v117, v117, v169\n"
    "v_mul_f32 v118, v118, v170\n"
    "v_mul_f32 v119, v119, v171\n"
    "v_mul_f32 v168, v120, v120\n"
    "v_mul_f32 v169, v121, v121\n"
    "v_mul_f32 v170, v122, v122\n"
    "v_mul_f32 v171, v123, v123\n"
    "v_fmamk_f32 v168, v168, 0xbdd2d3e8, v6\n"
    "v_fmamk_f32 v169, v169, 0xbdd2d3e8, v6\n"
    "v_fmamk_f32 v170, v170, 0xbdd2d3e8, v6\n"
    "v_fmamk_f32 v171, v171, 0xbdd2d3e8, v6\n"
    "v_mul_f32 v168, v120, v168\n"
    "v_mul_f32 v169, v121, v169\n"
    "v_mul_f32 v170, v122, v170\n"
    "v_mul_f32 v171, v123, v171\n"
    "v_exp_f32 v168, v168\n"
    "v_exp_f32 v169, v169\n"
    "v_exp_f32 v170, v170\n"
    "v_exp_f32 v171, v171\n"
    "v_add_f32 v168, 1.0, v168\n"
    "v_add_f32 v169, 1.0, v169\n"
    "v_add_f32 v170, 1.0, v170\n"
    "v_add_f32 v171, 1.0, v171\n"
    "v_rcp_f32 v168, v168\n"
    "v_rcp_f32 v169, v169\n"
    "v_rcp_f32 v170, v170\n"
    "v_rcp_f32 v171, v171\n"
    "v_mul_f32 v120, v120, v168\n"
    "v_mul_f32 v121, v121, v169\n"
    "v_mul_f32 v122, v122, v170\n"
    "v_mul_f32 v123, v123, v171\n"
    "v_mul_f32 v168, v124, v124\n"
    "v_mul_f32 v169, v125, v125\n"
    "v_mul_f32 v170, v126, v126\n"
    "v_mul_f32 v171, v127, v127\n"
    "v_fmamk_f32 v168, v168, 0xbdd2d3e8, v6\n"
    "v_fmamk_f32 v169, v169, 0xbdd2d3e8, v6\n"
    "v_fmamk_f32 v170, v170, 0xbdd2d3e8, v6\n"
    "v_fmamk_f32 v171, v171, 0xbdd2d3e8, v6\n"
    "v_mul_f32 v168, v124, v168\n"
    "v_mul_f32 v169, v125, v169\n"
    "v_mul_f32 v170, v126, v170\n"
    "v_mul_f32 v171, v127, v171\n"
    "v_exp_f32 v168, v168\n"
    "v_exp_f32 v169, v169\n"
    "v_exp_f32 v170, v170\n"
    "v_exp_f32 v171, v171\n"
    "v_add_f32 v168, 1.0, v168\n"
    "v_add_f32 v169, 1.0, v169\n"
    "v_add_f32 v170, 1.0, v170\n"
    "v_add_f32 v171, 1.0, v171\n"
    "v_rcp_f32 v168, v168\n"
    "v_rcp_f32 v169, v169\n"
    "v_rcp_f32 v170, v170\n"
    "v_rcp_f32 v171, v171\n"
    "v_mul_f32 v124, v124, v168\n"
    "v_mul_f32 v125, v125, v169\n"
    "v_mul_f32 v126, v126, v170\n"
    "v_mul_f32 v127, v127, v171\n"
    "v_mul_f32 v168, v128, v128\n"
    "v_mul_f32 v169, v129, v129\n"
    "v_mul_f32 v170, v130, v130\n"
    "v_mul_f32 v171, v131, v131\n"
    "v_fmamk_f32 v168, v168, 0xbdd2d3e8, v6\n"
    "v_fmamk_f32 v169, v169, 0xbdd2d3e8, v6\n"
    "v_fmamk_f32 v170, v170, 0xbdd2d3e8, v6\n"
    "v_fmamk_f32 v171, v171, 0xbdd2d3e8, v6\n"
    "v_mul_f32 v168, v128, v168\n"
    "v_mul_f32 v169, v129, v169\n"
    "v_mul_f32 v170, v130, v170\n"
    "v_mul_f32 v171, v131, v171\n"
    "v_exp_f32 v168, v168\n"
    "v_exp_f32 v169, v169\n"
    "v_exp_f32 v170, v170\n"
    "v_exp_f32 v171, v171\n"
    "v_add_f32 v168, 1.0, v168\n"
    "v_add_f32 v169, 1.0, v169\n"
    "v_add_f32 v170, 1.0, v170\n"
    "v_add_f32 v171, 1.0, v171\n"
    "v_rcp_f32 v168, v168\n"
    "v_rcp_f32 v169, v169\n"
    "v_rcp_f32 v170, v170\n"
    "v_rcp_f32 v171, v171\n"
    "v_mul_f32 v128, v128, v168\n"
    "v_mul_f32 v129, v129, v169\n"
    "v_mul_f32 v130, v130, v170\n"
    "v_mul_f32 v131, v131, v171\n"
    "v_add_f32 v160, v104, v108\n"
    "v_add_f32 v162, v105, v109\n"
    "v_add_f32 v164, v106, v110\n"
    "v_add_f32 v166, v107, v111\n"
    "v_add_f32 v160, v160, v112\n"
    "v_add_f32 v162, v162, v113\n"
    "v_add_f32 v164, v164, v114\n"
    "v_add_f32 v166, v166, v115\n"
    "v_add_f32 v160, v160, v116\n"
    "v_add_f32 v162, v162, v117\n"
    "v_add_f32 v164, v164, v118\n"
    "v_add_f32 v166, v166, v119\n"
    "v_add_f32 v160, v160, v120\n"
    "v_add_f32 v162, v162, v121\n"
    "v_add_f32 v164, v164, v122\n"
    "v_add_f32 v166, v166, v123\n"
    "v_add_f32 v160, v160, v124\n"
    "v_add_f32 v162, v162, v125\n"
    "v_add_f32 v164, v164, v126\n"
    "v_add_f32 v166, v166, v127\n"
    "v_add_f32 v160, v160, v128\n"
    "v_add_f32 v162, v162, v129\n"
    "v_add_f32 v164, v164, v130\n"
    "v_add_f32 v166, v166, v131\n"
    "v_add_f32_dpp v160, v160, v160 row_ror:1 row_mask:0xf bank_mask:0xf\n"
    "v_add_f32_dpp v162, v162, v162 row_ror:1 row_mask:0xf bank_mask:0xf\n"
    "v_add_f32_dpp v164, v164, v164 row_ror:1 row_mask:0xf bank_mask:0xf\n"
    "v_add_f32_dpp v166, v166, v166 row_ror:1 row_mask:0xf bank_mask:0xf\n"
    "v_add_f32_dpp v160, v160, v160 row_ror:2 row_mask:0xf bank_mask:0xf\n"
    "v_add_f32_dpp v162, v162, v162 row_ror:2 row_mask:0xf bank_mask:0xf\n"
    "v_add_f32_dpp v164, v164, v164 row_ror:2 row_mask:0xf bank_mask:0xf\n"
    "v_add_f32_dpp v166, v166, v166 row_ror:2 row_mask:0xf bank_mask:0xf\n"
    "v_add_f32_dpp v160, v160, v160 row_ror:4 row_mask:0xf bank_mask:0xf\n"
    "v_add_f32_dpp v162, v162, v162 row_ror:4 row_mask:0xf bank_mask:0xf\n"
    "v_add_f32_dpp v164, v164, v164 row_ror:4 row_mask:0xf bank_mask:0xf\n"
    "v_add_f32_dpp v166, v166, v166 row_ror:4 row_mask:0xf bank_mask:0xf\n"
    "v_add_f32_dpp v160, v160, v160 row_ror:8 row_mask:0xf bank_mask:0xf\n"
    "v_add_f32_dpp v162, v162, v162 row_ror:8 row_mask:0xf bank_mask:0xf\n"
    "v_add_f32_dpp v164, v164, v164 row_ror:8 row_mask:0xf bank_mask:0xf\n"
    "v_add_f32_dpp v166, v166, v166 row_ror:8 row_mask:0xf bank_mask:0xf\n"
    "v_mul_f32 v160, 0x3c124925, v160\n"
    "v_mul_f32 v162, 0x3c124925, v162\n"
    "v_mul_f32 v164, 0x3c124925, v164\n"
    "v_mul_f32 v166, 0x3c124925, v166\n"
    "v_sub_f32 v168, v104, v160\n"
    "v_sub_f32 v169, v105, v162\n"
    "v_sub_f32 v170, v106, v164\n"
    "v_sub_f32 v171, v107, v166\n"
    "v_mul_f32 v161, v168, v168\n"
    "v_mul_f32 v163, v169, v169\n"
    "v_mul_f32 v165, v170, v170\n"
    "v_mul_f32 v167, v171, v171\n"
    "v_sub_f32 v168, v108, v160\n"
    "v_sub_f32 v169, v109, v162\n"
    "v_sub_f32 v170, v110, v164\n"
    "v_sub_f32 v171, v111, v166\n"
    "v_fmac_f32 v161, v168, v168\n"
    "v_fmac_f32 v163, v169, v169\n"
    "v_fmac_f32 v165, v170, v170\n"
    "v_fmac_f32 v167, v171, v171\n"
    "v_sub_f32 v168, v112, v160\n"
    "v_sub_f32 v169, v113, v162\n"
    "v_sub_f32 v170, v114, v164\n"
    "v_sub_f32 v171, v115, v166\n"
    "v_fmac_f32 v161, v168, v168\n"
    "v_fmac_f32 v163, v169, v169\n"
    "v_fmac_f32 v165, v170, v170\n"
    "v_fmac_f32 v167, v171, v171\n"
    "v_sub_f32 v168, v116, v160\n"
    "v_sub_f32 v169, v117, v162\n"
    "v_sub_f32 v170, v118, v164\n"
    "v_sub_f32 v171, v119, v166\n"
    "v_fmac_f32 v161, v168, v168\n"
    "v_fmac_f32 v163, v169, v169\n"
    "v_fmac_f32 v165, v170, v170\n"
    "v_fmac_f32 v167, v171, v171\n"
    "v_sub_f32 v168, v120, v160\n"
    "v_sub_f32 v169, v121, v162\n"
    "v_sub_f32 v170, v122, v164\n"
    "v_sub_f32 v171, v123, v166\n"
    "v_fmac_f32 v161, v168, v168\n"
    "v_fmac_f32 v163, v169, v169\n"
    "v_fmac_f32 v165, v170, v170\n"
    "v_fmac_f32 v167, v171, v171\n"
    "v_sub_f32 v168, v124, v160\n"
    "v_sub_f32 v169, v125, v162\n"
    "v_sub_f32 v170, v126, v164\n"
    "v_sub_f32 v171, v127, v166\n"
    "v_fmac_f32 v161, v168, v168\n"
    "v_fmac_f32 v163, v169, v169\n"
    "v_fmac_f32 v165, v170, v170\n"
    "v_fmac_f32 v167, v171, v171\n"
    "v_sub_f32 v168, v128, v160\n"
    "v_sub_f32 v169, v129, v162\n"
    "v_sub_f32 v170, v130, v164\n"
    "v_sub_f32 v171, v131, v166\n"
    "v_fmac_f32 v161, v168, v168\n"
    "v_fmac_f32 v163, v169, v169\n"
    "v_fmac_f32 v165, v170, v170\n"
    "v_fmac_f32 v167, v171, v171\n"
    "v_add_f32_dpp v161, v161, v161 row_ror:1 row_mask:0xf bank_mask:0xf\n"
    "v_add_f32_dpp v163, v163, v163 row_ror:1 row_mask:0xf bank_mask:0xf\n"
    "v_add_f32_dpp v165, v165, v165 row_ror:1 row_mask:0xf bank_mask:0xf\n"
    "v_add_f32_dpp v167, v167, v167 row_ror:1 row_mask:0xf bank_mask:0xf\n"
    "v_add_f32_dpp v161, v161, v161 row_ror:2 row_mask:0xf bank_mask:0xf\n"
    "v_add_f32_dpp v163, v163, v163 row_ror:2 row_mask:0xf bank_mask:0xf\n"
    "v_add_f32_dpp v165, v165, v165 row_ror:2 row_mask:0xf bank_mask:0xf\n"
    "v_add_f32_dpp v167, v167, v167 row_ror:2 row_mask:0xf bank_mask:0xf\n"
    "v_add_f32_dpp v161, v161, v161 row_ror:4 row_mask:0xf bank_mask:0xf\n"
    "v_add_f32_dpp v163, v163, v163 row_ror:4 row_mask:0xf bank_mask:0xf\n"
    "v_add_f32_dpp v165, v165, v165 row_ror:4 row_mask:0xf bank_mask:0xf\n"
    "v_add_f32_dpp v167, v167, v167 row_ror:4 row_mask:0xf bank_mask:0xf\n"
    "v_add_f32_dpp v161, v161, v161 row_ror:8 row_mask:0xf bank_mask:0xf\n"
    "v_add_f32_dpp v163, v163, v163 row_ror:8 row_mask:0xf bank_mask:0xf\n"
    "v_add_f32_dpp v165, v165, v165 row_ror:8 row_mask:0xf bank_mask:0xf\n"
    "v_add_f32_dpp v167, v167, v167 row_ror:8 row_mask:0xf bank_mask:0xf\n"
    "s_lshl_b32 s58, s53, 12\n"
    "s_add_u32 s58, s58, 0x2790000\n"
    "s_lshl_b32 s59, s54, 9\n"
    "s_lshl_b32 s60, s52, 7\n"
    "s_add_u32 s59, s59, s60\n"
    "s_add_u32 s59, s59, s58\n"
    "v_lshlrev_b32 v8, 5, v3\n"
    "v_mov_b32 v9, 0x7ff00000\n"
    "v_cmp_eq_u32 vcc, 0, v2\n"
    "s_nop 1\n"
    "v_cndmask_b32 v8, v9, v8, vcc\n"
    "buffer_store_dwordx4 v[160:163], v8, s[40:43], s59 offen offset:0 sc1\n"
    "buffer_store_dwordx4 v[164:167], v8, s[40:43], s59 offen offset:16 sc1\n"
    "s_waitcnt vmcnt(2)\n"
    "s_barrier\n"
    "v_add_u32 v11, 0x20000, v4\n"
    "ds_read_b128 v[72:75], v5 offset:34816\n"
    "ds_read_b128 v[76:79], v5 offset:35840\n"
    "ds_read_b128 v[80:83], v5 offset:36864\n"
    "ds_read_b128 v[84:87], v5 offset:37888\n"
    "ds_read_b128 v[88:91], v5 offset:38912\n"
    "ds_read_b128 v[92:95], v5 offset:39936\n"
    "s_waitcnt lgkmcnt(5)\n"
    "v_mfma_f32_16x16x32_bf16 v[132:135], v[72:75], v[16:19], 0\n"
    "ds_read_b128 v[96:99], v5 offset:40960\n"
    "s_waitcnt lgkmcnt(5)\n"
    "v_mfma_f32_16x16x32_bf16 v[136:139], v[76:79], v[16:19], 0\n"
    "ds_read_b128 v[100:103], v5 offset:41984\n"
    "s_waitcnt lgkmcnt(5)\n"
    "v_mfma_f32_16x16x32_bf16 v[140:143], v[80:83], v[16:19], 0\n"
    "ds_read_b128 v[72:75], v5 offset:43008\n"
    "s_add_u32 s71, s90, 10\n"
    "s_and_b32 s71, s71, 15\n"
    "s_lshl_b32 s71, s71, 12\n"
    "s_add_u32 s71, s71, s66\n"
    "buffer_load_dwordx4 a[160:163], v0, s[40:43], s71 offen offset:0\n"
    "s_waitcnt lgkmcnt(5)\n"
    "v_mfma_f32_16x16x32_bf16 v[144:147], v[84:87], v[16:19], 0\n"
    "ds_read_b128 v[76:79], v5 offset:44032\n"
    "s_waitcnt lgkmcnt(5)\n"
    "v_mfma_f32_16x16x32_bf16 v[148:151], v[88:91], v[16:19], 0\n"
    "ds_read_b128 v[80:83], v5 offset:45056\n"
    "s_waitcnt lgkmcnt(5)\n"
    "v_mfma_f32_16x16x32_bf16 v[152:155], v[92:95], v[16:19], 0\n"
    "ds_read_b128 v[84:87], v5 offset:46080\n"
    "buffer_load_dwordx4 a[164:167], v0, s[40:43], s71 offen offset:1024\n"
    "s_waitcnt lgkmcnt(5)\n"
    "v_mfma_f32_16x16x32_bf16 v[156:159], v[96:99], v[16:19], 0\n"
    "ds_read_b128 v[88:91], v5 offset:47104\n"
    "s_waitcnt lgkmcnt(5)\n"
    "v_mfma_f32_16x16x32_bf16 v[132:135], v[100:103], v[20:23], v[132:135]\n"
    "ds_read_b128 v[92:95], v5 offset:48128\n"
    "s_waitcnt lgkmcnt(5)\n"
    "v_mfma_f32_16x16x32_bf16 v[136:139], v[72:75], v[20:23], v[136:139]\n"
    "ds_read_b128 v[96:99], v5 offset:49152\n"
    "buffer_load_dwordx4 a[168:171], v0, s[40:43], s71 offen offset:2048\n"
    "s_waitcnt lgkmcnt(5)\n"
    "v_mfma_f32_16x16x32_bf16 v[140:143], v[76:79], v[20:23], v[140:143]\n"
    "ds_read_b128 v[100:103], v5 offset:50176\n"
    "s_waitcnt lgkmcnt(5)\n"
    "v_mfma_f32_16x16x32_bf16 v[144:147], v[80:83], v[20:23], v[144:147]\n"
    "ds_read_b128 v[72:75], v5 offset:51200\n"
    "s_waitcnt lgkmcnt(5)\n"
    "v_mfma_f32_16x16x32_bf16 v[148:151], v[84:87], v[20:23], v[148:151]\n"
    "ds_read_b128 v[76:79], v5 offset:52224\n"
    "buffer_load_dwordx4 a[172:175], v0, s[40:43], s71 offen offset:3072\n"
    "s_waitcnt lgkmcnt(5)\n"
    "v_mfma_f32_16x16x32_bf16 v[152:155], v[88:91], v[20:23], v[152:155]\n"
    "ds_read_b128 v[80:83], v5 offset:53248\n"
    "s_waitcnt lgkmcnt(5)\n"
    "v_mfma_f32_16x16x32_bf16 v[156:159], v[92:95], v[20:23], v[156:159]\n"
    "ds_read_b128 v[84:87], v5 offset:54272\n"
    "s_waitcnt lgkmcnt(5)\n"
    "v_mfma_f32_16x16x32_bf16 v[132:135], v[96:99], v[24:27], v[132:135]\n"
    "ds_read_b128 v[88:91], v5 offset:55296\n"
    "s_add_u32 s71, s90, 11\n"
    "s_and_b32 s71, s71, 15\n"
    "s_lshl_b32 s71, s71, 12\n"
    "s_add_u32 s71, s71, s66\n"
    "buffer_load_dwordx4 a[176:179], v0, s[40:43], s71 offen offset:0\n"
    "s_waitcnt lgkmcnt(5)\n"
    "v_mfma_f32_16x16x32_bf16 v[136:139], v[100:103], v[24:27], v[136:139]\n"
    "ds_read_b128 v[92:95], v5 offset:56320\n"
    "s_waitcnt lgkmcnt(5)\n"
    "v_mfma_f32_16x16x32_bf16 v[140:143], v[72:75], v[24:27], v[140:143]\n"
    "ds_read_b128 v[96:99], v5 offset:57344\n"
    "s_waitcnt lgkmcnt(5)\n"
    "v_mfma_f32_16x16x32_bf16 v[144:147], v[76:79], v[24:27], v[144:147]\n"
    "ds_read_b128 v[100:103], v5 offset:58368\n"
    "buffer_load_dwordx4 a[180:183], v0, s[40:43], s71 offen offset:1024\n"
    "s_waitcnt lgkmcnt(5)\n"
    "v_mfma_f32_16x16x32_bf16 v[148:151], v[80:83], v[24:27], v[148:151]\n"
    "ds_read_b128 v[72:75], v5 offset:59392\n"
    "s_waitcnt lgkmcnt(5)\n"
    "v_mfma_f32_16x16x32_bf16 v[152:155], v[84:87], v[24:27], v[152:155]\n"
    "ds_read_b128 v[76:79], v5 offset:60416\n"
    "s_waitcnt lgkmcnt(5)\n"
    "v_mfma_f32_16x16x32_bf16 v[156:159], v[88:91], v[24:27], v[156:159]\n"
    "ds_read_b128 v[80:83], v5 offset:61440\n"
    "buffer_load_dwordx4 a[184:187], v0, s[40:43], s71 offen offset:2048\n"
    "s_waitcnt lgkmcnt(5)\n"
    "v_mfma_f32_16x16x32_bf16 v[132:135], v[92:95], v[28:31], v[132:135]\n"
    "ds_read_b128 v[84:87], v5 offset:62464\n"
    "s_waitcnt lgkmcnt(5)\n"
    "v_mfma_f32_16x16x32_bf16 v[136:139], v[96:99], v[28:31], v[136:139]\n"
    "ds_read_b128 v[88:91], v5 offset:63488\n"
    "s_waitcnt lgkmcnt(5)\n"
    "v_mfma_f32_16x16x32_bf16 v[140:143], v[100:103], v[28:31], v[140:143]\n"
    "ds_read_b128 v[92:95], v5 offset:64512\n"
    "buffer_load_dwordx4 a[188:191], v0, s[40:43], s71 offen offset:3072\n"
    "s_waitcnt lgkmcnt(5)\n"
    "v_mfma_f32_16x16x32_bf16 v[144:147], v[72:75], v[28:31], v[144:147]\n"
    "ds_read_b128 v[96:99], v11 offset:0\n"
    "s_waitcnt lgkmcnt(5)\n"
    "v_mfma_f32_16x16x32_bf16 v[148:151], v[76:79], v[28:31], v[148:151]\n"
    "ds_read_b128 v[100:103], v11 offset:1024\n"
    "s_waitcnt lgkmcnt(5)\n"
    "v_mfma_f32_16x16x32_bf16 v[152:155], v[80:83], v[28:31], v[152:155]\n"
    "ds_read_b128 v[72:75], v11 offset:2048\n"
    "s_add_u32 s71, s90, 12\n"
    "s_and_b32 s71, s71, 15\n"
    "s_lshl_b32 s71, s71, 12\n"
    "s_add_u32 s71, s71, s66\n"
    "buffer_load_dwordx4 a[192:195], v0, s[40:43], s71 offen offset:0\n"
    "s_waitcnt lgkmcnt(5)\n"
    "v_mfma_f32_16x16x32_bf16 v[156:159], v[84:87], v[28:31], v[156:159]\n"
    "ds_read_b128 v[76:79], v11 offset:3072\n"
    "s_waitcnt lgkmcnt(5)\n"
    "v_mfma_f32_16x16x32_bf16 v[132:135], v[88:91], v[32:35], v[132:135]\n"
    "ds_read_b128 v[80:83], v11 offset:4096\n"
    "s_waitcnt lgkmcnt(5)\n"
    "v_mfma_f32_16x16x32_bf16 v[136:139], v[92:95], v[32:35], v[136:139]\n"
    "ds_read_b128 v[84:87], v11 offset:5120\n"
    "buffer_load_dwordx4 a[196:199], v0, s[40:43], s71 offen offset:1024\n"
    "s_waitcnt lgkmcnt(5)\n"
    "v_mfma_f32_16x16x32_bf16 v[140:143], v[96:99], v[32:35], v[140:143]\n"
    "ds_read_b128 v[88:91], v11 offset:6144\n"
    "s_waitcnt lgkmcnt(5)\n"
    "v_mfma_f32_16x16x32_bf16 v[144:147], v[100:103], v[32:35], v[144:147]\n"
    "ds_read_b128 v[92:95], v11 offset:7168\n"
    "s_waitcnt lgkmcnt(5)\n"
    "v_mfma_f32_16x16x32_bf16 v[148:151], v[72:75], v[32:35], v[148:151]\n"
    "ds_read_b128 v[96:99], v11 offset:8192\n"
    "buffer_load_dwordx4 a[200:203], v0, s[40:43], s71 offen offset:2048\n"
    "s_waitcnt lgkmcnt(5)\n"
    "v_mfma_f32_16x16x32_bf16 v[152:155], v[76:79], v[32:35], v[152:155]\n"
    "ds_read_b128 v[100:103], v11 offset:9216\n"
    "s_waitcnt lgkmcnt(5)\n"
    "v_mfma_f32_16x16x32_bf16 v[156:159], v[80:83], v[32:35], v[156:159]\n"
    "ds_read_b128 v[72:75], v11 offset:10240\n"
    "s_waitcnt lgkmcnt(5)\n"
    "v_mfma_f32_16x16x32_bf16 v[132:135], v[84:87], v[36:39], v[132:135]\n"
    "ds_read_b128 v[76:79], v11 offset:11264\n"
    "buffer_load_dwordx4 a[204:207], v0, s[40:43], s71 offen offset:3072\n"
    "s_waitcnt lgkmcnt(5)\n"
    "v_mfma_f32_16x16x32_bf16 v[136:139], v[88:91], v[36:39], v[136:139]\n"
    "ds_read_b128 v[80:83], v11 offset:12288\n"
    "s_waitcnt lgkmcnt(5)\n"
    "v_mfma_f32_16x16x32_bf16 v[140:143], v[92:95], v[36:39], v[140:143]\n"
    "ds_read_b128 v[84:87], v11 offset:13312\n"
    "s_waitcnt lgkmcnt(5)\n"
    "v_mfma_f32_16x16x32_bf16 v[144:147], v[96:99], v[36:39], v[144:147]\n"
    "ds_read_b128 v[88:91], v11 offset:14336\n"
    "s_add_u32 s71, s90, 13\n"
    "s_and_b32 s71, s71, 15\n"
    "s_lshl_b32 s71, s71, 12\n"
    "s_add_u32 s71, s71, s66\n"
    "buffer_load_dwordx4 a[208:211], v0, s[40:43], s71 offen offset:0\n"
    "s_waitcnt lgkmcnt(5)\n"
    "v_mfma_f32_16x16x32_bf16 v[148:151], v[100:103], v[36:39], v[148:151]\n"
    "ds_read_b128 v[92:95], v11 offset:15360\n"
    "s_waitcnt lgkmcnt(5)\n"
    "v_mfma_f32_16x16x32_bf16 v[152:155], v[72:75], v[36:39], v[152:155]\n"
    "ds_read_b128 v[96:99], v11 offset:16384\n"
    "s_waitcnt lgkmcnt(5)\n"
    "v_mfma_f32_16x16x32_bf16 v[156:159], v[76:79], v[36:39], v[156:159]\n"
    "ds_read_b128 v[100:103], v11 offset:17408\n"
    "buffer_load_dwordx4 a[212:215], v0, s[40:43], s71 offen offset:1024\n"
    "s_waitcnt lgkmcnt(5)\n"
    "v_mfma_f32_16x16x32_bf16 v[132:135], v[80:83], v[40:43], v[132:135]\n"
    "ds_read_b128 v[72:75], v11 offset:18432\n"
    "s_waitcnt lgkmcnt(5)\n"
    "v_mfma_f32_16x16x32_bf16 v[136:139], v[84:87], v[40:43], v[136:139]\n"
    "ds_read_b128 v[76:79], v11 offset:19456\n"
    "s_waitcnt lgkmcnt(5)\n"
    "v_mfma_f32_16x16x32_bf16 v[140:143], v[88:91], v[40:43], v[140:143]\n"
    "ds_read_b128 v[80:83], v11 offset:20480\n"
    "buffer_load_dwordx4 a[216:219], v0, s[40:43], s71 offen offset:2048\n"
    "s_waitcnt lgkmcnt(5)\n"
    "v_mfma_f32_16x16x32_bf16 v[144:147], v[92:95], v[40:43], v[144:147]\n"
    "ds_read_b128 v[84:87], v11 offset:21504\n"
    "s_waitcnt lgkmcnt(5)\n"
    "v_mfma_f32_16x16x32_bf16 v[148:151], v[96:99], v[40:43], v[148:151]\n"
    "ds_read_b128 v[88:91], v11 offset:22528\n"
    "s_waitcnt lgkmcnt(5)\n"
    "v_mfma_f32_16x16x32_bf16 v[152:155], v[100:103], v[40:43], v[152:155]\n"
    "ds_read_b128 v[92:95], v11 offset:23552\n"
    "buffer_load_dwordx4 a[220:223], v0, s[40:43], s71 offen offset:3072\n"
    "s_waitcnt lgkmcnt(5)\n"
    "v_mfma_f32_16x16x32_bf16 v[156:159], v[72:75], v[40:43], v[156:159]\n"
    "ds_read_b128 v[96:99], v11 offset:24576\n"
    "s_waitcnt lgkmcnt(5)\n"
    "v_mfma_f32_16x16x32_bf16 v[132:135], v[76:79], v[44:47], v[132:135]\n"
    "ds_read_b128 v[100:103], v11 offset:25600\n"
    "s_waitcnt lgkmcnt(5)\n"
    "v_mfma_f32_16x16x32_bf16 v[136:139], v[80:83], v[44:47], v[136:139]\n"
    "ds_read_b128 v[72:75], v11 offset:26624\n"
    "s_add_u32 s71, s90, 14\n"
    "s_and_b32 s71, s71, 15\n"
    "s_lshl_b32 s71, s71, 12\n"
    "s_add_u32 s71, s71, s66\n"
    "buffer_load_dwordx4 a[224:227], v0, s[40:43], s71 offen offset:0\n"
    "s_waitcnt lgkmcnt(5)\n"
    "v_mfma_f32_16x16x32_bf16 v[140:143], v[84:87], v[44:47], v[140:143]\n"
    "ds_read_b128 v[76:79], v11 offset:27648\n"
    "s_waitcnt lgkmcnt(5)\n"
    "v_mfma_f32_16x16x32_bf16 v[144:147], v[88:91], v[44:47], v[144:147]\n"
    "ds_read_b128 v[80:83], v11 offset:28672\n"
    "s_waitcnt lgkmcnt(5)\n"
    "v_mfma_f32_16x16x32_bf16 v[148:151], v[92:95], v[44:47], v[148:151]\n"
    "ds_read_b128 v[84:87], v11 offset:29696\n"
    "buffer_load_dwordx4 a[228:231], v0, s[40:43], s71 offen offset:1024\n"
    "s_waitcnt lgkmcnt(5)\n"
    "v_mfma_f32_16x16x32_bf16 v[152:155], v[96:99], v[44:47], v[152:155]\n"
    "ds_read_b128 v[88:91], v4 offset:0\n"
    "s_waitcnt lgkmcnt(5)\n"
    "v_mfma_f32_16x16x32_bf16 v[156:159], v[100:103], v[44:47], v[156:159]\n"
    "ds_read_b128 v[92:95], v4 offset:1024\n"
    "s_waitcnt lgkmcnt(5)\n"
    "v_mfma_f32_16x16x32_bf16 v[132:135], v[72:75], v[48:51], v[132:135]\n"
    "ds_read_b128 v[96:99], v4 offset:2048\n"
    "buffer_load_dwordx4 a[232:235], v0, s[40:43], s71 offen offset:2048\n"
    "s_waitcnt lgkmcnt(5)\n"
    "v_mfma_f32_16x16x32_bf16 v[136:139], v[76:79], v[48:51], v[136:139]\n"
    "ds_read_b128 v[100:103], v4 offset:3072\n"
    "s_waitcnt lgkmcnt(5)\n"
    "v_mfma_f32_16x16x32_bf16 v[140:143], v[80:83], v[48:51], v[140:143]\n"
    "ds_read_b128 v[72:75], v4 offset:4096\n"
    "s_waitcnt lgkmcnt(5)\n"
    "v_mfma_f32_16x16x32_bf16 v[144:147], v[84:87], v[48:51], v[144:147]\n"
    "ds_read_b128 v[76:79], v4 offset:5120\n"
    "buffer_load_dwordx4 a[236:239], v0, s[40:43], s71 offen offset:3072\n"
    "s_waitcnt lgkmcnt(5)\n"
    "v_mfma_f32_16x16x32_bf16 v[148:151], v[88:91], v[48:51], v[148:151]\n"
    "ds_read_b128 v[80:83], v4 offset:6144\n"
    "s_waitcnt lgkmcnt(5)\n"
    "v_mfma_f32_16x16x32_bf16 v[152:155], v[92:95], v[48:51], v[152:155]\n"
    "ds_read_b128 v[84:87], v4 offset:7168\n"
    "s_waitcnt lgkmcnt(5)\n"
    "v_mfma_f32_16x16x32_bf16 v[156:159], v[96:99], v[48:51], v[156:159]\n"
    "ds_read_b128 v[88:91], v4 offset:8192\n"
    "s_add_u32 s71, s90, 15\n"
    "s_and_b32 s71, s71, 15\n"
    "s_lshl_b32 s71, s71, 12\n"
    "s_add_u32 s71, s71, s66\n"
    "buffer_load_dwordx4 a[240:243], v0, s[40:43], s71 offen offset:0\n"
    "s_waitcnt lgkmcnt(5)\n"
    "v_mfma_f32_16x16x32_bf16 v[132:135], v[100:103], v[52:55], v[132:135]\n"
    "ds_read_b128 v[92:95], v4 offset:9216\n"
    "s_waitcnt lgkmcnt(5)\n"
    "v_mfma_f32_16x16x32_bf16 v[136:139], v[72:75], v[52:55], v[136:139]\n"
    "ds_read_b128 v[96:99], v4 offset:10240\n"
    "s_waitcnt lgkmcnt(5)\n"
    "v_mfma_f32_16x16x32_bf16 v[140:143], v[76:79], v[52:55], v[140:143]\n"
    "ds_read_b128 v[100:103], v4 offset:11264\n"
    "buffer_load_dwordx4 a[244:247], v0, s[40:43], s71 offen offset:1024\n"
    "s_waitcnt lgkmcnt(5)\n"
    "v_mfma_f32_16x16x32_bf16 v[144:147], v[80:83], v[52:55], v[144:147]\n"
    "ds_read_b128 v[72:75], v4 offset:12288\n"
    "s_waitcnt lgkmcnt(5)\n"
    "v_mfma_f32_16x16x32_bf16 v[148:151], v[84:87], v[52:55], v[148:151]\n"
    "ds_read_b128 v[76:79], v4 offset:13312\n"
    "s_waitcnt lgkmcnt(5)\n"
    "v_mfma_f32_16x16x32_bf16 v[152:155], v[88:91], v[52:55], v[152:155]\n"
    "ds_read_b128 v[80:83], v4 offset:14336\n"
    "buffer_load_dwordx4 a[248:251], v0, s[40:43], s71 offen offset:2048\n"
    "s_waitcnt lgkmcnt(5)\n"
    "v_mfma_f32_16x16x32_bf16 v[156:159], v[92:95], v[52:55], v[156:159]\n"
    "ds_read_b128 v[84:87], v4 offset:15360\n"
    "s_waitcnt lgkmcnt(5)\n"
    "v_mfma_f32_16x16x32_bf16 v[132:135], v[96:99], v[56:59], v[132:135]\n"
    "ds_read_b128 v[88:91], v4 offset:16384\n"
    "s_waitcnt lgkmcnt(5)\n"
    "v_mfma_f32_16x16x32_bf16 v[136:139], v[100:103], v[56:59], v[136:139]\n"
    "ds_read_b128 v[92:95], v4 offset:17408\n"
    "buffer_load_dwordx4 a[252:255], v0, s[40:43], s71 offen offset:3072\n"
    "s_waitcnt lgkmcnt(5)\n"
    "v_mfma_f32_16x16x32_bf16 v[140:143], v[72:75], v[56:59], v[140:143]\n"
    "ds_read_b128 v[96:99], v4 offset:18432\n"
    "s_waitcnt lgkmcnt(5)\n"
    "v_mfma_f32_16x16x32_bf16 v[144:147], v[76:79], v[56:59], v[144:147]\n"
    "ds_read_b128 v[100:103], v4 offset:19456\n"
    "s_waitcnt lgkmcnt(5)\n"
    "v_mfma_f32_16x16x32_bf16 v[148:151], v[80:83], v[56:59], v[148:151]\n"
    "ds_read_b128 v[72:75], v4 offset:20480\n"
    "s_waitcnt lgkmcnt(5)\n"
    "v_mfma_f32_16x16x32_bf16 v[152:155], v[84:87], v[56:59], v[152:155]\n"
    "ds_read_b128 v[76:79], v4 offset:21504\n"
    "s_waitcnt lgkmcnt(5)\n"
    "v_mfma_f32_16x16x32_bf16 v[156:159], v[88:91], v[56:59], v[156:159]\n"
    "ds_read_b128 v[80:83], v4 offset:22528\n"
    "s_waitcnt lgkmcnt(5)\n"
    "v_mfma_f32_16x16x32_bf16 v[132:135], v[92:95], v[60:63], v[132:135]\n"
    "ds_read_b128 v[84:87], v4 offset:23552\n"
    "s_waitcnt lgkmcnt(5)\n"
    "v_mfma_f32_16x16x32_bf16 v[136:139], v[96:99], v[60:63], v[136:139]\n"
    "ds_read_b128 v[88:91], v4 offset:24576\n"
    "s_waitcnt lgkmcnt(5)\n"
    "v_mfma_f32_16x16x32_bf16 v[140:143], v[100:103], v[60:63], v[140:143]\n"
    "ds_read_b128 v[92:95], v4 offset:25600\n"
    "s_waitcnt lgkmcnt(5)\n"
    "v_mfma_f32_16x16x32_bf16 v[144:147], v[72:75], v[60:63], v[144:147]\n"
    "ds_read_b128 v[96:99], v4 offset:26624\n"
    "s_waitcnt lgkmcnt(5)\n"
    "v_mfma_f32_16x16x32_bf16 v[148:151], v[76:79], v[60:63], v[148:151]\n"
    "ds_read_b128 v[100:103], v4 offset:27648\n"
    "s_waitcnt lgkmcnt(5)\n"
    "v_mfma_f32_16x16x32_bf16 v[152:155], v[80:83], v[60:63], v[152:155]\n"
    "ds_read_b128 v[72:75], v4 offset:28672\n"
    "s_waitcnt lgkmcnt(5)\n"
    "v_mfma_f32_16x16x32_bf16 v[156:159], v[84:87], v[60:63], v[156:159]\n"
    "ds_read_b128 v[76:79], v4 offset:29696\n"
    "s_waitcnt lgkmcnt(5)\n"
    "v_mfma_f32_16x16x32_bf16 v[132:135], v[88:91], v[64:67], v[132:135]\n"
    "ds_read_b128 v[80:83], v4 offset:30720\n"
    "s_waitcnt lgkmcnt(5)\n"
    "v_mfma_f32_16x16x32_bf16 v[136:139], v[92:95], v[64:67], v[136:139]\n"
    "ds_read_b128 v[84:87], v4 offset:31744\n"
    "s_waitcnt lgkmcnt(5)\n"
    "v_mfma_f32_16x16x32_bf16 v[140:143], v[96:99], v[64:67], v[140:143]\n"
    "ds_read_b128 v[88:91], v4 offset:32768\n"
    "s_waitcnt lgkmcnt(5)\n"
    "v_mfma_f32_16x16x32_bf16 v[144:147], v[100:103], v[64:67], v[144:147]\n"
    "ds_read_b128 v[92:95], v4 offset:33792\n"
    "s_waitcnt lgkmcnt(5)\n"
    "v_mfma_f32_16x16x32_bf16 v[148:151], v[72:75], v[64:67], v[148:151]\n"
    "ds_read_b128 v[96:99], v4 offset:34816\n"
    "s_waitcnt lgkmcnt(5)\n"
    "v_mfma_f32_16x16x32_bf16 v[152:155], v[76:79], v[64:67], v[152:155]\n"
    "ds_read_b128 v[100:103], v4 offset:35840\n"
    "s_waitcnt lgkmcnt(5)\n"
    "v_mfma_f32_16x16x32_bf16 v[156:159], v[80:83], v[64:67], v[156:159]\n"
    "ds_read_b128 v[72:75], v4 offset:36864\n"
    "s_waitcnt lgkmcnt(5)\n"
    "v_mfma_f32_16x16x32_bf16 v[132:135], v[84:87], v[68:71], v[132:135]\n"
    "ds_read_b128 v[76:79], v4 offset:37888\n"
    "s_waitcnt lgkmcnt(5)\n"
    "v_mfma_f32_16x16x32_bf16 v[136:139], v[88:91], v[68:71], v[136:139]\n"
    "s_waitcnt lgkmcnt(4)\n"
    "v_mfma_f32_16x16x32_bf16 v[140:143], v[92:95], v[68:71], v[140:143]\n"
    "s_waitcnt lgkmcnt(3)\n"
    "v_mfma_f32_16x16x32_bf16 v[144:147], v[96:99], v[68:71], v[144:147]\n"
    "s_waitcnt lgkmcnt(2)\n"
    "v_mfma_f32_16x16x32_bf16 v[148:151], v[100:103], v[68:71], v[148:151]\n"
    "s_waitcnt lgkmcnt(1)\n"
    "v_mfma_f32_16x16x32_bf16 v[152:155], v[72:75], v[68:71], v[152:155]\n"
    "s_waitcnt lgkmcnt(0)\n"
    "v_mfma_f32_16x16x32_bf16 v[156:159], v[76:79], v[68:71], v[156:159]\n"
    "s_nop 7\n"
    "v_mul_f32 v168, v132, v132\n"
    "v_mul_f32 v169, v133, v133\n"
    "v_mul_f32 v170, v134, v134\n"
    "v_mul_f32 v171, v135, v135\n"
    "v_fmamk_f32 v168, v168, 0xbdd2d3e8, v6\n"
    "v_fmamk_f32 v169, v169, 0xbdd2d3e8, v6\n"
    "v_fmamk_f32 v170, v170, 0xbdd2d3e8, v6\n"
    "v_fmamk_f32 v171, v171, 0xbdd2d3e8, v6\n"
    "v_mul_f32 v168, v132, v168\n"
    "v_mul_f32 v169, v133, v169\n"
    "v_mul_f32 v170, v134, v170\n"
    "v_mul_f32 v171, v135, v171\n"
    "v_exp_f32 v168, v168\n"
    "v_exp_f32 v169, v169\n"
    "v_exp_f32 v170, v170\n"
    "v_exp_f32 v171, v171\n"
    "v_add_f32 v168, 1.0, v168\n"
    "v_add_f32 v169, 1.0, v169\n"
    "v_add_f32 v170, 1.0, v170\n"
    "v_add_f32 v171, 1.0, v171\n"
    "v_rcp_f32 v168, v168\n"
    "v_rcp_f32 v169, v169\n"
    "v_rcp_f32 v170, v170\n"
    "v_rcp_f32 v171, v171\n"
    "v_mul_f32 v132, v132, v168\n"
    "v_mul_f32 v133, v133, v169\n"
    "v_mul_f32 v134, v134, v170\n"
    "v_mul_f32 v135, v135, v171\n"
    "v_mul_f32 v168, v136, v136\n"
    "v_mul_f32 v169, v137, v137\n"
    "v_mul_f32 v170, v138, v138\n"
    "v_mul_f32 v171, v139, v139\n"
    "v_fmamk_f32 v168, v168, 0xbdd2d3e8, v6\n"
    "v_fmamk_f32 v169, v169, 0xbdd2d3e8, v6\n"
    "v_fmamk_f32 v170, v170, 0xbdd2d3e8, v6\n"
    "v_fmamk_f32 v171, v171, 0xbdd2d3e8, v6\n"
    "v_mul_f32 v168, v136, v168\n"
    "v_mul_f32 v169, v137, v169\n"
    "v_mul_f32 v170, v138, v170\n"
    "v_mul_f32 v171, v139, v171\n"
    "v_exp_f32 v168, v168\n"
    "v_exp_f32 v169, v169\n"
    "v_exp_f32 v170, v170\n"
    "v_exp_f32 v171, v171\n"
    "v_add_f32 v168, 1.0, v168\n"
    "v_add_f32 v169, 1.0, v169\n"
    "v_add_f32 v170, 1.0, v170\n"
    "v_add_f32 v171, 1.0, v171\n"
    "v_rcp_f32 v168, v168\n"
    "v_rcp_f32 v169, v169\n"
    "v_rcp_f32 v170, v170\n"
    "v_rcp_f32 v171, v171\n"
    "v_mul_f32 v136, v136, v168\n"
    "v_mul_f32 v137, v137, v169\n"
    "v_mul_f32 v138, v138, v170\n"
    "v_mul_f32 v139, v139, v171\n"
    "v_mul_f32 v168, v140, v140\n"
    "v_mul_f32 v169, v141, v141\n"
    "v_mul_f32 v170, v142, v142\n"
    "v_mul_f32 v171, v143, v143\n"
    "v_fmamk_f32 v168, v168, 0xbdd2d3e8, v6\n"
    "v_fmamk_f32 v169, v169, 0xbdd2d3e8, v6\n"
    "v_fmamk_f32 v170, v170, 0xbdd2d3e8, v6\n"
    "v_fmamk_f32 v171, v171, 0xbdd2d3e8, v6\n"
    "v_mul_f32 v168, v140, v168\n"
    "v_mul_f32 v169, v141, v169\n"
    "v_mul_f32 v170, v142, v170\n"
    "v_mul_f32 v171, v143, v171\n"
    "v_exp_f32 v168, v168\n"
    "v_exp_f32 v169, v169\n"
    "v_exp_f32 v170, v170\n"
    "v_exp_f32 v171, v171\n"
    "v_add_f32 v168, 1.0, v168\n"
    "v_add_f32 v169, 1.0, v169\n"
    "v_add_f32 v170, 1.0, v170\n"
    "v_add_f32 v171, 1.0, v171\n"
    "v_rcp_f32 v168, v168\n"
    "v_rcp_f32 v169, v169\n"
    "v_rcp_f32 v170, v170\n"
    "v_rcp_f32 v171, v171\n"
    "v_mul_f32 v140, v140, v168\n"
    "v_mul_f32 v141, v141, v169\n"
    "v_mul_f32 v142, v142, v170\n"
    "v_mul_f32 v143, v143, v171\n"
    "v_mul_f32 v168, v144, v144\n"
    "v_mul_f32 v169, v145, v145\n"
    "v_mul_f32 v170, v146, v146\n"
    "v_mul_f32 v171, v147, v147\n"
    "v_fmamk_f32 v168, v168, 0xbdd2d3e8, v6\n"
    "v_fmamk_f32 v169, v169, 0xbdd2d3e8, v6\n"
    "v_fmamk_f32 v170, v170, 0xbdd2d3e8, v6\n"
    "v_fmamk_f32 v171, v171, 0xbdd2d3e8, v6\n"
    "v_mul_f32 v168, v144, v168\n"
    "v_mul_f32 v169, v145, v169\n"
    "v_mul_f32 v170, v146, v170\n"
    "v_mul_f32 v171, v147, v171\n"
    "v_exp_f32 v168, v168\n"
    "v_exp_f32 v169, v169\n"
    "v_exp_f32 v170, v170\n"
    "v_exp_f32 v171, v171\n"
    "v_add_f32 v168, 1.0, v168\n"
    "v_add_f32 v169, 1.0, v169\n"
    "v_add_f32 v170, 1.0, v170\n"
    "v_add_f32 v171, 1.0, v171\n"
    "v_rcp_f32 v168, v168\n"
    "v_rcp_f32 v169, v169\n"
    "v_rcp_f32 v170, v170\n"
    "v_rcp_f32 v171, v171\n"
    "v_mul_f32 v144, v144, v168\n"
    "v_mul_f32 v145, v145, v169\n"
    "v_mul_f32 v146, v146, v170\n"
    "v_mul_f32 v147, v147, v171\n"
    "v_mul_f32 v168, v148, v148\n"
    "v_mul_f32 v169, v149, v149\n"
    "v_mul_f32 v170, v150, v150\n"
    "v_mul_f32 v171, v151, v151\n"
    "v_fmamk_f32 v168, v168, 0xbdd2d3e8, v6\n"
    "v_fmamk_f32 v169, v169, 0xbdd2d3e8, v6\n"
    "v_fmamk_f32 v170, v170, 0xbdd2d3e8, v6\n"
    "v_fmamk_f32 v171, v171, 0xbdd2d3e8, v6\n"
    "v_mul_f32 v168, v148, v168\n"
    "v_mul_f32 v169, v149, v169\n"
    "v_mul_f32 v170, v150, v170\n"
    "v_mul_f32 v171, v151, v171\n"
    "v_exp_f32 v168, v168\n"
    "v_exp_f32 v169, v169\n"
    "v_exp_f32 v170, v170\n"
    "v_exp_f32 v171, v171\n"
    "v_add_f32 v168, 1.0, v168\n"
    "v_add_f32 v169, 1.0, v169\n"
    "v_add_f32 v170, 1.0, v170\n"
    "v_add_f32 v171, 1.0, v171\n"
    "v_rcp_f32 v168, v168\n"
    "v_rcp_f32 v169, v169\n"
    "v_rcp_f32 v170, v170\n"
    "v_rcp_f32 v171, v171\n"
    "v_mul_f32 v148, v148, v168\n"
    "v_mul_f32 v149, v149, v169\n"
    "v_mul_f32 v150, v150, v170\n"
    "v_mul_f32 v151, v151, v171\n"
    "v_mul_f32 v168, v152, v152\n"
    "v_mul_f32 v169, v153, v153\n"
    "v_mul_f32 v170, v154, v154\n"
    "v_mul_f32 v171, v155, v155\n"
    "v_fmamk_f32 v168, v168, 0xbdd2d3e8, v6\n"
    "v_fmamk_f32 v169, v169, 0xbdd2d3e8, v6\n"
    "v_fmamk_f32 v170, v170, 0xbdd2d3e8, v6\n"
    "v_fmamk_f32 v171, v171, 0xbdd2d3e8, v6\n"
    "v_mul_f32 v168, v152, v168\n"
    "v_mul_f32 v169, v153, v169\n"
    "v_mul_f32 v170, v154, v170\n"
    "v_mul_f32 v171, v155, v171\n"
    "v_exp_f32 v168, v168\n"
    "v_exp_f32 v169, v169\n"
    "v_exp_f32 v170, v170\n"
    "v_exp_f32 v171, v171\n"
    "v_add_f32 v168, 1.0, v168\n"
    "v_add_f32 v169, 1.0, v169\n"
    "v_add_f32 v170, 1.0, v170\n"
    "v_add_f32 v171, 1.0, v171\n"
    "v_rcp_f32 v168, v168\n"
    "v_rcp_f32 v169, v169\n"
    "v_rcp_f32 v170, v170\n"
    "v_rcp_f32 v171, v171\n"
    "v_mul_f32 v152, v152, v168\n"
    "v_mul_f32 v153, v153, v169\n"
    "v_mul_f32 v154, v154, v170\n"
    "v_mul_f32 v155, v155, v171\n"
    "v_mul_f32 v168, v156, v156\n"
    "v_mul_f32 v169, v157, v157\n"
    "v_mul_f32 v170, v158, v158\n"
    "v_mul_f32 v171, v159, v159\n"
    "v_fmamk_f32 v168, v168, 0xbdd2d3e8, v6\n"
    "v_fmamk_f32 v169, v169, 0xbdd2d3e8, v6\n"
    "v_fmamk_f32 v170, v170, 0xbdd2d3e8, v6\n"
    "v_fmamk_f32 v171, v171, 0xbdd2d3e8, v6\n"
    "v_mul_f32 v168, v156, v168\n"
    "v_mul_f32 v169, v157, v169\n"
    "v_mul_f32 v170, v158, v170\n"
    "v_mul_f32 v171, v159, v171\n"
    "v_exp_f32 v168, v168\n"
    "v_exp_f32 v169, v169\n"
    "v_exp_f32 v170, v170\n"
    "v_exp_f32 v171, v171\n"
    "v_add_f32 v168, 1.0, v168\n"
    "v_add_f32 v169, 1.0, v169\n"
    "v_add_f32 v170, 1.0, v170\n"
    "v_add_f32 v171, 1.0, v171\n"
    "v_rcp_f32 v168, v168\n"
    "v_rcp_f32 v169, v169\n"
    "v_rcp_f32 v170, v170\n"
    "v_rcp_f32 v171, v171\n"
    "v_mul_f32 v156, v156, v168\n"
    "v_mul_f32 v157, v157, v169\n"
    "v_mul_f32 v158, v158, v170\n"
    "v_mul_f32 v159, v159, v171\n"
    "s_waitcnt lgkmcnt(0)\n"
    "s_barrier\n"
    "v_and_b32 v8, 7, v2\n"
    "v_lshlrev_b32 v8, 9, v8\n"
    "v_lshrrev_b32 v9, 3, v2\n"
    "v_lshl_add_u32 v8, v9, 4, v8\n"
    "v_lshl_add_u32 v8, v3, 5, v8\n"
    "s_lshl_b32 s58, s53, 12\n"
    "s_add_u32 s58, s58, 0x2790000\n"
    "s_lshl_b32 s60, s52, 7\n"
    "s_add_u32 s59, s58, s60\n"
    "s_mov_b32 s62, 0\n"
    "L_st_%=:\n"
    "buffer_load_dwordx4 v[168:171], v8, s[40:43], s59 offen offset:0 sc1\n"
    "s_waitcnt vmcnt(0)\n"
    "v_max_u32 v7, v168, v169\n"
    "v_max3_u32 v7, v170, v171, v7\n"
    "v_cmp_eq_u32 vcc, -1, v7\n"
    "s_add_u32 s62, s62, 1\n"
    "s_cmp_gt_u32 s62, 0x40000\n"
    "s_cbranch_scc1 L_st_done_%=\n"
    "s_cbranch_vccz L_st_done_%=\n"
    "s_sleep 1\n"
    "s_branch L_st_%=\n"
    "L_st_done_%=:\n"
    "v_mov_b32 v172, v168\n"
    "v_mov_b32 v173, v170\n"
    "s_nop 0\n"
    "v_add_f32_dpp v172, v172, v172 quad_perm:[1,0,3,2] row_mask:0xf bank_mask:0xf\n"
    "v_add_f32_dpp v173, v173, v173 quad_perm:[1,0,3,2] row_mask:0xf bank_mask:0xf\n"
    "s_nop 0\n"
    "v_add_f32_dpp v172, v172, v172 quad_perm:[2,3,0,1] row_mask:0xf bank_mask:0xf\n"
    "v_add_f32_dpp v173, v173, v173 quad_perm:[2,3,0,1] row_mask:0xf bank_mask:0xf\n"
    "s_nop 0\n"
    "v_add_f32_dpp v172, v172, v172 row_half_mirror row_mask:0xf bank_mask:0xf\n"
    "v_add_f32_dpp v173, v173, v173 row_half_mirror row_mask:0xf bank_mask:0xf\n"
    "s_nop 0\n"
    "v_mul_f32 v172, 0x3e000000, v172\n"
    "v_mul_f32 v173, 0x3e000000, v173\n"
    "v_sub_f32 v174, v168, v172\n"
    "v_sub_f32 v175, v170, v173\n"
    "v_mul_f32 v174, v174, v174\n"
    "v_mul_f32 v175, v175, v175\n"
    "v_fmamk_f32 v174, v174, 0x42e00000, v169\n"
    "v_fmamk_f32 v175, v175, 0x42e00000, v171\n"
    "s_nop 0\n"
    "v_add_f32_dpp v174, v174, v174 quad_perm:[1,0,3,2] row_mask:0xf bank_mask:0xf\n"
    "v_add_f32_dpp v175, v175, v175 quad_perm:[1,0,3,2] row_mask:0xf bank_mask:0xf\n"
    "s_nop 0\n"
    "v_add_f32_dpp v174, v174, v174 quad_perm:[2,3,0,1] row_mask:0xf bank_mask:0xf\n"
    "v_add_f32_dpp v175, v175, v175 quad_perm:[2,3,0,1] row_mask:0xf bank_mask:0xf\n"
    "s_nop 0\n"
    "v_add_f32_dpp v174, v174, v174 row_half_mirror row_mask:0xf bank_mask:0xf\n"
    "v_add_f32_dpp v175, v175, v175 row_half_mirror row_mask:0xf bank_mask:0xf\n"
    "s_nop 0\n"
    "v_mov_b32 v10, 0x3727c5ac\n"
    "v_fmamk_f32 v174, v174, 0x3a924925, v10\n"
    "v_fmamk_f32 v175, v175, 0x3a924925, v10\n"
    "v_rsq_f32 v174, v174\n"
    "v_rsq_f32 v175, v175\n"
    "s_nop 0\n"
    "v_mul_f32 v172, v172, v174\n"
    "v_mul_f32 v173, v173, v175\n"
    "v_sub_f32 v172, 0, v172\n"
    "v_sub_f32 v173, 0, v173\n"
    "s_nop 0\n"
    "v_mov_b32_dpp v176, v172 row_ror:8 row_mask:0xf bank_mask:0xf\n"
    "v_mov_b32_dpp v178, v174 row_ror:8 row_mask:0xf bank_mask:0xf\n"
    "v_mov_b32_dpp v177, v173 row_ror:8 row_mask:0xf bank_mask:0xf\n"
    "v_mov_b32_dpp v179, v175 row_ror:8 row_mask:0xf bank_mask:0xf\n"
    "v_cndmask_b32 v180, v172, v176, s[72:73]\n"
    "v_cndmask_b32 v182, v176, v172, s[72:73]\n"
    "v_cndmask_b32 v184, v174, v178, s[72:73]\n"
    "v_cndmask_b32 v186, v178, v174, s[72:73]\n"
    "v_cndmask_b32 v181, v173, v177, s[72:73]\n"
    "v_cndmask_b32 v183, v177, v173, s[72:73]\n"
    "v_cndmask_b32 v185, v175, v179, s[72:73]\n"
    "v_cndmask_b32 v187, v179, v175, s[72:73]\n"
    "s_lshl_b32 s58, s53, 1\n"
    "s_lshr_b32 s59, s52, 1\n"
    "s_add_u32 s58, s58, s59\n"
    "s_mul_i32 s58, s58, 7168\n"
    "s_add_u32 s58, s58, s64\n"
    "s_and_b32 s59, s52, 1\n"
    "s_lshl_b32 s59, s59, 9\n"
    "s_add_u32 s58, s58, s59\n"
    "v_lshrrev_b32 v8, 1, v3\n"
    "v_lshlrev_b32 v8, 8, v8\n"
    "v_lshl_add_u32 v8, v2, 4, v8\n"
    "v_and_b32 v9, 1, v3\n"
    "v_lshl_add_u32 v8, v9, 3, v8\n"
    "v_fma_f32 v104, v104, v184, v180\n"
    "v_fma_f32 v105, v105, v185, v181\n"
    "v_fma_f32 v106, v106, v186, v182\n"
    "v_fma_f32 v107, v107, v187, v183\n"
    "v_cvt_pk_bf16_f32 v104, v104, v105\n"
    "v_cvt_pk_bf16_f32 v105, v106, v107\n"
    "buffer_store_dwordx2 v[104:105], v8, s[40:43], s58 offen offset:0 sc1\n"
    "v_fma_f32 v108, v108, v184, v180\n"
    "v_fma_f32 v109, v109, v185, v181\n"
    "v_fma_f32 v110, v110, v186, v182\n"
    "v_fma_f32 v111, v111, v187, v183\n"
    "v_cvt_pk_bf16_f32 v108, v108, v109\n"
    "v_cvt_pk_bf16_f32 v109, v110, v111\n"
    "buffer_store_dwordx2 v[108:109], v8, s[40:43], s58 offen offset:1024 sc1\n"
    "v_fma_f32 v112, v112, v184, v180\n"
    "v_fma_f32 v113, v113, v185, v181\n"
    "v_fma_f32 v114, v114, v186, v182\n"
    "v_fma_f32 v115, v115, v187, v183\n"
    "v_cvt_pk_bf16_f32 v112, v112, v113\n"
    "v_cvt_pk_bf16_f32 v113, v114, v115\n"
    "buffer_store_dwordx2 v[112:113], v8, s[40:43], s58 offen offset:2048 sc1\n"
    "v_fma_f32 v116, v116, v184, v180\n"
    "v_fma_f32 v117, v117, v185, v181\n"
    "v_fma_f32 v118, v118, v186, v182\n"
    "v_fma_f32 v119, v119, v187, v183\n"
    "v_cvt_pk_bf16_f32 v116, v116, v117\n"
    "v_cvt_pk_bf16_f32 v117, v118, v119\n"
    "buffer_store_dwordx2 v[116:117], v8, s[40:43], s58 offen offset:3072 sc1\n"
    "v_fma_f32 v120, v120, v184, v180\n"
    "v_fma_f32 v121, v121, v185, v181\n"
    "v_fma_f32 v122, v122, v186, v182\n"
    "v_fma_f32 v123, v123, v187, v183\n"
    "v_cvt_pk_bf16_f32 v120, v120, v121\n"
    "v_cvt_pk_bf16_f32 v121, v122, v123\n"
    "s_add_u32 s70, s58, 0x1000\n"
    "buffer_store_dwordx2 v[120:121], v8, s[40:43], s70 offen offset:0 sc1\n"
    "v_fma_f32 v124, v124, v184, v180\n"
    "v_fma_f32 v125, v125, v185, v181\n"
    "v_fma_f32 v126, v126, v186, v182\n"
    "v_fma_f32 v127, v127, v187, v183\n"
    "v_cvt_pk_bf16_f32 v124, v124, v125\n"
    "v_cvt_pk_bf16_f32 v125, v126, v127\n"
    "buffer_store_dwordx2 v[124:125], v8, s[40:43], s70 offen offset:1024 sc1\n"
    "v_fma_f32 v128, v128, v184, v180\n"
    "v_fma_f32 v129, v129, v185, v181\n"
    "v_fma_f32 v130, v130, v186, v182\n"
    "v_fma_f32 v131, v131, v187, v183\n"
    "v_cvt_pk_bf16_f32 v128, v128, v129\n"
    "v_cvt_pk_bf16_f32 v129, v130, v131\n"
    "buffer_store_dwordx2 v[128:129], v8, s[40:43], s70 offen offset:2048 sc1\n"
    "s_waitcnt vmcnt(0)\n"
    "s_barrier\n"
    "s_cmp_lg_u32 s52, 0\n"
    "s_cbranch_scc1 L_noflag_%=\n"
    "s_lshl_b32 s58, s54, 5\n"
    "s_add_u32 s58, s58, s53\n"
    "s_lshl_b32 s58, s58, 7\n"
    "s_add_u32 s58, s58, 0x2eb0000\n"
    "v_mov_b32 v8, 0x7ff00000\n"
    "v_cmp_eq_u32 vcc, 0, v1\n"
    "v_mov_b32 v9, 0\n"
    "s_nop 0\n"
    "v_cndmask_b32 v8, v8, v9, vcc\n"
    "v_mov_b32 v9, 1\n"
    "buffer_store_dword v9, v8, s[40:43], s58 offen offset:0 sc1\n"
    "L_noflag_%=:\n"
    "s_cmp_eq_u32 s52, 0\n"
    "s_cbranch_scc1 L_nowout_%=\n"
    "s_sub_u32 s69, s52, 1\n"
    "s_mul_i32 s69, s69, 36864\n"
    "s_add_u32 s56, s69, 0x0\n"
    "s_add_u32 m0, s55, s56\n"
    "s_add_u32 m0, m0, 0xc000\n"
    "s_add_u32 s57, s68, s56\n"
    "buffer_load_dwordx4 v0, s[40:43], s57 offen offset:0 lds\n"
    "buffer_load_dwordx4 v0, s[40:43], s57 offen offset:1024 lds\n"
    "buffer_load_dwordx4 v0, s[40:43], s57 offen offset:2048 lds\n"
    "buffer_load_dwordx4 v0, s[40:43], s57 offen offset:3072 lds\n"
    "s_add_u32 s56, s69, 0x1000\n"
    "s_add_u32 m0, s55, s56\n"
    "s_add_u32 m0, m0, 0xc000\n"
    "s_add_u32 s57, s68, s56\n"
    "buffer_load_dwordx4 v0, s[40:43], s57 offen offset:0 lds\n"
    "buffer_load_dwordx4 v0, s[40:43], s57 offen offset:1024 lds\n"
    "buffer_load_dwordx4 v0, s[40:43], s57 offen offset:2048 lds\n"
    "buffer_load_dwordx4 v0, s[40:43], s57 offen offset:3072 lds\n"
    "s_add_u32 s56, s69, 0x2000\n"
    "s_add_u32 m0, s55, s56\n"
    "s_add_u32 m0, m0, 0xc000\n"
    "s_add_u32 s57, s68, s56\n"
    "buffer_load_dwordx4 v0, s[40:43], s57 offen offset:0 lds\n"
    "buffer_load_dwordx4 v0, s[40:43], s57 offen offset:1024 lds\n"
    "buffer_load_dwordx4 v0, s[40:43], s57 offen offset:2048 lds\n"
    "buffer_load_dwordx4 v0, s[40:43], s57 offen offset:3072 lds\n"
    "s_add_u32 s56, s69, 0x3000\n"
    "s_add_u32 m0, s55, s56\n"
    "s_add_u32 m0, m0, 0xc000\n"
    "s_add_u32 s57, s68, s56\n"
    "buffer_load_dwordx4 v0, s[40:43], s57 offen offset:0 lds\n"
    "buffer_load_dwordx4 v0, s[40:43], s57 offen offset:1024 lds\n"
    "buffer_load_dwordx4 v0, s[40:43], s57 offen offset:2048 lds\n"
    "buffer_load_dwordx4 v0, s[40:43], s57 offen offset:3072 lds\n"
    "s_add_u32 s56, s69, 0x4000\n"
    "s_add_u32 m0, s55, s56\n"
    "s_add_u32 m0, m0, 0xc000\n"
    "s_add_u32 s57, s68, s56\n"
    "buffer_load_dwordx4 v0, s[40:43], s57 offen offset:0 lds\n"
    "buffer_load_dwordx4 v0, s[40:43], s57 offen offset:1024 lds\n"
    "buffer_load_dwordx4 v0, s[40:43], s57 offen offset:2048 lds\n"
    "buffer_load_dwordx4 v0, s[40:43], s57 offen offset:3072 lds\n"
    "s_add_u32 s56, s69, 0x5000\n"
    "s_add_u32 m0, s55, s56\n"
    "s_add_u32 m0, m0, 0xc000\n"
    "s_add_u32 s57, s68, s56\n"
    "buffer_load_dwordx4 v0, s[40:43], s57 offen offset:0 lds\n"
    "buffer_load_dwordx4 v0, s[40:43], s57 offen offset:1024 lds\n"
    "buffer_load_dwordx4 v0, s[40:43], s57 offen offset:2048 lds\n"
    "buffer_load_dwordx4 v0, s[40:43], s57 offen offset:3072 lds\n"
    "s_add_u32 s56, s69, 0x6000\n"
    "s_add_u32 m0, s55, s56\n"
    "s_add_u32 m0, m0, 0xc000\n"
    "s_add_u32 s57, s68, s56\n"
    "buffer_load_dwordx4 v0, s[40:43], s57 offen offset:0 lds\n"
    "buffer_load_dwordx4 v0, s[40:43], s57 offen offset:1024 lds\n"
    "buffer_load_dwordx4 v0, s[40:43], s57 offen offset:2048 lds\n"
    "buffer_load_dwordx4 v0, s[40:43], s57 offen offset:3072 lds\n"
    "s_add_u32 s56, s69, 0x7000\n"
    "s_add_u32 m0, s55, s56\n"
    "s_add_u32 m0, m0, 0xc000\n"
    "s_add_u32 s57, s68, s56\n"
    "buffer_load_dwordx4 v0, s[40:43], s57 offen offset:0 lds\n"
    "buffer_load_dwordx4 v0, s[40:43], s57 offen offset:1024 lds\n"
    "buffer_load_dwordx4 v0, s[40:43], s57 offen offset:2048 lds\n"
    "buffer_load_dwordx4 v0, s[40:43], s57 offen offset:3072 lds\n"
    "s_add_u32 s56, s69, 0x8000\n"
    "s_add_u32 m0, s55, s56\n"
    "s_add_u32 m0, m0, 0xc000\n"
    "s_add_u32 s57, s68, s56\n"
    "buffer_load_dwordx4 v0, s[40:43], s57 offen offset:0 lds\n"
    "buffer_load_dwordx4 v0, s[40:43], s57 offen offset:1024 lds\n"
    "buffer_load_dwordx4 v0, s[40:43], s57 offen offset:2048 lds\n"
    "buffer_load_dwordx4 v0, s[40:43], s57 offen offset:3072 lds\n"
    "s_add_u32 s56, s69, 0x9000\n"
    "s_add_u32 m0, s55, s56\n"
    "s_add_u32 m0, m0, 0xc000\n"
    "s_add_u32 s57, s68, s56\n"
    "buffer_load_dwordx4 v0, s[40:43], s57 offen offset:0 lds\n"
    "buffer_load_dwordx4 v0, s[40:43], s57 offen offset:1024 lds\n"
    "buffer_load_dwordx4 v0, s[40:43], s57 offen offset:2048 lds\n"
    "buffer_load_dwordx4 v0, s[40:43], s57 offen offset:3072 lds\n"
    "L_nowout_%=:\n"
    "s_lshl_b32 s58, s53, 6\n"
    "s_lshl_b32 s59, s52, 4\n"
    "s_add_u32 s58, s58, s59\n"
    "v_add_u32 v253, s58, v2\n"
    "v_mul_u32_u24 v252, 0x380, v253\n"
    "v_lshl_add_u32 v252, v3, 3, v252\n"
    "s_min_u32 s58, s54, 6\n"
    "s_lshl_b32 s78, s58, 7\n"
    "s_add_u32 s59, s78, 0x25d0000\n"
    "buffer_load_dwordx2 v[244:245], v252, s[40:43], s59 offen offset:0 sc1\n"
    "buffer_load_dwordx2 v[246:247], v252, s[40:43], s59 offen offset:32 sc1\n"
    "buffer_load_dwordx2 v[248:249], v252, s[40:43], s59 offen offset:64 sc1\n"
    "buffer_load_dwordx2 v[250:251], v252, s[40:43], s59 offen offset:96 sc1\n"
    "v_lshlrev_b32 v8, 1, v253\n"
    "buffer_load_ushort v254, v8, s[48:51], 0 offen\n"
    "s_cmp_lg_u32 s52, 0\n"
    "s_cbranch_scc1 L_fl_done_%=\n"
    "s_lshl_b32 s58, s54, 12\n"
    "s_add_u32 s58, s58, 0x2eb0000\n"
    "v_and_b32 v8, 31, v1\n"
    "v_lshlrev_b32 v8, 7, v8\n"
    "s_mov_b32 s62, 0\n"
    "L_fl_%=:\n"
    "buffer_load_dword v9, v8, s[40:43], s58 offen offset:0 sc1\n"
    "s_waitcnt vmcnt(0)\n"
    "s_waitcnt vmcnt(0)\n"
    "v_cmp_ne_u32 vcc, 1, v9\n"
    "s_add_u32 s62, s62, 1\n"
    "s_cmp_gt_u32 s62, 0x40000\n"
    "s_cbranch_scc1 L_fl_ok_%=\n"
    "s_cbranch_vccz L_fl_ok_%=\n"
    "s_sleep 1\n"
    "s_branch L_fl_%=\n"
    "L_fl_ok_%=:\n"
    "L_fl_done_%=:\n"
    "s_waitcnt lgkmcnt(0)\n"
    "s_barrier\n"
    "s_mul_i32 s58, s52, 114688\n"
    "s_add_u32 s58, s58, s64\n"
    "s_add_u32 s93, s90, 0\n"
    "s_and_b32 s93, s93, 15\n"
    "s_mul_i32 s93, s93, 7168\n"
    "s_add_u32 s93, s93, s58\n"
    "s_add_u32 s94, s93, 4096\n"
    "buffer_load_dwordx4 v[160:163], v0, s[40:43], s93 offen offset:0 sc1\n"
    "buffer_load_dwordx4 v[164:167], v0, s[40:43], s93 offen offset:1024 sc1\n"
    "buffer_load_dwordx4 v[168:171], v0, s[40:43], s93 offen offset:2048 sc1\n"
    "buffer_load_dwordx4 v[172:175], v0, s[40:43], s93 offen offset:3072 sc1\n"
    "buffer_load_dwordx4 v[176:179], v0, s[40:43], s94 offen offset:0 sc1\n"
    "buffer_load_dwordx4 v[180:183], v0, s[40:43], s94 offen offset:1024 sc1\n"
    "buffer_load_dwordx4 v[184:187], v0, s[40:43], s94 offen offset:2048 sc1\n"
    "s_add_u32 s93, s90, 1\n"
    "s_and_b32 s93, s93, 15\n"
    "s_mul_i32 s93, s93, 7168\n"
    "s_add_u32 s93, s93, s58\n"
    "s_add_u32 s94, s93, 4096\n"
    "buffer_load_dwordx4 v[188:191], v0, s[40:43], s93 offen offset:0 sc1\n"
    "buffer_load_dwordx4 v[192:195], v0, s[40:43], s93 offen offset:1024 sc1\n"
    "buffer_load_dwordx4 v[196:199], v0, s[40:43], s93 offen offset:2048 sc1\n"
    "buffer_load_dwordx4 v[200:203], v0, s[40:43], s93 offen offset:3072 sc1\n"
    "buffer_load_dwordx4 v[204:207], v0, s[40:43], s94 offen offset:0 sc1\n"
    "buffer_load_dwordx4 v[208:211], v0, s[40:43], s94 offen offset:1024 sc1\n"
    "buffer_load_dwordx4 v[212:215], v0, s[40:43], s94 offen offset:2048 sc1\n"
    "s_add_u32 s93, s90, 2\n"
    "s_and_b32 s93, s93, 15\n"
    "s_mul_i32 s93, s93, 7168\n"
    "s_add_u32 s93, s93, s58\n"
    "s_add_u32 s94, s93, 4096\n"
    "buffer_load_dwordx4 v[216:219], v0, s[40:43], s93 offen offset:0 sc1\n"
    "s_waitcnt vmcnt(14)\n"
    "v_mfma_f32_16x16x32_bf16 v[16:19], v[160:163], a[0:3], 0\n"
    "v_mfma_f32_16x16x32_bf16 v[20:23], v[160:163], a[4:7], 0\n"
    "v_mfma_f32_16x16x32_bf16 v[24:27], v[160:163], a[8:11], 0\n"
    "v_mfma_f32_16x16x32_bf16 v[28:31], v[160:163], a[12:15], 0\n"
    "buffer_load_dwordx4 v[220:223], v0, s[40:43], s93 offen offset:1024 sc1\n"
    "s_waitcnt vmcnt(14)\n"
    "v_mfma_f32_16x16x32_bf16 v[32:35], v[164:167], a[0:3], 0\n"
    "v_mfma_f32_16x16x32_bf16 v[36:39], v[164:167], a[4:7], 0\n"
    "v_mfma_f32_16x16x32_bf16 v[40:43], v[164:167], a[8:11], 0\n"
    "v_mfma_f32_16x16x32_bf16 v[44:47], v[164:167], a[12:15], 0\n"
    "buffer_load_dwordx4 v[224:227], v0, s[40:43], s93 offen offset:2048 sc1\n"
    "s_waitcnt vmcnt(14)\n"
    "v_mfma_f32_16x16x32_bf16 v[48:51], v[168:171], a[0:3], 0\n"
    "v_mfma_f32_16x16x32_bf16 v[52:55], v[168:171], a[4:7], 0\n"
    "v_mfma_f32_16x16x32_bf16 v[56:59], v[168:171], a[8:11], 0\n"
    "v_mfma_f32_16x16x32_bf16 v[60:63], v[168:171], a[12:15], 0\n"
    "buffer_load_dwordx4 v[228:231], v0, s[40:43], s93 offen offset:3072 sc1\n"
    "s_waitcnt vmcnt(14)\n"
    "v_mfma_f32_16x16x32_bf16 v[64:67], v[172:175], a[0:3], 0\n"
    "v_mfma_f32_16x16x32_bf16 v[68:71], v[172:175], a[4:7], 0\n"
    "v_mfma_f32_16x16x32_bf16 v[72:75], v[172:175], a[8:11], 0\n"
    "v_mfma_f32_16x16x32_bf16 v[76:79], v[172:175], a[12:15], 0\n"
    "buffer_load_dwordx4 v[232:235], v0, s[40:43], s94 offen offset:0 sc1\n"
    "s_waitcnt vmcnt(14)\n"
    "v_mfma_f32_16x16x32_bf16 v[80:83], v[176:179], a[0:3], 0\n"
    "v_mfma_f32_16x16x32_bf16 v[84:87], v[176:179], a[4:7], 0\n"
    "v_mfma_f32_16x16x32_bf16 v[88:91], v[176:179], a[8:11], 0\n"
    "v_mfma_f32_16x16x32_bf16 v[92:95], v[176:179], a[12:15], 0\n"
    "buffer_load_dwordx4 v[236:239], v0, s[40:43], s94 offen offset:1024 sc1\n"
    "s_waitcnt vmcnt(14)\n"
    "v_mfma_f32_16x16x32_bf16 v[96:99], v[180:183], a[0:3], 0\n"
    "v_mfma_f32_16x16x32_bf16 v[100:103], v[180:183], a[4:7], 0\n"
    "v_mfma_f32_16x16x32_bf16 v[104:107], v[180:183], a[8:11], 0\n"
    "v_mfma_f32_16x16x32_bf16 v[108:111], v[180:183], a[12:15], 0\n"
    "buffer_load_dwordx4 v[240:243], v0, s[40:43], s94 offen offset:2048 sc1\n"
    "s_waitcnt vmcnt(14)\n"
    "v_mfma_f32_16x16x32_bf16 v[112:115], v[184:187], a[0:3], 0\n"
    "v_mfma_f32_16x16x32_bf16 v[116:119], v[184:187], a[4:7], 0\n"
    "v_mfma_f32_16x16x32_bf16 v[120:123], v[184:187], a[8:11], 0\n"
    "v_mfma_f32_16x16x32_bf16 v[124:127], v[184:187], a[12:15], 0\n"
    "s_add_u32 s93, s90, 3\n"
    "s_and_b32 s93, s93, 15\n"
    "s_mul_i32 s93, s93, 7168\n"
    "s_add_u32 s93, s93, s58\n"
    "s_add_u32 s94, s93, 4096\n"
    "buffer_load_dwordx4 v[160:163], v0, s[40:43], s93 offen offset:0 sc1\n"
    "s_waitcnt vmcnt(14)\n"
    "v_mfma_f32_16x16x32_bf16 v[16:19], v[188:191], a[16:19], v[16:19]\n"
    "v_mfma_f32_16x16x32_bf16 v[20:23], v[188:191], a[20:23], v[20:23]\n"
    "v_mfma_f32_16x16x32_bf16 v[24:27], v[188:191], a[24:27], v[24:27]\n"
    "v_mfma_f32_16x16x32_bf16 v[28:31], v[188:191], a[28:31], v[28:31]\n"
    "buffer_load_dwordx4 v[164:167], v0, s[40:43], s93 offen offset:1024 sc1\n"
    "s_waitcnt vmcnt(14)\n"
    "v_mfma_f32_16x16x32_bf16 v[32:35], v[192:195], a[16:19], v[32:35]\n"
    "v_mfma_f32_16x16x32_bf16 v[36:39], v[192:195], a[20:23], v[36:39]\n"
    "v_mfma_f32_16x16x32_bf16 v[40:43], v[192:195], a[24:27], v[40:43]\n"
    "v_mfma_f32_16x16x32_bf16 v[44:47], v[192:195], a[28:31], v[44:47]\n"
    "buffer_load_dwordx4 v[168:171], v0, s[40:43], s93 offen offset:2048 sc1\n"
    "s_waitcnt vmcnt(14)\n"
    "v_mfma_f32_16x16x32_bf16 v[48:51], v[196:199], a[16:19], v[48:51]\n"
    "v_mfma_f32_16x16x32_bf16 v[52:55], v[196:199], a[20:23], v[52:55]\n"
    "v_mfma_f32_16x16x32_bf16 v[56:59], v[196:199], a[24:27], v[56:59]\n"
    "v_mfma_f32_16x16x32_bf16 v[60:63], v[196:199], a[28:31], v[60:63]\n"
    "buffer_load_dwordx4 v[172:175], v0, s[40:43], s93 offen offset:3072 sc1\n"
    "s_waitcnt vmcnt(14)\n"
    "v_mfma_f32_16x16x32_bf16 v[64:67], v[200:203], a[16:19], v[64:67]\n"
    "v_mfma_f32_16x16x32_bf16 v[68:71], v[200:203], a[20:23], v[68:71]\n"
    "v_mfma_f32_16x16x32_bf16 v[72:75], v[200:203], a[24:27], v[72:75]\n"
    "v_mfma_f32_16x16x32_bf16 v[76:79], v[200:203], a[28:31], v[76:79]\n"
    "buffer_load_dwordx4 v[176:179], v0, s[40:43], s94 offen offset:0 sc1\n"
    "s_waitcnt vmcnt(14)\n"
    "v_mfma_f32_16x16x32_bf16 v[80:83], v[204:207], a[16:19], v[80:83]\n"
    "v_mfma_f32_16x16x32_bf16 v[84:87], v[204:207], a[20:23], v[84:87]\n"
    "v_mfma_f32_16x16x32_bf16 v[88:91], v[204:207], a[24:27], v[88:91]\n"
    "v_mfma_f32_16x16x32_bf16 v[92:95], v[204:207], a[28:31], v[92:95]\n"
    "buffer_load_dwordx4 v[180:183], v0, s[40:43], s94 offen offset:1024 sc1\n"
    "s_waitcnt vmcnt(14)\n"
    "v_mfma_f32_16x16x32_bf16 v[96:99], v[208:211], a[16:19], v[96:99]\n"
    "v_mfma_f32_16x16x32_bf16 v[100:103], v[208:211], a[20:23], v[100:103]\n"
    "v_mfma_f32_16x16x32_bf16 v[104:107], v[208:211], a[24:27], v[104:107]\n"
    "v_mfma_f32_16x16x32_bf16 v[108:111], v[208:211], a[28:31], v[108:111]\n"
    "buffer_load_dwordx4 v[184:187], v0, s[40:43], s94 offen offset:2048 sc1\n"
    "s_waitcnt vmcnt(14)\n"
    "v_mfma_f32_16x16x32_bf16 v[112:115], v[212:215], a[16:19], v[112:115]\n"
    "v_mfma_f32_16x16x32_bf16 v[116:119], v[212:215], a[20:23], v[116:119]\n"
    "v_mfma_f32_16x16x32_bf16 v[120:123], v[212:215], a[24:27], v[120:123]\n"
    "v_mfma_f32_16x16x32_bf16 v[124:127], v[212:215], a[28:31], v[124:127]\n"
    "s_add_u32 s93, s90, 4\n"
    "s_and_b32 s93, s93, 15\n"
    "s_mul_i32 s93, s93, 7168\n"
    "s_add_u32 s93, s93, s58\n"
    "s_add_u32 s94, s93, 4096\n"
    "buffer_load_dwordx4 v[188:191], v0, s[40:43], s93 offen offset:0 sc1\n"
    "s_waitcnt vmcnt(14)\n"
    "v_mfma_f32_16x16x32_bf16 v[16:19], v[216:219], a[32:35], v[16:19]\n"
    "v_mfma_f32_16x16x32_bf16 v[20:23], v[216:219], a[36:39], v[20:23]\n"
    "v_mfma_f32_16x16x32_bf16 v[24:27], v[216:219], a[40:43], v[24:27]\n"
    "v_mfma_f32_16x16x32_bf16 v[28:31], v[216:219], a[44:47], v[28:31]\n"
    "buffer_load_dwordx4 v[192:195], v0, s[40:43], s93 offen offset:1024 sc1\n"
    "s_waitcnt vmcnt(14)\n"
    "v_mfma_f32_16x16x32_bf16 v[32:35], v[220:223], a[32:35], v[32:35]\n"
    "v_mfma_f32_16x16x32_bf16 v[36:39], v[220:223], a[36:39], v[36:39]\n"
    "v_mfma_f32_16x16x32_bf16 v[40:43], v[220:223], a[40:43], v[40:43]\n"
    "v_mfma_f32_16x16x32_bf16 v[44:47], v[220:223], a[44:47], v[44:47]\n"
    "buffer_load_dwordx4 v[196:199], v0, s[40:43], s93 offen offset:2048 sc1\n"
    "s_waitcnt vmcnt(14)\n"
    "v_mfma_f32_16x16x32_bf16 v[48:51], v[224:227], a[32:35], v[48:51]\n"
    "v_mfma_f32_16x16x32_bf16 v[52:55], v[224:227], a[36:39], v[52:55]\n"
    "v_mfma_f32_16x16x32_bf16 v[56:59], v[224:227], a[40:43], v[56:59]\n"
    "v_mfma_f32_16x16x32_bf16 v[60:63], v[224:227], a[44:47], v[60:63]\n"
    "buffer_load_dwordx4 v[200:203], v0, s[40:43], s93 offen offset:3072 sc1\n"
    "s_waitcnt vmcnt(14)\n"
    "v_mfma_f32_16x16x32_bf16 v[64:67], v[228:231], a[32:35], v[64:67]\n"
    "v_mfma_f32_16x16x32_bf16 v[68:71], v[228:231], a[36:39], v[68:71]\n"
    "v_mfma_f32_16x16x32_bf16 v[72:75], v[228:231], a[40:43], v[72:75]\n"
    "v_mfma_f32_16x16x32_bf16 v[76:79], v[228:231], a[44:47], v[76:79]\n"
    "buffer_load_dwordx4 v[204:207], v0, s[40:43], s94 offen offset:0 sc1\n"
    "s_waitcnt vmcnt(14)\n"
    "v_mfma_f32_16x16x32_bf16 v[80:83], v[232:235], a[32:35], v[80:83]\n"
    "v_mfma_f32_16x16x32_bf16 v[84:87], v[232:235], a[36:39], v[84:87]\n"
    "v_mfma_f32_16x16x32_bf16 v[88:91], v[232:235], a[40:43], v[88:91]\n"
    "v_mfma_f32_16x16x32_bf16 v[92:95], v[232:235], a[44:47], v[92:95]\n"
    "buffer_load_dwordx4 v[208:211], v0, s[40:43], s94 offen offset:1024 sc1\n"
    "s_waitcnt vmcnt(14)\n"
    "v_mfma_f32_16x16x32_bf16 v[96:99], v[236:239], a[32:35], v[96:99]\n"
    "v_mfma_f32_16x16x32_bf16 v[100:103], v[236:239], a[36:39], v[100:103]\n"
    "v_mfma_f32_16x16x32_bf16 v[104:107], v[236:239], a[40:43], v[104:107]\n"
    "v_mfma_f32_16x16x32_bf16 v[108:111], v[236:239], a[44:47], v[108:111]\n"
    "buffer_load_dwordx4 v[212:215], v0, s[40:43], s94 offen offset:2048 sc1\n"
    "s_waitcnt vmcnt(14)\n"
    "v_mfma_f32_16x16x32_bf16 v[112:115], v[240:243], a[32:35], v[112:115]\n"
    "v_mfma_f32_16x16x32_bf16 v[116:119], v[240:243], a[36:39], v[116:119]\n"
    "v_mfma_f32_16x16x32_bf16 v[120:123], v[240:243], a[40:43], v[120:123]\n"
    "v_mfma_f32_16x16x32_bf16 v[124:127], v[240:243], a[44:47], v[124:127]\n"
    "s_add_u32 s93, s90, 5\n"
    "s_and_b32 s93, s93, 15\n"
    "s_mul_i32 s93, s93, 7168\n"
    "s_add_u32 s93, s93, s58\n"
    "s_add_u32 s94, s93, 4096\n"
    "buffer_load_dwordx4 v[216:219], v0, s[40:43], s93 offen offset:0 sc1\n"
    "s_waitcnt vmcnt(14)\n"
    "v_mfma_f32_16x16x32_bf16 v[16:19], v[160:163], a[48:51], v[16:19]\n"
    "v_mfma_f32_16x16x32_bf16 v[20:23], v[160:163], a[52:55], v[20:23]\n"
    "v_mfma_f32_16x16x32_bf16 v[24:27], v[160:163], a[56:59], v[24:27]\n"
    "v_mfma_f32_16x16x32_bf16 v[28:31], v[160:163], a[60:63], v[28:31]\n"
    "buffer_load_dwordx4 v[220:223], v0, s[40:43], s93 offen offset:1024 sc1\n"
    "s_waitcnt vmcnt(14)\n"
    "v_mfma_f32_16x16x32_bf16 v[32:35], v[164:167], a[48:51], v[32:35]\n"
    "v_mfma_f32_16x16x32_bf16 v[36:39], v[164:167], a[52:55], v[36:39]\n"
    "v_mfma_f32_16x16x32_bf16 v[40:43], v[164:167], a[56:59], v[40:43]\n"
    "v_mfma_f32_16x16x32_bf16 v[44:47], v[164:167], a[60:63], v[44:47]\n"
    "buffer_load_dwordx4 v[224:227], v0, s[40:43], s93 offen offset:2048 sc1\n"
    "s_waitcnt vmcnt(14)\n"
    "v_mfma_f32_16x16x32_bf16 v[48:51], v[168:171], a[48:51], v[48:51]\n"
    "v_mfma_f32_16x16x32_bf16 v[52:55], v[168:171], a[52:55], v[52:55]\n"
    "v_mfma_f32_16x16x32_bf16 v[56:59], v[168:171], a[56:59], v[56:59]\n"
    "v_mfma_f32_16x16x32_bf16 v[60:63], v[168:171], a[60:63], v[60:63]\n"
    "buffer_load_dwordx4 v[228:231], v0, s[40:43], s93 offen offset:3072 sc1\n"
    "s_waitcnt vmcnt(14)\n"
    "v_mfma_f32_16x16x32_bf16 v[64:67], v[172:175], a[48:51], v[64:67]\n"
    "v_mfma_f32_16x16x32_bf16 v[68:71], v[172:175], a[52:55], v[68:71]\n"
    "v_mfma_f32_16x16x32_bf16 v[72:75], v[172:175], a[56:59], v[72:75]\n"
    "v_mfma_f32_16x16x32_bf16 v[76:79], v[172:175], a[60:63], v[76:79]\n"
    "buffer_load_dwordx4 v[232:235], v0, s[40:43], s94 offen offset:0 sc1\n"
    "s_waitcnt vmcnt(14)\n"
    "v_mfma_f32_16x16x32_bf16 v[80:83], v[176:179], a[48:51], v[80:83]\n"
    "v_mfma_f32_16x16x32_bf16 v[84:87], v[176:179], a[52:55], v[84:87]\n"
    "v_mfma_f32_16x16x32_bf16 v[88:91], v[176:179], a[56:59], v[88:91]\n"
    "v_mfma_f32_16x16x32_bf16 v[92:95], v[176:179], a[60:63], v[92:95]\n"
    "buffer_load_dwordx4 v[236:239], v0, s[40:43], s94 offen offset:1024 sc1\n"
    "s_waitcnt vmcnt(14)\n"
    "v_mfma_f32_16x16x32_bf16 v[96:99], v[180:183], a[48:51], v[96:99]\n"
    "v_mfma_f32_16x16x32_bf16 v[100:103], v[180:183], a[52:55], v[100:103]\n"
    "v_mfma_f32_16x16x32_bf16 v[104:107], v[180:183], a[56:59], v[104:107]\n"
    "v_mfma_f32_16x16x32_bf16 v[108:111], v[180:183], a[60:63], v[108:111]\n"
    "buffer_load_dwordx4 v[240:243], v0, s[40:43], s94 offen offset:2048 sc1\n"
    "s_waitcnt vmcnt(14)\n"
    "v_mfma_f32_16x16x32_bf16 v[112:115], v[184:187], a[48:51], v[112:115]\n"
    "v_mfma_f32_16x16x32_bf16 v[116:119], v[184:187], a[52:55], v[116:119]\n"
    "v_mfma_f32_16x16x32_bf16 v[120:123], v[184:187], a[56:59], v[120:123]\n"
    "v_mfma_f32_16x16x32_bf16 v[124:127], v[184:187], a[60:63], v[124:127]\n"
    "s_add_u32 s93, s90, 6\n"
    "s_and_b32 s93, s93, 15\n"
    "s_mul_i32 s93, s93, 7168\n"
    "s_add_u32 s93, s93, s58\n"
    "s_add_u32 s94, s93, 4096\n"
    "buffer_load_dwordx4 v[160:163], v0, s[40:43], s93 offen offset:0 sc1\n"
    "s_waitcnt vmcnt(14)\n"
    "v_mfma_f32_16x16x32_bf16 v[16:19], v[188:191], a[64:67], v[16:19]\n"
    "v_mfma_f32_16x16x32_bf16 v[20:23], v[188:191], a[68:71], v[20:23]\n"
    "v_mfma_f32_16x16x32_bf16 v[24:27], v[188:191], a[72:75], v[24:27]\n"
    "v_mfma_f32_16x16x32_bf16 v[28:31], v[188:191], a[76:79], v[28:31]\n"
    "buffer_load_dwordx4 v[164:167], v0, s[40:43], s93 offen offset:1024 sc1\n"
    "s_waitcnt vmcnt(14)\n"
    "v_mfma_f32_16x16x32_bf16 v[32:35], v[192:195], a[64:67], v[32:35]\n"
    "v_mfma_f32_16x16x32_bf16 v[36:39], v[192:195], a[68:71], v[36:39]\n"
    "v_mfma_f32_16x16x32_bf16 v[40:43], v[192:195], a[72:75], v[40:43]\n"
    "v_mfma_f32_16x16x32_bf16 v[44:47], v[192:195], a[76:79], v[44:47]\n"
    "buffer_load_dwordx4 v[168:171], v0, s[40:43], s93 offen offset:2048 sc1\n"
    "s_waitcnt vmcnt(14)\n"
    "v_mfma_f32_16x16x32_bf16 v[48:51], v[196:199], a[64:67], v[48:51]\n"
    "v_mfma_f32_16x16x32_bf16 v[52:55], v[196:199], a[68:71], v[52:55]\n"
    "v_mfma_f32_16x16x32_bf16 v[56:59], v[196:199], a[72:75], v[56:59]\n"
    "v_mfma_f32_16x16x32_bf16 v[60:63], v[196:199], a[76:79], v[60:63]\n"
    "buffer_load_dwordx4 v[172:175], v0, s[40:43], s93 offen offset:3072 sc1\n"
    "s_waitcnt vmcnt(14)\n"
    "v_mfma_f32_16x16x32_bf16 v[64:67], v[200:203], a[64:67], v[64:67]\n"
    "v_mfma_f32_16x16x32_bf16 v[68:71], v[200:203], a[68:71], v[68:71]\n"
    "v_mfma_f32_16x16x32_bf16 v[72:75], v[200:203], a[72:75], v[72:75]\n"
    "v_mfma_f32_16x16x32_bf16 v[76:79], v[200:203], a[76:79], v[76:79]\n"
    "buffer_load_dwordx4 v[176:179], v0, s[40:43], s94 offen offset:0 sc1\n"
    "s_waitcnt vmcnt(14)\n"
    "v_mfma_f32_16x16x32_bf16 v[80:83], v[204:207], a[64:67], v[80:83]\n"
    "v_mfma_f32_16x16x32_bf16 v[84:87], v[204:207], a[68:71], v[84:87]\n"
    "v_mfma_f32_16x16x32_bf16 v[88:91], v[204:207], a[72:75], v[88:91]\n"
    "v_mfma_f32_16x16x32_bf16 v[92:95], v[204:207], a[76:79], v[92:95]\n"
    "buffer_load_dwordx4 v[180:183], v0, s[40:43], s94 offen offset:1024 sc1\n"
    "s_waitcnt vmcnt(14)\n"
    "v_mfma_f32_16x16x32_bf16 v[96:99], v[208:211], a[64:67], v[96:99]\n"
    "v_mfma_f32_16x16x32_bf16 v[100:103], v[208:211], a[68:71], v[100:103]\n"
    "v_mfma_f32_16x16x32_bf16 v[104:107], v[208:211], a[72:75], v[104:107]\n"
    "v_mfma_f32_16x16x32_bf16 v[108:111], v[208:211], a[76:79], v[108:111]\n"
    "buffer_load_dwordx4 v[184:187], v0, s[40:43], s94 offen offset:2048 sc1\n"
    "s_waitcnt vmcnt(14)\n"
    "v_mfma_f32_16x16x32_bf16 v[112:115], v[212:215], a[64:67], v[112:115]\n"
    "v_mfma_f32_16x16x32_bf16 v[116:119], v[212:215], a[68:71], v[116:119]\n"
    "v_mfma_f32_16x16x32_bf16 v[120:123], v[212:215], a[72:75], v[120:123]\n"
    "v_mfma_f32_16x16x32_bf16 v[124:127], v[212:215], a[76:79], v[124:127]\n"
    "s_add_u32 s93, s90, 7\n"
    "s_and_b32 s93, s93, 15\n"
    "s_mul_i32 s93, s93, 7168\n"
    "s_add_u32 s93, s93, s58\n"
    "s_add_u32 s94, s93, 4096\n"
    "buffer_load_dwordx4 v[188:191], v0, s[40:43], s93 offen offset:0 sc1\n"
    "s_waitcnt vmcnt(14)\n"
    "v_mfma_f32_16x16x32_bf16 v[16:19], v[216:219], a[80:83], v[16:19]\n"
    "v_mfma_f32_16x16x32_bf16 v[20:23], v[216:219], a[84:87], v[20:23]\n"
    "v_mfma_f32_16x16x32_bf16 v[24:27], v[216:219], a[88:91], v[24:27]\n"
    "v_mfma_f32_16x16x32_bf16 v[28:31], v[216:219], a[92:95], v[28:31]\n"
    "buffer_load_dwordx4 v[192:195], v0, s[40:43], s93 offen offset:1024 sc1\n"
    "s_waitcnt vmcnt(14)\n"
    "v_mfma_f32_16x16x32_bf16 v[32:35], v[220:223], a[80:83], v[32:35]\n"
    "v_mfma_f32_16x16x32_bf16 v[36:39], v[220:223], a[84:87], v[36:39]\n"
    "v_mfma_f32_16x16x32_bf16 v[40:43], v[220:223], a[88:91], v[40:43]\n"
    "v_mfma_f32_16x16x32_bf16 v[44:47], v[220:223], a[92:95], v[44:47]\n"
    "buffer_load_dwordx4 v[196:199], v0, s[40:43], s93 offen offset:2048 sc1\n"
    "s_waitcnt vmcnt(14)\n"
    "v_mfma_f32_16x16x32_bf16 v[48:51], v[224:227], a[80:83], v[48:51]\n"
    "v_mfma_f32_16x16x32_bf16 v[52:55], v[224:227], a[84:87], v[52:55]\n"
    "v_mfma_f32_16x16x32_bf16 v[56:59], v[224:227], a[88:91], v[56:59]\n"
    "v_mfma_f32_16x16x32_bf16 v[60:63], v[224:227], a[92:95], v[60:63]\n"
    "buffer_load_dwordx4 v[200:203], v0, s[40:43], s93 offen offset:3072 sc1\n"
    "s_waitcnt vmcnt(14)\n"
    "v_mfma_f32_16x16x32_bf16 v[64:67], v[228:231], a[80:83], v[64:67]\n"
    "v_mfma_f32_16x16x32_bf16 v[68:71], v[228:231], a[84:87], v[68:71]\n"
    "v_mfma_f32_16x16x32_bf16 v[72:75], v[228:231], a[88:91], v[72:75]\n"
    "v_mfma_f32_16x16x32_bf16 v[76:79], v[228:231], a[92:95], v[76:79]\n"
    "buffer_load_dwordx4 v[204:207], v0, s[40:43], s94 offen offset:0 sc1\n"
    "s_waitcnt vmcnt(14)\n"
    "v_mfma_f32_16x16x32_bf16 v[80:83], v[232:235], a[80:83], v[80:83]\n"
    "v_mfma_f32_16x16x32_bf16 v[84:87], v[232:235], a[84:87], v[84:87]\n"
    "v_mfma_f32_16x16x32_bf16 v[88:91], v[232:235], a[88:91], v[88:91]\n"
    "v_mfma_f32_16x16x32_bf16 v[92:95], v[232:235], a[92:95], v[92:95]\n"
    "buffer_load_dwordx4 v[208:211], v0, s[40:43], s94 offen offset:1024 sc1\n"
    "s_waitcnt vmcnt(14)\n"
    "v_mfma_f32_16x16x32_bf16 v[96:99], v[236:239], a[80:83], v[96:99]\n"
    "v_mfma_f32_16x16x32_bf16 v[100:103], v[236:239], a[84:87], v[100:103]\n"
    "v_mfma_f32_16x16x32_bf16 v[104:107], v[236:239], a[88:91], v[104:107]\n"
    "v_mfma_f32_16x16x32_bf16 v[108:111], v[236:239], a[92:95], v[108:111]\n"
    "buffer_load_dwordx4 v[212:215], v0, s[40:43], s94 offen offset:2048 sc1\n"
    "s_waitcnt vmcnt(14)\n"
    "v_mfma_f32_16x16x32_bf16 v[112:115], v[240:243], a[80:83], v[112:115]\n"
    "v_mfma_f32_16x16x32_bf16 v[116:119], v[240:243], a[84:87], v[116:119]\n"
    "v_mfma_f32_16x16x32_bf16 v[120:123], v[240:243], a[88:91], v[120:123]\n"
    "v_mfma_f32_16x16x32_bf16 v[124:127], v[240:243], a[92:95], v[124:127]\n"
    "s_add_u32 s93, s90, 8\n"
    "s_and_b32 s93, s93, 15\n"
    "s_mul_i32 s93, s93, 7168\n"
    "s_add_u32 s93, s93, s58\n"
    "s_add_u32 s94, s93, 4096\n"
    "buffer_load_dwordx4 v[216:219], v0, s[40:43], s93 offen offset:0 sc1\n"
    "s_waitcnt vmcnt(14)\n"
    "v_mfma_f32_16x16x32_bf16 v[16:19], v[160:163], a[96:99], v[16:19]\n"
    "v_mfma_f32_16x16x32_bf16 v[20:23], v[160:163], a[100:103], v[20:23]\n"
    "v_mfma_f32_16x16x32_bf16 v[24:27], v[160:163], a[104:107], v[24:27]\n"
    "v_mfma_f32_16x16x32_bf16 v[28:31], v[160:163], a[108:111], v[28:31]\n"
    "buffer_load_dwordx4 v[220:223], v0, s[40:43], s93 offen offset:1024 sc1\n"
    "s_waitcnt vmcnt(14)\n"
    "v_mfma_f32_16x16x32_bf16 v[32:35], v[164:167], a[96:99], v[32:35]\n"
    "v_mfma_f32_16x16x32_bf16 v[36:39], v[164:167], a[100:103], v[36:39]\n"
    "v_mfma_f32_16x16x32_bf16 v[40:43], v[164:167], a[104:107], v[40:43]\n"
    "v_mfma_f32_16x16x32_bf16 v[44:47], v[164:167], a[108:111], v[44:47]\n"
    "buffer_load_dwordx4 v[224:227], v0, s[40:43], s93 offen offset:2048 sc1\n"
    "s_waitcnt vmcnt(14)\n"
    "v_mfma_f32_16x16x32_bf16 v[48:51], v[168:171], a[96:99], v[48:51]\n"
    "v_mfma_f32_16x16x32_bf16 v[52:55], v[168:171], a[100:103], v[52:55]\n"
    "v_mfma_f32_16x16x32_bf16 v[56:59], v[168:171], a[104:107], v[56:59]\n"
    "v_mfma_f32_16x16x32_bf16 v[60:63], v[168:171], a[108:111], v[60:63]\n"
    "buffer_load_dwordx4 v[228:231], v0, s[40:43], s93 offen offset:3072 sc1\n"
    "s_waitcnt vmcnt(14)\n"
    "v_mfma_f32_16x16x32_bf16 v[64:67], v[172:175], a[96:99], v[64:67]\n"
    "v_mfma_f32_16x16x32_bf16 v[68:71], v[172:175], a[100:103], v[68:71]\n"
    "v_mfma_f32_16x16x32_bf16 v[72:75], v[172:175], a[104:107], v[72:75]\n"
    "v_mfma_f32_16x16x32_bf16 v[76:79], v[172:175], a[108:111], v[76:79]\n"
    "buffer_load_dwordx4 v[232:235], v0, s[40:43], s94 offen offset:0 sc1\n"
    "s_waitcnt vmcnt(14)\n"
    "v_mfma_f32_16x16x32_bf16 v[80:83], v[176:179], a[96:99], v[80:83]\n"
    "v_mfma_f32_16x16x32_bf16 v[84:87], v[176:179], a[100:103], v[84:87]\n"
    "v_mfma_f32_16x16x32_bf16 v[88:91], v[176:179], a[104:107], v[88:91]\n"
    "v_mfma_f32_16x16x32_bf16 v[92:95], v[176:179], a[108:111], v[92:95]\n"
    "buffer_load_dwordx4 v[236:239], v0, s[40:43], s94 offen offset:1024 sc1\n"
    "s_waitcnt vmcnt(14)\n"
    "v_mfma_f32_16x16x32_bf16 v[96:99], v[180:183], a[96:99], v[96:99]\n"
    "v_mfma_f32_16x16x32_bf16 v[100:103], v[180:183], a[100:103], v[100:103]\n"
    "v_mfma_f32_16x16x32_bf16 v[104:107], v[180:183], a[104:107], v[104:107]\n"
    "v_mfma_f32_16x16x32_bf16 v[108:111], v[180:183], a[108:111], v[108:111]\n"
    "buffer_load_dwordx4 v[240:243], v0, s[40:43], s94 offen offset:2048 sc1\n"
    "s_waitcnt vmcnt(14)\n"
    "v_mfma_f32_16x16x32_bf16 v[112:115], v[184:187], a[96:99], v[112:115]\n"
    "v_mfma_f32_16x16x32_bf16 v[116:119], v[184:187], a[100:103], v[116:119]\n"
    "v_mfma_f32_16x16x32_bf16 v[120:123], v[184:187], a[104:107], v[120:123]\n"
    "v_mfma_f32_16x16x32_bf16 v[124:127], v[184:187], a[108:111], v[124:127]\n"
    "s_add_u32 s93, s90, 9\n"
    "s_and_b32 s93, s93, 15\n"
    "s_mul_i32 s93, s93, 7168\n"
    "s_add_u32 s93, s93, s58\n"
    "s_add_u32 s94, s93, 4096\n"
    "buffer_load_dwordx4 v[160:163], v0, s[40:43], s93 offen offset:0 sc1\n"
    "s_waitcnt vmcnt(14)\n"
    "v_mfma_f32_16x16x32_bf16 v[16:19], v[188:191], a[112:115], v[16:19]\n"
    "v_mfma_f32_16x16x32_bf16 v[20:23], v[188:191], a[116:119], v[20:23]\n"
    "v_mfma_f32_16x16x32_bf16 v[24:27], v[188:191], a[120:123], v[24:27]\n"
    "v_mfma_f32_16x16x32_bf16 v[28:31], v[188:191], a[124:127], v[28:31]\n"
    "buffer_load_dwordx4 v[164:167], v0, s[40:43], s93 offen offset:1024 sc1\n"
    "s_waitcnt vmcnt(14)\n"
    "v_mfma_f32_16x16x32_bf16 v[32:35], v[192:195], a[112:115], v[32:35]\n"
    "v_mfma_f32_16x16x32_bf16 v[36:39], v[192:195], a[116:119], v[36:39]\n"
    "v_mfma_f32_16x16x32_bf16 v[40:43], v[192:195], a[120:123], v[40:43]\n"
    "v_mfma_f32_16x16x32_bf16 v[44:47], v[192:195], a[124:127], v[44:47]\n"
    "buffer_load_dwordx4 v[168:171], v0, s[40:43], s93 offen offset:2048 sc1\n"
    "s_waitcnt vmcnt(14)\n"
    "v_mfma_f32_16x16x32_bf16 v[48:51], v[196:199], a[112:115], v[48:51]\n"
    "v_mfma_f32_16x16x32_bf16 v[52:55], v[196:199], a[116:119], v[52:55]\n"
    "v_mfma_f32_16x16x32_bf16 v[56:59], v[196:199], a[120:123], v[56:59]\n"
    "v_mfma_f32_16x16x32_bf16 v[60:63], v[196:199], a[124:127], v[60:63]\n"
    "buffer_load_dwordx4 v[172:175], v0, s[40:43], s93 offen offset:3072 sc1\n"
    "s_waitcnt vmcnt(14)\n"
    "v_mfma_f32_16x16x32_bf16 v[64:67], v[200:203], a[112:115], v[64:67]\n"
    "v_mfma_f32_16x16x32_bf16 v[68:71], v[200:203], a[116:119], v[68:71]\n"
    "v_mfma_f32_16x16x32_bf16 v[72:75], v[200:203], a[120:123], v[72:75]\n"
    "v_mfma_f32_16x16x32_bf16 v[76:79], v[200:203], a[124:127], v[76:79]\n"
    "buffer_load_dwordx4 v[176:179], v0, s[40:43], s94 offen offset:0 sc1\n"
    "s_waitcnt vmcnt(14)\n"
    "v_mfma_f32_16x16x32_bf16 v[80:83], v[204:207], a[112:115], v[80:83]\n"
    "v_mfma_f32_16x16x32_bf16 v[84:87], v[204:207], a[116:119], v[84:87]\n"
    "v_mfma_f32_16x16x32_bf16 v[88:91], v[204:207], a[120:123], v[88:91]\n"
    "v_mfma_f32_16x16x32_bf16 v[92:95], v[204:207], a[124:127], v[92:95]\n"
    "buffer_load_dwordx4 v[180:183], v0, s[40:43], s94 offen offset:1024 sc1\n"
    "s_waitcnt vmcnt(14)\n"
    "v_mfma_f32_16x16x32_bf16 v[96:99], v[208:211], a[112:115], v[96:99]\n"
    "v_mfma_f32_16x16x32_bf16 v[100:103], v[208:211], a[116:119], v[100:103]\n"
    "v_mfma_f32_16x16x32_bf16 v[104:107], v[208:211], a[120:123], v[104:107]\n"
    "v_mfma_f32_16x16x32_bf16 v[108:111], v[208:211], a[124:127], v[108:111]\n"
    "buffer_load_dwordx4 v[184:187], v0, s[40:43], s94 offen offset:2048 sc1\n"
    "s_waitcnt vmcnt(14)\n"
    "v_mfma_f32_16x16x32_bf16 v[112:115], v[212:215], a[112:115], v[112:115]\n"
    "v_mfma_f32_16x16x32_bf16 v[116:119], v[212:215], a[116:119], v[116:119]\n"
    "v_mfma_f32_16x16x32_bf16 v[120:123], v[212:215], a[120:123], v[120:123]\n"
    "v_mfma_f32_16x16x32_bf16 v[124:127], v[212:215], a[124:127], v[124:127]\n"
    "s_add_u32 s93, s90, 10\n"
    "s_and_b32 s93, s93, 15\n"
    "s_mul_i32 s93, s93, 7168\n"
    "s_add_u32 s93, s93, s58\n"
    "s_add_u32 s94, s93, 4096\n"
    "buffer_load_dwordx4 v[188:191], v0, s[40:43], s93 offen offset:0 sc1\n"
    "s_waitcnt vmcnt(14)\n"
    "v_mfma_f32_16x16x32_bf16 v[16:19], v[216:219], a[128:131], v[16:19]\n"
    "v_mfma_f32_16x16x32_bf16 v[20:23], v[216:219], a[132:135], v[20:23]\n"
    "v_mfma_f32_16x16x32_bf16 v[24:27], v[216:219], a[136:139], v[24:27]\n"
    "v_mfma_f32_16x16x32_bf16 v[28:31], v[216:219], a[140:143], v[28:31]\n"
    "buffer_load_dwordx4 v[192:195], v0, s[40:43], s93 offen offset:1024 sc1\n"
    "s_waitcnt vmcnt(14)\n"
    "v_mfma_f32_16x16x32_bf16 v[32:35], v[220:223], a[128:131], v[32:35]\n"
    "v_mfma_f32_16x16x32_bf16 v[36:39], v[220:223], a[132:135], v[36:39]\n"
    "v_mfma_f32_16x16x32_bf16 v[40:43], v[220:223], a[136:139], v[40:43]\n"
    "v_mfma_f32_16x16x32_bf16 v[44:47], v[220:223], a[140:143], v[44:47]\n"
    "buffer_load_dwordx4 v[196:199], v0, s[40:43], s93 offen offset:2048 sc1\n"
    "s_waitcnt vmcnt(14)\n"
    "v_mfma_f32_16x16x32_bf16 v[48:51], v[224:227], a[128:131], v[48:51]\n"
    "v_mfma_f32_16x16x32_bf16 v[52:55], v[224:227], a[132:135], v[52:55]\n"
    "v_mfma_f32_16x16x32_bf16 v[56:59], v[224:227], a[136:139], v[56:59]\n"
    "v_mfma_f32_16x16x32_bf16 v[60:63], v[224:227], a[140:143], v[60:63]\n"
    "buffer_load_dwordx4 v[200:203], v0, s[40:43], s93 offen offset:3072 sc1\n"
    "s_waitcnt vmcnt(14)\n"
    "v_mfma_f32_16x16x32_bf16 v[64:67], v[228:231], a[128:131], v[64:67]\n"
    "v_mfma_f32_16x16x32_bf16 v[68:71], v[228:231], a[132:135], v[68:71]\n"
    "v_mfma_f32_16x16x32_bf16 v[72:75], v[228:231], a[136:139], v[72:75]\n"
    "v_mfma_f32_16x16x32_bf16 v[76:79], v[228:231], a[140:143], v[76:79]\n"
    "buffer_load_dwordx4 v[204:207], v0, s[40:43], s94 offen offset:0 sc1\n"
    "s_waitcnt vmcnt(14)\n"
    "v_mfma_f32_16x16x32_bf16 v[80:83], v[232:235], a[128:131], v[80:83]\n"
    "v_mfma_f32_16x16x32_bf16 v[84:87], v[232:235], a[132:135], v[84:87]\n"
    "v_mfma_f32_16x16x32_bf16 v[88:91], v[232:235], a[136:139], v[88:91]\n"
    "v_mfma_f32_16x16x32_bf16 v[92:95], v[232:235], a[140:143], v[92:95]\n"
    "buffer_load_dwordx4 v[208:211], v0, s[40:43], s94 offen offset:1024 sc1\n"
    "s_waitcnt vmcnt(14)\n"
    "v_mfma_f32_16x16x32_bf16 v[96:99], v[236:239], a[128:131], v[96:99]\n"
    "v_mfma_f32_16x16x32_bf16 v[100:103], v[236:239], a[132:135], v[100:103]\n"
    "v_mfma_f32_16x16x32_bf16 v[104:107], v[236:239], a[136:139], v[104:107]\n"
    "v_mfma_f32_16x16x32_bf16 v[108:111], v[236:239], a[140:143], v[108:111]\n"
    "buffer_load_dwordx4 v[212:215], v0, s[40:43], s94 offen offset:2048 sc1\n"
    "s_waitcnt vmcnt(14)\n"
    "v_mfma_f32_16x16x32_bf16 v[112:115], v[240:243], a[128:131], v[112:115]\n"
    "v_mfma_f32_16x16x32_bf16 v[116:119], v[240:243], a[132:135], v[116:119]\n"
    "v_mfma_f32_16x16x32_bf16 v[120:123], v[240:243], a[136:139], v[120:123]\n"
    "v_mfma_f32_16x16x32_bf16 v[124:127], v[240:243], a[140:143], v[124:127]\n"
    "s_add_u32 s93, s90, 11\n"
    "s_and_b32 s93, s93, 15\n"
    "s_mul_i32 s93, s93, 7168\n"
    "s_add_u32 s93, s93, s58\n"
    "s_add_u32 s94, s93, 4096\n"
    "buffer_load_dwordx4 v[216:219], v0, s[40:43], s93 offen offset:0 sc1\n"
    "s_waitcnt vmcnt(14)\n"
    "v_mfma_f32_16x16x32_bf16 v[16:19], v[160:163], a[144:147], v[16:19]\n"
    "v_mfma_f32_16x16x32_bf16 v[20:23], v[160:163], a[148:151], v[20:23]\n"
    "v_mfma_f32_16x16x32_bf16 v[24:27], v[160:163], a[152:155], v[24:27]\n"
    "v_mfma_f32_16x16x32_bf16 v[28:31], v[160:163], a[156:159], v[28:31]\n"
    "buffer_load_dwordx4 v[220:223], v0, s[40:43], s93 offen offset:1024 sc1\n"
    "s_waitcnt vmcnt(14)\n"
    "v_mfma_f32_16x16x32_bf16 v[32:35], v[164:167], a[144:147], v[32:35]\n"
    "v_mfma_f32_16x16x32_bf16 v[36:39], v[164:167], a[148:151], v[36:39]\n"
    "v_mfma_f32_16x16x32_bf16 v[40:43], v[164:167], a[152:155], v[40:43]\n"
    "v_mfma_f32_16x16x32_bf16 v[44:47], v[164:167], a[156:159], v[44:47]\n"
    "buffer_load_dwordx4 v[224:227], v0, s[40:43], s93 offen offset:2048 sc1\n"
    "s_waitcnt vmcnt(14)\n"
    "v_mfma_f32_16x16x32_bf16 v[48:51], v[168:171], a[144:147], v[48:51]\n"
    "v_mfma_f32_16x16x32_bf16 v[52:55], v[168:171], a[148:151], v[52:55]\n"
    "v_mfma_f32_16x16x32_bf16 v[56:59], v[168:171], a[152:155], v[56:59]\n"
    "v_mfma_f32_16x16x32_bf16 v[60:63], v[168:171], a[156:159], v[60:63]\n"
    "buffer_load_dwordx4 v[228:231], v0, s[40:43], s93 offen offset:3072 sc1\n"
    "s_waitcnt vmcnt(14)\n"
    "v_mfma_f32_16x16x32_bf16 v[64:67], v[172:175], a[144:147], v[64:67]\n"
    "v_mfma_f32_16x16x32_bf16 v[68:71], v[172:175], a[148:151], v[68:71]\n"
    "v_mfma_f32_16x16x32_bf16 v[72:75], v[172:175], a[152:155], v[72:75]\n"
    "v_mfma_f32_16x16x32_bf16 v[76:79], v[172:175], a[156:159], v[76:79]\n"
    "buffer_load_dwordx4 v[232:235], v0, s[40:43], s94 offen offset:0 sc1\n"
    "s_waitcnt vmcnt(14)\n"
    "v_mfma_f32_16x16x32_bf16 v[80:83], v[176:179], a[144:147], v[80:83]\n"
    "v_mfma_f32_16x16x32_bf16 v[84:87], v[176:179], a[148:151], v[84:87]\n"
    "v_mfma_f32_16x16x32_bf16 v[88:91], v[176:179], a[152:155], v[88:91]\n"
    "v_mfma_f32_16x16x32_bf16 v[92:95], v[176:179], a[156:159], v[92:95]\n"
    "buffer_load_dwordx4 v[236:239], v0, s[40:43], s94 offen offset:1024 sc1\n"
    "s_waitcnt vmcnt(14)\n"
    "v_mfma_f32_16x16x32_bf16 v[96:99], v[180:183], a[144:147], v[96:99]\n"
    "v_mfma_f32_16x16x32_bf16 v[100:103], v[180:183], a[148:151], v[100:103]\n"
    "v_mfma_f32_16x16x32_bf16 v[104:107], v[180:183], a[152:155], v[104:107]\n"
    "v_mfma_f32_16x16x32_bf16 v[108:111], v[180:183], a[156:159], v[108:111]\n"
    "buffer_load_dwordx4 v[240:243], v0, s[40:43], s94 offen offset:2048 sc1\n"
    "s_waitcnt vmcnt(14)\n"
    "v_mfma_f32_16x16x32_bf16 v[112:115], v[184:187], a[144:147], v[112:115]\n"
    "v_mfma_f32_16x16x32_bf16 v[116:119], v[184:187], a[148:151], v[116:119]\n"
    "v_mfma_f32_16x16x32_bf16 v[120:123], v[184:187], a[152:155], v[120:123]\n"
    "v_mfma_f32_16x16x32_bf16 v[124:127], v[184:187], a[156:159], v[124:127]\n"
    "s_add_u32 s93, s90, 12\n"
    "s_and_b32 s93, s93, 15\n"
    "s_mul_i32 s93, s93, 7168\n"
    "s_add_u32 s93, s93, s58\n"
    "s_add_u32 s94, s93, 4096\n"
    "buffer_load_dwordx4 v[160:163], v0, s[40:43], s93 offen offset:0 sc1\n"
    "s_waitcnt vmcnt(14)\n"
    "v_mfma_f32_16x16x32_bf16 v[16:19], v[188:191], a[160:163], v[16:19]\n"
    "v_mfma_f32_16x16x32_bf16 v[20:23], v[188:191], a[164:167], v[20:23]\n"
    "v_mfma_f32_16x16x32_bf16 v[24:27], v[188:191], a[168:171], v[24:27]\n"
    "v_mfma_f32_16x16x32_bf16 v[28:31], v[188:191], a[172:175], v[28:31]\n"
    "buffer_load_dwordx4 v[164:167], v0, s[40:43], s93 offen offset:1024 sc1\n"
    "s_waitcnt vmcnt(14)\n"
    "v_mfma_f32_16x16x32_bf16 v[32:35], v[192:195], a[160:163], v[32:35]\n"
    "v_mfma_f32_16x16x32_bf16 v[36:39], v[192:195], a[164:167], v[36:39]\n"
    "v_mfma_f32_16x16x32_bf16 v[40:43], v[192:195], a[168:171], v[40:43]\n"
    "v_mfma_f32_16x16x32_bf16 v[44:47], v[192:195], a[172:175], v[44:47]\n"
    "buffer_load_dwordx4 v[168:171], v0, s[40:43], s93 offen offset:2048 sc1\n"
    "s_waitcnt vmcnt(14)\n"
    "v_mfma_f32_16x16x32_bf16 v[48:51], v[196:199], a[160:163], v[48:51]\n"
    "v_mfma_f32_16x16x32_bf16 v[52:55], v[196:199], a[164:167], v[52:55]\n"
    "v_mfma_f32_16x16x32_bf16 v[56:59], v[196:199], a[168:171], v[56:59]\n"
    "v_mfma_f32_16x16x32_bf16 v[60:63], v[196:199], a[172:175], v[60:63]\n"
    "buffer_load_dwordx4 v[172:175], v0, s[40:43], s93 offen offset:3072 sc1\n"
    "s_waitcnt vmcnt(14)\n"
    "v_mfma_f32_16x16x32_bf16 v[64:67], v[200:203], a[160:163], v[64:67]\n"
    "v_mfma_f32_16x16x32_bf16 v[68:71], v[200:203], a[164:167], v[68:71]\n"
    "v_mfma_f32_16x16x32_bf16 v[72:75], v[200:203], a[168:171], v[72:75]\n"
    "v_mfma_f32_16x16x32_bf16 v[76:79], v[200:203], a[172:175], v[76:79]\n"
    "buffer_load_dwordx4 v[176:179], v0, s[40:43], s94 offen offset:0 sc1\n"
    "s_waitcnt vmcnt(14)\n"
    "v_mfma_f32_16x16x32_bf16 v[80:83], v[204:207], a[160:163], v[80:83]\n"
    "v_mfma_f32_16x16x32_bf16 v[84:87], v[204:207], a[164:167], v[84:87]\n"
    "v_mfma_f32_16x16x32_bf16 v[88:91], v[204:207], a[168:171], v[88:91]\n"
    "v_mfma_f32_16x16x32_bf16 v[92:95], v[204:207], a[172:175], v[92:95]\n"
    "buffer_load_dwordx4 v[180:183], v0, s[40:43], s94 offen offset:1024 sc1\n"
    "s_waitcnt vmcnt(14)\n"
    "v_mfma_f32_16x16x32_bf16 v[96:99], v[208:211], a[160:163], v[96:99]\n"
    "v_mfma_f32_16x16x32_bf16 v[100:103], v[208:211], a[164:167], v[100:103]\n"
    "v_mfma_f32_16x16x32_bf16 v[104:107], v[208:211], a[168:171], v[104:107]\n"
    "v_mfma_f32_16x16x32_bf16 v[108:111], v[208:211], a[172:175], v[108:111]\n"
    "buffer_load_dwordx4 v[184:187], v0, s[40:43], s94 offen offset:2048 sc1\n"
    "s_waitcnt vmcnt(14)\n"
    "v_mfma_f32_16x16x32_bf16 v[112:115], v[212:215], a[160:163], v[112:115]\n"
    "v_mfma_f32_16x16x32_bf16 v[116:119], v[212:215], a[164:167], v[116:119]\n"
    "v_mfma_f32_16x16x32_bf16 v[120:123], v[212:215], a[168:171], v[120:123]\n"
    "v_mfma_f32_16x16x32_bf16 v[124:127], v[212:215], a[172:175], v[124:127]\n"
    "s_add_u32 s93, s90, 13\n"
    "s_and_b32 s93, s93, 15\n"
    "s_mul_i32 s93, s93, 7168\n"
    "s_add_u32 s93, s93, s58\n"
    "s_add_u32 s94, s93, 4096\n"
    "buffer_load_dwordx4 v[188:191], v0, s[40:43], s93 offen offset:0 sc1\n"
    "s_waitcnt vmcnt(14)\n"
    "v_mfma_f32_16x16x32_bf16 v[16:19], v[216:219], a[176:179], v[16:19]\n"
    "v_mfma_f32_16x16x32_bf16 v[20:23], v[216:219], a[180:183], v[20:23]\n"
    "v_mfma_f32_16x16x32_bf16 v[24:27], v[216:219], a[184:187], v[24:27]\n"
    "v_mfma_f32_16x16x32_bf16 v[28:31], v[216:219], a[188:191], v[28:31]\n"
    "buffer_load_dwordx4 v[192:195], v0, s[40:43], s93 offen offset:1024 sc1\n"
    "s_waitcnt vmcnt(14)\n"
    "v_mfma_f32_16x16x32_bf16 v[32:35], v[220:223], a[176:179], v[32:35]\n"
    "v_mfma_f32_16x16x32_bf16 v[36:39], v[220:223], a[180:183], v[36:39]\n"
    "v_mfma_f32_16x16x32_bf16 v[40:43], v[220:223], a[184:187], v[40:43]\n"
    "v_mfma_f32_16x16x32_bf16 v[44:47], v[220:223], a[188:191], v[44:47]\n"
    "buffer_load_dwordx4 v[196:199], v0, s[40:43], s93 offen offset:2048 sc1\n"
    "s_waitcnt vmcnt(14)\n"
    "v_mfma_f32_16x16x32_bf16 v[48:51], v[224:227], a[176:179], v[48:51]\n"
    "v_mfma_f32_16x16x32_bf16 v[52:55], v[224:227], a[180:183], v[52:55]\n"
    "v_mfma_f32_16x16x32_bf16 v[56:59], v[224:227], a[184:187], v[56:59]\n"
    "v_mfma_f32_16x16x32_bf16 v[60:63], v[224:227], a[188:191], v[60:63]\n"
    "buffer_load_dwordx4 v[200:203], v0, s[40:43], s93 offen offset:3072 sc1\n"
    "s_waitcnt vmcnt(14)\n"
    "v_mfma_f32_16x16x32_bf16 v[64:67], v[228:231], a[176:179], v[64:67]\n"
    "v_mfma_f32_16x16x32_bf16 v[68:71], v[228:231], a[180:183], v[68:71]\n"
    "v_mfma_f32_16x16x32_bf16 v[72:75], v[228:231], a[184:187], v[72:75]\n"
    "v_mfma_f32_16x16x32_bf16 v[76:79], v[228:231], a[188:191], v[76:79]\n"
    "buffer_load_dwordx4 v[204:207], v0, s[40:43], s94 offen offset:0 sc1\n"
    "s_waitcnt vmcnt(14)\n"
    "v_mfma_f32_16x16x32_bf16 v[80:83], v[232:235], a[176:179], v[80:83]\n"
    "v_mfma_f32_16x16x32_bf16 v[84:87], v[232:235], a[180:183], v[84:87]\n"
    "v_mfma_f32_16x16x32_bf16 v[88:91], v[232:235], a[184:187], v[88:91]\n"
    "v_mfma_f32_16x16x32_bf16 v[92:95], v[232:235], a[188:191], v[92:95]\n"
    "buffer_load_dwordx4 v[208:211], v0, s[40:43], s94 offen offset:1024 sc1\n"
    "s_waitcnt vmcnt(14)\n"
    "v_mfma_f32_16x16x32_bf16 v[96:99], v[236:239], a[176:179], v[96:99]\n"
    "v_mfma_f32_16x16x32_bf16 v[100:103], v[236:239], a[180:183], v[100:103]\n"
    "v_mfma_f32_16x16x32_bf16 v[104:107], v[236:239], a[184:187], v[104:107]\n"
    "v_mfma_f32_16x16x32_bf16 v[108:111], v[236:239], a[188:191], v[108:111]\n"
    "buffer_load_dwordx4 v[212:215], v0, s[40:43], s94 offen offset:2048 sc1\n"
    "s_waitcnt vmcnt(14)\n"
    "v_mfma_f32_16x16x32_bf16 v[112:115], v[240:243], a[176:179], v[112:115]\n"
    "v_mfma_f32_16x16x32_bf16 v[116:119], v[240:243], a[180:183], v[116:119]\n"
    "v_mfma_f32_16x16x32_bf16 v[120:123], v[240:243], a[184:187], v[120:123]\n"
    "v_mfma_f32_16x16x32_bf16 v[124:127], v[240:243], a[188:191], v[124:127]\n"
    "s_add_u32 s93, s90, 14\n"
    "s_and_b32 s93, s93, 15\n"
    "s_mul_i32 s93, s93, 7168\n"
    "s_add_u32 s93, s93, s58\n"
    "s_add_u32 s94, s93, 4096\n"
    "buffer_load_dwordx4 v[216:219], v0, s[40:43], s93 offen offset:0 sc1\n"
    "s_waitcnt vmcnt(14)\n"
    "v_mfma_f32_16x16x32_bf16 v[16:19], v[160:163], a[192:195], v[16:19]\n"
    "v_mfma_f32_16x16x32_bf16 v[20:23], v[160:163], a[196:199], v[20:23]\n"
    "v_mfma_f32_16x16x32_bf16 v[24:27], v[160:163], a[200:203], v[24:27]\n"
    "v_mfma_f32_16x16x32_bf16 v[28:31], v[160:163], a[204:207], v[28:31]\n"
    "buffer_load_dwordx4 v[220:223], v0, s[40:43], s93 offen offset:1024 sc1\n"
    "s_waitcnt vmcnt(14)\n"
    "v_mfma_f32_16x16x32_bf16 v[32:35], v[164:167], a[192:195], v[32:35]\n"
    "v_mfma_f32_16x16x32_bf16 v[36:39], v[164:167], a[196:199], v[36:39]\n"
    "v_mfma_f32_16x16x32_bf16 v[40:43], v[164:167], a[200:203], v[40:43]\n"
    "v_mfma_f32_16x16x32_bf16 v[44:47], v[164:167], a[204:207], v[44:47]\n"
    "buffer_load_dwordx4 v[224:227], v0, s[40:43], s93 offen offset:2048 sc1\n"
    "s_waitcnt vmcnt(14)\n"
    "v_mfma_f32_16x16x32_bf16 v[48:51], v[168:171], a[192:195], v[48:51]\n"
    "v_mfma_f32_16x16x32_bf16 v[52:55], v[168:171], a[196:199], v[52:55]\n"
    "v_mfma_f32_16x16x32_bf16 v[56:59], v[168:171], a[200:203], v[56:59]\n"
    "v_mfma_f32_16x16x32_bf16 v[60:63], v[168:171], a[204:207], v[60:63]\n"
    "buffer_load_dwordx4 v[228:231], v0, s[40:43], s93 offen offset:3072 sc1\n"
    "s_waitcnt vmcnt(14)\n"
    "v_mfma_f32_16x16x32_bf16 v[64:67], v[172:175], a[192:195], v[64:67]\n"
    "v_mfma_f32_16x16x32_bf16 v[68:71], v[172:175], a[196:199], v[68:71]\n"
    "v_mfma_f32_16x16x32_bf16 v[72:75], v[172:175], a[200:203], v[72:75]\n"
    "v_mfma_f32_16x16x32_bf16 v[76:79], v[172:175], a[204:207], v[76:79]\n"
    "buffer_load_dwordx4 v[232:235], v0, s[40:43], s94 offen offset:0 sc1\n"
    "s_waitcnt vmcnt(14)\n"
    "v_mfma_f32_16x16x32_bf16 v[80:83], v[176:179], a[192:195], v[80:83]\n"
    "v_mfma_f32_16x16x32_bf16 v[84:87], v[176:179], a[196:199], v[84:87]\n"
    "v_mfma_f32_16x16x32_bf16 v[88:91], v[176:179], a[200:203], v[88:91]\n"
    "v_mfma_f32_16x16x32_bf16 v[92:95], v[176:179], a[204:207], v[92:95]\n"
    "buffer_load_dwordx4 v[236:239], v0, s[40:43], s94 offen offset:1024 sc1\n"
    "s_waitcnt vmcnt(14)\n"
    "v_mfma_f32_16x16x32_bf16 v[96:99], v[180:183], a[192:195], v[96:99]\n"
    "v_mfma_f32_16x16x32_bf16 v[100:103], v[180:183], a[196:199], v[100:103]\n"
    "v_mfma_f32_16x16x32_bf16 v[104:107], v[180:183], a[200:203], v[104:107]\n"
    "v_mfma_f32_16x16x32_bf16 v[108:111], v[180:183], a[204:207], v[108:111]\n"
    "buffer_load_dwordx4 v[240:243], v0, s[40:43], s94 offen offset:2048 sc1\n"
    "s_waitcnt vmcnt(14)\n"
    "v_mfma_f32_16x16x32_bf16 v[112:115], v[184:187], a[192:195], v[112:115]\n"
    "v_mfma_f32_16x16x32_bf16 v[116:119], v[184:187], a[196:199], v[116:119]\n"
    "v_mfma_f32_16x16x32_bf16 v[120:123], v[184:187], a[200:203], v[120:123]\n"
    "v_mfma_f32_16x16x32_bf16 v[124:127], v[184:187], a[204:207], v[124:127]\n"
    "s_add_u32 s93, s90, 15\n"
    "s_and_b32 s93, s93, 15\n"
    "s_mul_i32 s93, s93, 7168\n"
    "s_add_u32 s93, s93, s58\n"
    "s_add_u32 s94, s93, 4096\n"
    "buffer_load_dwordx4 v[160:163], v0, s[40:43], s93 offen offset:0 sc1\n"
    "s_waitcnt vmcnt(14)\n"
    "v_mfma_f32_16x16x32_bf16 v[16:19], v[188:191], a[208:211], v[16:19]\n"
    "v_mfma_f32_16x16x32_bf16 v[20:23], v[188:191], a[212:215], v[20:23]\n"
    "v_mfma_f32_16x16x32_bf16 v[24:27], v[188:191], a[216:219], v[24:27]\n"
    "v_mfma_f32_16x16x32_bf16 v[28:31], v[188:191], a[220:223], v[28:31]\n"
    "buffer_load_dwordx4 v[164:167], v0, s[40:43], s93 offen offset:1024 sc1\n"
    "s_waitcnt vmcnt(14)\n"
    "v_mfma_f32_16x16x32_bf16 v[32:35], v[192:195], a[208:211], v[32:35]\n"
    "v_mfma_f32_16x16x32_bf16 v[36:39], v[192:195], a[212:215], v[36:39]\n"
    "v_mfma_f32_16x16x32_bf16 v[40:43], v[192:195], a[216:219], v[40:43]\n"
    "v_mfma_f32_16x16x32_bf16 v[44:47], v[192:195], a[220:223], v[44:47]\n"
    "buffer_load_dwordx4 v[168:171], v0, s[40:43], s93 offen offset:2048 sc1\n"
    "s_waitcnt vmcnt(14)\n"
    "v_mfma_f32_16x16x32_bf16 v[48:51], v[196:199], a[208:211], v[48:51]\n"
    "v_mfma_f32_16x16x32_bf16 v[52:55], v[196:199], a[212:215], v[52:55]\n"
    "v_mfma_f32_16x16x32_bf16 v[56:59], v[196:199], a[216:219], v[56:59]\n"
    "v_mfma_f32_16x16x32_bf16 v[60:63], v[196:199], a[220:223], v[60:63]\n"
    "buffer_load_dwordx4 v[172:175], v0, s[40:43], s93 offen offset:3072 sc1\n"
    "s_waitcnt vmcnt(14)\n"
    "v_mfma_f32_16x16x32_bf16 v[64:67], v[200:203], a[208:211], v[64:67]\n"
    "v_mfma_f32_16x16x32_bf16 v[68:71], v[200:203], a[212:215], v[68:71]\n"
    "v_mfma_f32_16x16x32_bf16 v[72:75], v[200:203], a[216:219], v[72:75]\n"
    "v_mfma_f32_16x16x32_bf16 v[76:79], v[200:203], a[220:223], v[76:79]\n"
    "buffer_load_dwordx4 v[176:179], v0, s[40:43], s94 offen offset:0 sc1\n"
    "s_waitcnt vmcnt(14)\n"
    "v_mfma_f32_16x16x32_bf16 v[80:83], v[204:207], a[208:211], v[80:83]\n"
    "v_mfma_f32_16x16x32_bf16 v[84:87], v[204:207], a[212:215], v[84:87]\n"
    "v_mfma_f32_16x16x32_bf16 v[88:91], v[204:207], a[216:219], v[88:91]\n"
    "v_mfma_f32_16x16x32_bf16 v[92:95], v[204:207], a[220:223], v[92:95]\n"
    "buffer_load_dwordx4 v[180:183], v0, s[40:43], s94 offen offset:1024 sc1\n"
    "s_waitcnt vmcnt(14)\n"
    "v_mfma_f32_16x16x32_bf16 v[96:99], v[208:211], a[208:211], v[96:99]\n"
    "v_mfma_f32_16x16x32_bf16 v[100:103], v[208:211], a[212:215], v[100:103]\n"
    "v_mfma_f32_16x16x32_bf16 v[104:107], v[208:211], a[216:219], v[104:107]\n"
    "v_mfma_f32_16x16x32_bf16 v[108:111], v[208:211], a[220:223], v[108:111]\n"
    "buffer_load_dwordx4 v[184:187], v0, s[40:43], s94 offen offset:2048 sc1\n"
    "s_waitcnt vmcnt(14)\n"
    "v_mfma_f32_16x16x32_bf16 v[112:115], v[212:215], a[208:211], v[112:115]\n"
    "v_mfma_f32_16x16x32_bf16 v[116:119], v[212:215], a[212:215], v[116:119]\n"
    "v_mfma_f32_16x16x32_bf16 v[120:123], v[212:215], a[216:219], v[120:123]\n"
    "v_mfma_f32_16x16x32_bf16 v[124:127], v[212:215], a[220:223], v[124:127]\n"
    "s_waitcnt vmcnt(13)\n"
    "v_mfma_f32_16x16x32_bf16 v[16:19], v[216:219], a[224:227], v[16:19]\n"
    "v_mfma_f32_16x16x32_bf16 v[20:23], v[216:219], a[228:231], v[20:23]\n"
    "v_mfma_f32_16x16x32_bf16 v[24:27], v[216:219], a[232:235], v[24:27]\n"
    "v_mfma_f32_16x16x32_bf16 v[28:31], v[216:219], a[236:239], v[28:31]\n"
    "s_waitcnt vmcnt(12)\n"
    "v_mfma_f32_16x16x32_bf16 v[32:35], v[220:223], a[224:227], v[32:35]\n"
    "v_mfma_f32_16x16x32_bf16 v[36:39], v[220:223], a[228:231], v[36:39]\n"
    "v_mfma_f32_16x16x32_bf16 v[40:43], v[220:223], a[232:235], v[40:43]\n"
    "v_mfma_f32_16x16x32_bf16 v[44:47], v[220:223], a[236:239], v[44:47]\n"
    "s_waitcnt vmcnt(11)\n"
    "v_mfma_f32_16x16x32_bf16 v[48:51], v[224:227], a[224:227], v[48:51]\n"
    "v_mfma_f32_16x16x32_bf16 v[52:55], v[224:227], a[228:231], v[52:55]\n"
    "v_mfma_f32_16x16x32_bf16 v[56:59], v[224:227], a[232:235], v[56:59]\n"
    "v_mfma_f32_16x16x32_bf16 v[60:63], v[224:227], a[236:239], v[60:63]\n"
    "s_waitcnt vmcnt(10)\n"
    "v_mfma_f32_16x16x32_bf16 v[64:67], v[228:231], a[224:227], v[64:67]\n"
    "v_mfma_f32_16x16x32_bf16 v[68:71], v[228:231], a[228:231], v[68:71]\n"
    "v_mfma_f32_16x16x32_bf16 v[72:75], v[228:231], a[232:235], v[72:75]\n"
    "v_mfma_f32_16x16x32_bf16 v[76:79], v[228:231], a[236:239], v[76:79]\n"
    "s_waitcnt vmcnt(9)\n"
    "v_mfma_f32_16x16x32_bf16 v[80:83], v[232:235], a[224:227], v[80:83]\n"
    "v_mfma_f32_16x16x32_bf16 v[84:87], v[232:235], a[228:231], v[84:87]\n"
    "v_mfma_f32_16x16x32_bf16 v[88:91], v[232:235], a[232:235], v[88:91]\n"
    "v_mfma_f32_16x16x32_bf16 v[92:95], v[232:235], a[236:239], v[92:95]\n"
    "s_waitcnt vmcnt(8)\n"
    "v_mfma_f32_16x16x32_bf16 v[96:99], v[236:239], a[224:227], v[96:99]\n"
    "v_mfma_f32_16x16x32_bf16 v[100:103], v[236:239], a[228:231], v[100:103]\n"
    "v_mfma_f32_16x16x32_bf16 v[104:107], v[236:239], a[232:235], v[104:107]\n"
    "v_mfma_f32_16x16x32_bf16 v[108:111], v[236:239], a[236:239], v[108:111]\n"
    "s_waitcnt vmcnt(7)\n"
    "v_mfma_f32_16x16x32_bf16 v[112:115], v[240:243], a[224:227], v[112:115]\n"
    "v_mfma_f32_16x16x32_bf16 v[116:119], v[240:243], a[228:231], v[116:119]\n"
    "v_mfma_f32_16x16x32_bf16 v[120:123], v[240:243], a[232:235], v[120:123]\n"
    "v_mfma_f32_16x16x32_bf16 v[124:127], v[240:243], a[236:239], v[124:127]\n"
    "s_waitcnt vmcnt(6)\n"
    "v_mfma_f32_16x16x32_bf16 v[16:19], v[160:163], a[240:243], v[16:19]\n"
    "v_mfma_f32_16x16x32_bf16 v[20:23], v[160:163], a[244:247], v[20:23]\n"
    "v_mfma_f32_16x16x32_bf16 v[24:27], v[160:163], a[248:251], v[24:27]\n"
    "v_mfma_f32_16x16x32_bf16 v[28:31], v[160:163], a[252:255], v[28:31]\n"
    "s_waitcnt vmcnt(5)\n"
    "v_mfma_f32_16x16x32_bf16 v[32:35], v[164:167], a[240:243], v[32:35]\n"
    "v_mfma_f32_16x16x32_bf16 v[36:39], v[164:167], a[244:247], v[36:39]\n"
    "v_mfma_f32_16x16x32_bf16 v[40:43], v[164:167], a[248:251], v[40:43]\n"
    "v_mfma_f32_16x16x32_bf16 v[44:47], v[164:167], a[252:255], v[44:47]\n"
    "s_waitcnt vmcnt(4)\n"
    "v_mfma_f32_16x16x32_bf16 v[48:51], v[168:171], a[240:243], v[48:51]\n"
    "v_mfma_f32_16x16x32_bf16 v[52:55], v[168:171], a[244:247], v[52:55]\n"
    "v_mfma_f32_16x16x32_bf16 v[56:59], v[168:171], a[248:251], v[56:59]\n"
    "v_mfma_f32_16x16x32_bf16 v[60:63], v[168:171], a[252:255], v[60:63]\n"
    "s_waitcnt vmcnt(3)\n"
    "v_mfma_f32_16x16x32_bf16 v[64:67], v[172:175], a[240:243], v[64:67]\n"
    "v_mfma_f32_16x16x32_bf16 v[68:71], v[172:175], a[244:247], v[68:71]\n"
    "v_mfma_f32_16x16x32_bf16 v[72:75], v[172:175], a[248:251], v[72:75]\n"
    "v_mfma_f32_16x16x32_bf16 v[76:79], v[172:175], a[252:255], v[76:79]\n"
    "s_waitcnt vmcnt(2)\n"
    "v_mfma_f32_16x16x32_bf16 v[80:83], v[176:179], a[240:243], v[80:83]\n"
    "v_mfma_f32_16x16x32_bf16 v[84:87], v[176:179], a[244:247], v[84:87]\n"
    "v_mfma_f32_16x16x32_bf16 v[88:91], v[176:179], a[248:251], v[88:91]\n"
    "v_mfma_f32_16x16x32_bf16 v[92:95], v[176:179], a[252:255], v[92:95]\n"
    "s_waitcnt vmcnt(1)\n"
    "v_mfma_f32_16x16x32_bf16 v[96:99], v[180:183], a[240:243], v[96:99]\n"
    "v_mfma_f32_16x16x32_bf16 v[100:103], v[180:183], a[244:247], v[100:103]\n"
    "v_mfma_f32_16x16x32_bf16 v[104:107], v[180:183], a[248:251], v[104:107]\n"
    "v_mfma_f32_16x16x32_bf16 v[108:111], v[180:183], a[252:255], v[108:111]\n"
    "s_waitcnt vmcnt(0)\n"
    "v_mfma_f32_16x16x32_bf16 v[112:115], v[184:187], a[240:243], v[112:115]\n"
    "v_mfma_f32_16x16x32_bf16 v[116:119], v[184:187], a[244:247], v[116:119]\n"
    "v_mfma_f32_16x16x32_bf16 v[120:123], v[184:187], a[248:251], v[120:123]\n"
    "v_mfma_f32_16x16x32_bf16 v[124:127], v[184:187], a[252:255], v[124:127]\n"
    "s_nop 7\n"
    "s_cmp_eq_u32 s52, 1\n"
    "s_cbranch_scc1 L_rs1_%=\n"
    "s_cmp_eq_u32 s52, 2\n"
    "s_cbranch_scc1 L_rs2_%=\n"
    "s_cmp_eq_u32 s52, 3\n"
    "s_cbranch_scc1 L_rs3_%=\n"
    "ds_write_b128 v4, v[20:23] offset:20480\n"
    "ds_write_b128 v4, v[36:39] offset:21504\n"
    "ds_write_b128 v4, v[52:55] offset:22528\n"
    "ds_write_b128 v4, v[68:71] offset:23552\n"
    "ds_write_b128 v4, v[24:27] offset:28672\n"
    "ds_write_b128 v4, v[40:43] offset:29696\n"
    "ds_write_b128 v4, v[56:59] offset:30720\n"
    "ds_write_b128 v4, v[72:75] offset:31744\n"
    "ds_write_b128 v4, v[28:31] offset:36864\n"
    "ds_write_b128 v4, v[44:47] offset:37888\n"
    "ds_write_b128 v4, v[60:63] offset:38912\n"
    "ds_write_b128 v4, v[76:79] offset:39936\n"
    "s_waitcnt lgkmcnt(0)\n"
    "s_barrier\n"
    "ds_read_b128 v[188:191], v4 offset:0\n"
    "ds_read_b128 v[192:195], v4 offset:4096\n"
    "ds_read_b128 v[196:199], v4 offset:8192\n"
    "ds_read_b128 v[200:203], v4 offset:1024\n"
    "ds_read_b128 v[204:207], v4 offset:5120\n"
    "ds_read_b128 v[208:211], v4 offset:9216\n"
    "ds_read_b128 v[212:215], v4 offset:2048\n"
    "ds_read_b128 v[216:219], v4 offset:6144\n"
    "ds_read_b128 v[220:223], v4 offset:10240\n"
    "ds_read_b128 v[224:227], v4 offset:3072\n"
    "ds_read_b128 v[228:231], v4 offset:7168\n"
    "ds_read_b128 v[232:235], v4 offset:11264\n"
    "s_waitcnt lgkmcnt(0)\n"
    "v_add_f32 v160, v16, v188\n"
    "s_waitcnt lgkmcnt(0)\n"
    "v_add_f32 v192, v192, v196\n"
    "v_add_f32 v161, v17, v189\n"
    "v_add_f32 v193, v193, v197\n"
    "v_add_f32 v162, v18, v190\n"
    "v_add_f32 v194, v194, v198\n"
    "v_add_f32 v163, v19, v191\n"
    "v_add_f32 v195, v195, v199\n"
    "v_add_f32 v160, v160, v192\n"
    "v_add_f32 v161, v161, v193\n"
    "v_add_f32 v162, v162, v194\n"
    "v_add_f32 v163, v163, v195\n"
    "s_waitcnt lgkmcnt(0)\n"
    "v_add_f32 v164, v32, v200\n"
    "s_waitcnt lgkmcnt(0)\n"
    "v_add_f32 v204, v204, v208\n"
    "v_add_f32 v165, v33, v201\n"
    "v_add_f32 v205, v205, v209\n"
    "v_add_f32 v166, v34, v202\n"
    "v_add_f32 v206, v206, v210\n"
    "v_add_f32 v167, v35, v203\n"
    "v_add_f32 v207, v207, v211\n"
    "v_add_f32 v164, v164, v204\n"
    "v_add_f32 v165, v165, v205\n"
    "v_add_f32 v166, v166, v206\n"
    "v_add_f32 v167, v167, v207\n"
    "s_waitcnt lgkmcnt(0)\n"
    "v_add_f32 v168, v48, v212\n"
    "s_waitcnt lgkmcnt(0)\n"
    "v_add_f32 v216, v216, v220\n"
    "v_add_f32 v169, v49, v213\n"
    "v_add_f32 v217, v217, v221\n"
    "v_add_f32 v170, v50, v214\n"
    "v_add_f32 v218, v218, v222\n"
    "v_add_f32 v171, v51, v215\n"
    "v_add_f32 v219, v219, v223\n"
    "v_add_f32 v168, v168, v216\n"
    "v_add_f32 v169, v169, v217\n"
    "v_add_f32 v170, v170, v218\n"
    "v_add_f32 v171, v171, v219\n"
    "s_waitcnt lgkmcnt(0)\n"
    "v_add_f32 v172, v64, v224\n"
    "s_waitcnt lgkmcnt(0)\n"
    "v_add_f32 v228, v228, v232\n"
    "v_add_f32 v173, v65, v225\n"
    "v_add_f32 v229, v229, v233\n"
    "v_add_f32 v174, v66, v226\n"
    "v_add_f32 v230, v230, v234\n"
    "v_add_f32 v175, v67, v227\n"
    "v_add_f32 v231, v231, v235\n"
    "v_add_f32 v172, v172, v228\n"
    "v_add_f32 v173, v173, v229\n"
    "v_add_f32 v174, v174, v230\n"
    "v_add_f32 v175, v175, v231\n"
    "s_waitcnt lgkmcnt(0)\n"
    "s_barrier\n"
    "ds_write_b128 v4, v[84:87] offset:15360\n"
    "ds_write_b128 v4, v[100:103] offset:16384\n"
    "ds_write_b128 v4, v[116:119] offset:17408\n"
    "ds_write_b128 v4, v[88:91] offset:21504\n"
    "ds_write_b128 v4, v[104:107] offset:22528\n"
    "ds_write_b128 v4, v[120:123] offset:23552\n"
    "ds_write_b128 v4, v[92:95] offset:27648\n"
    "ds_write_b128 v4, v[108:111] offset:28672\n"
    "ds_write_b128 v4, v[124:127] offset:29696\n"
    "s_waitcnt lgkmcnt(0)\n"
    "s_barrier\n"
    "ds_read_b128 v[188:191], v4 offset:0\n"
    "ds_read_b128 v[192:195], v4 offset:3072\n"
    "ds_read_b128 v[196:199], v4 offset:6144\n"
    "ds_read_b128 v[200:203], v4 offset:1024\n"
    "ds_read_b128 v[204:207], v4 offset:4096\n"
    "ds_read_b128 v[208:211], v4 offset:7168\n"
    "ds_read_b128 v[212:215], v4 offset:2048\n"
    "ds_read_b128 v[216:219], v4 offset:5120\n"
    "ds_read_b128 v[220:223], v4 offset:8192\n"
    "s_waitcnt lgkmcnt(0)\n"
    "v_add_f32 v176, v80, v188\n"
    "s_waitcnt lgkmcnt(0)\n"
    "v_add_f32 v192, v192, v196\n"
    "v_add_f32 v177, v81, v189\n"
    "v_add_f32 v193, v193, v197\n"
    "v_add_f32 v178, v82, v190\n"
    "v_add_f32 v194, v194, v198\n"
    "v_add_f32 v179, v83, v191\n"
    "v_add_f32 v195, v195, v199\n"
    "v_add_f32 v176, v176, v192\n"
    "v_add_f32 v177, v177, v193\n"
    "v_add_f32 v178, v178, v194\n"
    "v_add_f32 v179, v179, v195\n"
    "s_waitcnt lgkmcnt(0)\n"
    "v_add_f32 v180, v96, v200\n"
    "s_waitcnt lgkmcnt(0)\n"
    "v_add_f32 v204, v204, v208\n"
    "v_add_f32 v181, v97, v201\n"
    "v_add_f32 v205, v205, v209\n"
    "v_add_f32 v182, v98, v202\n"
    "v_add_f32 v206, v206, v210\n"
    "v_add_f32 v183, v99, v203\n"
    "v_add_f32 v207, v207, v211\n"
    "v_add_f32 v180, v180, v204\n"
    "v_add_f32 v181, v181, v205\n"
    "v_add_f32 v182, v182, v206\n"
    "v_add_f32 v183, v183, v207\n"
    "s_waitcnt lgkmcnt(0)\n"
    "v_add_f32 v184, v112, v212\n"
    "s_waitcnt lgkmcnt(0)\n"
    "v_add_f32 v216, v216, v220\n"
    "v_add_f32 v185, v113, v213\n"
    "v_add_f32 v217, v217, v221\n"
    "v_add_f32 v186, v114, v214\n"
    "v_add_f32 v218, v218, v222\n"
    "v_add_f32 v187, v115, v215\n"
    "v_add_f32 v219, v219, v223\n"
    "v_add_f32 v184, v184, v216\n"
    "v_add_f32 v185, v185, v217\n"
    "v_add_f32 v186, v186, v218\n"
    "v_add_f32 v187, v187, v219\n"
    "s_branch L_rs_done_%=\n"
    "L_rs1_%=:\n"
    "ds_write_b128 v4, v[16:19] offset:0\n"
    "ds_write_b128 v4, v[32:35] offset:1024\n"
    "ds_write_b128 v4, v[48:51] offset:2048\n"
    "ds_write_b128 v4, v[64:67] offset:3072\n"
    "ds_write_b128 v4, v[24:27] offset:32768\n"
    "ds_write_b128 v4, v[40:43] offset:33792\n"
    "ds_write_b128 v4, v[56:59] offset:34816\n"
    "ds_write_b128 v4, v[72:75] offset:35840\n"
    "ds_write_b128 v4, v[28:31] offset:40960\n"
    "ds_write_b128 v4, v[44:47] offset:41984\n"
    "ds_write_b128 v4, v[60:63] offset:43008\n"
    "ds_write_b128 v4, v[76:79] offset:44032\n"
    "s_waitcnt lgkmcnt(0)\n"
    "s_barrier\n"
    "ds_read_b128 v[188:191], v4 offset:12288\n"
    "ds_read_b128 v[192:195], v4 offset:16384\n"
    "ds_read_b128 v[196:199], v4 offset:20480\n"
    "ds_read_b128 v[200:203], v4 offset:13312\n"
    "ds_read_b128 v[204:207], v4 offset:17408\n"
    "ds_read_b128 v[208:211], v4 offset:21504\n"
    "ds_read_b128 v[212:215], v4 offset:14336\n"
    "ds_read_b128 v[216:219], v4 offset:18432\n"
    "ds_read_b128 v[220:223], v4 offset:22528\n"
    "ds_read_b128 v[224:227], v4 offset:15360\n"
    "ds_read_b128 v[228:231], v4 offset:19456\n"
    "ds_read_b128 v[232:235], v4 offset:23552\n"
    "s_waitcnt lgkmcnt(0)\n"
    "v_add_f32 v160, v20, v188\n"
    "s_waitcnt lgkmcnt(0)\n"
    "v_add_f32 v192, v192, v196\n"
    "v_add_f32 v161, v21, v189\n"
    "v_add_f32 v193, v193, v197\n"
    "v_add_f32 v162, v22, v190\n"
    "v_add_f32 v194, v194, v198\n"
    "v_add_f32 v163, v23, v191\n"
    "v_add_f32 v195, v195, v199\n"
    "v_add_f32 v160, v160, v192\n"
    "v_add_f32 v161, v161, v193\n"
    "v_add_f32 v162, v162, v194\n"
    "v_add_f32 v163, v163, v195\n"
    "s_waitcnt lgkmcnt(0)\n"
    "v_add_f32 v164, v36, v200\n"
    "s_waitcnt lgkmcnt(0)\n"
    "v_add_f32 v204, v204, v208\n"
    "v_add_f32 v165, v37, v201\n"
    "v_add_f32 v205, v205, v209\n"
    "v_add_f32 v166, v38, v202\n"
    "v_add_f32 v206, v206, v210\n"
    "v_add_f32 v167, v39, v203\n"
    "v_add_f32 v207, v207, v211\n"
    "v_add_f32 v164, v164, v204\n"
    "v_add_f32 v165, v165, v205\n"
    "v_add_f32 v166, v166, v206\n"
    "v_add_f32 v167, v167, v207\n"
    "s_waitcnt lgkmcnt(0)\n"
    "v_add_f32 v168, v52, v212\n"
    "s_waitcnt lgkmcnt(0)\n"
    "v_add_f32 v216, v216, v220\n"
    "v_add_f32 v169, v53, v213\n"
    "v_add_f32 v217, v217, v221\n"
    "v_add_f32 v170, v54, v214\n"
    "v_add_f32 v218, v218, v222\n"
    "v_add_f32 v171, v55, v215\n"
    "v_add_f32 v219, v219, v223\n"
    "v_add_f32 v168, v168, v216\n"
    "v_add_f32 v169, v169, v217\n"
    "v_add_f32 v170, v170, v218\n"
    "v_add_f32 v171, v171, v219\n"
    "s_waitcnt lgkmcnt(0)\n"
    "v_add_f32 v172, v68, v224\n"
    "s_waitcnt lgkmcnt(0)\n"
    "v_add_f32 v228, v228, v232\n"
    "v_add_f32 v173, v69, v225\n"
    "v_add_f32 v229, v229, v233\n"
    "v_add_f32 v174, v70, v226\n"
    "v_add_f32 v230, v230, v234\n"
    "v_add_f32 v175, v71, v227\n"
    "v_add_f32 v231, v231, v235\n"
    "v_add_f32 v172, v172, v228\n"
    "v_add_f32 v173, v173, v229\n"
    "v_add_f32 v174, v174, v230\n"
    "v_add_f32 v175, v175, v231\n"
    "s_waitcnt lgkmcnt(0)\n"
    "s_barrier\n"
    "ds_write_b128 v4, v[80:83] offset:0\n"
    "ds_write_b128 v4, v[96:99] offset:1024\n"
    "ds_write_b128 v4, v[112:115] offset:2048\n"
    "ds_write_b128 v4, v[88:91] offset:24576\n"
    "ds_write_b128 v4, v[104:107] offset:25600\n"
    "ds_write_b128 v4, v[120:123] offset:26624\n"
    "ds_write_b128 v4, v[92:95] offset:30720\n"
    "ds_write_b128 v4, v[108:111] offset:31744\n"
    "ds_write_b128 v4, v[124:127] offset:32768\n"
    "s_waitcnt lgkmcnt(0)\n"
    "s_barrier\n"
    "ds_read_b128 v[188:191], v4 offset:9216\n"
    "ds_read_b128 v[192:195], v4 offset:12288\n"
    "ds_read_b128 v[196:199], v4 offset:15360\n"
    "ds_read_b128 v[200:203], v4 offset:10240\n"
    "ds_read_b128 v[204:207], v4 offset:13312\n"
    "ds_read_b128 v[208:211], v4 offset:16384\n"
    "ds_read_b128 v[212:215], v4 offset:11264\n"
    "ds_read_b128 v[216:219], v4 offset:14336\n"
    "ds_read_b128 v[220:223], v4 offset:17408\n"
    "s_waitcnt lgkmcnt(0)\n"
    "v_add_f32 v176, v84, v188\n"
    "s_waitcnt lgkmcnt(0)\n"
    "v_add_f32 v192, v192, v196\n"
    "v_add_f32 v177, v85, v189\n"
    "v_add_f32 v193, v193, v197\n"
    "v_add_f32 v178, v86, v190\n"
    "v_add_f32 v194, v194, v198\n"
    "v_add_f32 v179, v87, v191\n"
    "v_add_f32 v195, v195, v199\n"
    "v_add_f32 v176, v176, v192\n"
    "v_add_f32 v177, v177, v193\n"
    "v_add_f32 v178, v178, v194\n"
    "v_add_f32 v179, v179, v195\n"
    "s_waitcnt lgkmcnt(0)\n"
    "v_add_f32 v180, v100, v200\n"
    "s_waitcnt lgkmcnt(0)\n"
    "v_add_f32 v204, v204, v208\n"
    "v_add_f32 v181, v101, v201\n"
    "v_add_f32 v205, v205, v209\n"
    "v_add_f32 v182, v102, v202\n"
    "v_add_f32 v206, v206, v210\n"
    "v_add_f32 v183, v103, v203\n"
    "v_add_f32 v207, v207, v211\n"
    "v_add_f32 v180, v180, v204\n"
    "v_add_f32 v181, v181, v205\n"
    "v_add_f32 v182, v182, v206\n"
    "v_add_f32 v183, v183, v207\n"
    "s_waitcnt lgkmcnt(0)\n"
    "v_add_f32 v184, v116, v212\n"
    "s_waitcnt lgkmcnt(0)\n"
    "v_add_f32 v216, v216, v220\n"
    "v_add_f32 v185, v117, v213\n"
    "v_add_f32 v217, v217, v221\n"
    "v_add_f32 v186, v118, v214\n"
    "v_add_f32 v218, v218, v222\n"
    "v_add_f32 v187, v119, v215\n"
    "v_add_f32 v219, v219, v223\n"
    "v_add_f32 v184, v184, v216\n"
    "v_add_f32 v185, v185, v217\n"
    "v_add_f32 v186, v186, v218\n"
    "v_add_f32 v187, v187, v219\n"
    "s_branch L_rs_done_%=\n"
    "L_rs2_%=:\n"
    "ds_write_b128 v4, v[16:19] offset:4096\n"
    "ds_write_b128 v4, v[32:35] offset:5120\n"
    "ds_write_b128 v4, v[48:51] offset:6144\n"
    "ds_write_b128 v4, v[64:67] offset:7168\n"
    "ds_write_b128 v4, v[20:23] offset:12288\n"
    "ds_write_b128 v4, v[36:39] offset:13312\n"
    "ds_write_b128 v4, v[52:55] offset:14336\n"
    "ds_write_b128 v4, v[68:71] offset:15360\n"
    "ds_write_b128 v4, v[28:31] offset:45056\n"
    "ds_write_b128 v4, v[44:47] offset:46080\n"
    "ds_write_b128 v4, v[60:63] offset:47104\n"
    "ds_write_b128 v4, v[76:79] offset:48128\n"
    "s_waitcnt lgkmcnt(0)\n"
    "s_barrier\n"
    "ds_read_b128 v[188:191], v4 offset:24576\n"
    "ds_read_b128 v[192:195], v4 offset:28672\n"
    "ds_read_b128 v[196:199], v4 offset:32768\n"
    "ds_read_b128 v[200:203], v4 offset:25600\n"
    "ds_read_b128 v[204:207], v4 offset:29696\n"
    "ds_read_b128 v[208:211], v4 offset:33792\n"
    "ds_read_b128 v[212:215], v4 offset:26624\n"
    "ds_read_b128 v[216:219], v4 offset:30720\n"
    "ds_read_b128 v[220:223], v4 offset:34816\n"
    "ds_read_b128 v[224:227], v4 offset:27648\n"
    "ds_read_b128 v[228:231], v4 offset:31744\n"
    "ds_read_b128 v[232:235], v4 offset:35840\n"
    "s_waitcnt lgkmcnt(0)\n"
    "v_add_f32 v160, v24, v188\n"
    "s_waitcnt lgkmcnt(0)\n"
    "v_add_f32 v192, v192, v196\n"
    "v_add_f32 v161, v25, v189\n"
    "v_add_f32 v193, v193, v197\n"
    "v_add_f32 v162, v26, v190\n"
    "v_add_f32 v194, v194, v198\n"
    "v_add_f32 v163, v27, v191\n"
    "v_add_f32 v195, v195, v199\n"
    "v_add_f32 v160, v160, v192\n"
    "v_add_f32 v161, v161, v193\n"
    "v_add_f32 v162, v162, v194\n"
    "v_add_f32 v163, v163, v195\n"
    "s_waitcnt lgkmcnt(0)\n"
    "v_add_f32 v164, v40, v200\n"
    "s_waitcnt lgkmcnt(0)\n"
    "v_add_f32 v204, v204, v208\n"
    "v_add_f32 v165, v41, v201\n"
    "v_add_f32 v205, v205, v209\n"
    "v_add_f32 v166, v42, v202\n"
    "v_add_f32 v206, v206, v210\n"
    "v_add_f32 v167, v43, v203\n"
    "v_add_f32 v207, v207, v211\n"
    "v_add_f32 v164, v164, v204\n"
    "v_add_f32 v165, v165, v205\n"
    "v_add_f32 v166, v166, v206\n"
    "v_add_f32 v167, v167, v207\n"
    "s_waitcnt lgkmcnt(0)\n"
    "v_add_f32 v168, v56, v212\n"
    "s_waitcnt lgkmcnt(0)\n"
    "v_add_f32 v216, v216, v220\n"
    "v_add_f32 v169, v57, v213\n"
    "v_add_f32 v217, v217, v221\n"
    "v_add_f32 v170, v58, v214\n"
    "v_add_f32 v218, v218, v222\n"
    "v_add_f32 v171, v59, v215\n"
    "v_add_f32 v219, v219, v223\n"
    "v_add_f32 v168, v168, v216\n"
    "v_add_f32 v169, v169, v217\n"
    "v_add_f32 v170, v170, v218\n"
    "v_add_f32 v171, v171, v219\n"
    "s_waitcnt lgkmcnt(0)\n"
    "v_add_f32 v172, v72, v224\n"
    "s_waitcnt lgkmcnt(0)\n"
    "v_add_f32 v228, v228, v232\n"
    "v_add_f32 v173, v73, v225\n"
    "v_add_f32 v229, v229, v233\n"
    "v_add_f32 v174, v74, v226\n"
    "v_add_f32 v230, v230, v234\n"
    "v_add_f32 v175, v75, v227\n"
    "v_add_f32 v231, v231, v235\n"
    "v_add_f32 v172, v172, v228\n"
    "v_add_f32 v173, v173, v229\n"
    "v_add_f32 v174, v174, v230\n"
    "v_add_f32 v175, v175, v231\n"
    "s_waitcnt lgkmcnt(0)\n"
    "s_barrier\n"
    "ds_write_b128 v4, v[80:83] offset:3072\n"
    "ds_write_b128 v4, v[96:99] offset:4096\n"
    "ds_write_b128 v4, v[112:115] offset:5120\n"
    "ds_write_b128 v4, v[84:87] offset:9216\n"
    "ds_write_b128 v4, v[100:103] offset:10240\n"
    "ds_write_b128 v4, v[116:119] offset:11264\n"
    "ds_write_b128 v4, v[92:95] offset:33792\n"
    "ds_write_b128 v4, v[108:111] offset:34816\n"
    "ds_write_b128 v4, v[124:127] offset:35840\n"
    "s_waitcnt lgkmcnt(0)\n"
    "s_barrier\n"
    "ds_read_b128 v[188:191], v4 offset:18432\n"
    "ds_read_b128 v[192:195], v4 offset:21504\n"
    "ds_read_b128 v[196:199], v4 offset:24576\n"
    "ds_read_b128 v[200:203], v4 offset:19456\n"
    "ds_read_b128 v[204:207], v4 offset:22528\n"
    "ds_read_b128 v[208:211], v4 offset:25600\n"
    "ds_read_b128 v[212:215], v4 offset:20480\n"
    "ds_read_b128 v[216:219], v4 offset:23552\n"
    "ds_read_b128 v[220:223], v4 offset:26624\n"
    "s_waitcnt lgkmcnt(0)\n"
    "v_add_f32 v176, v88, v188\n"
    "s_waitcnt lgkmcnt(0)\n"
    "v_add_f32 v192, v192, v196\n"
    "v_add_f32 v177, v89, v189\n"
    "v_add_f32 v193, v193, v197\n"
    "v_add_f32 v178, v90, v190\n"
    "v_add_f32 v194, v194, v198\n"
    "v_add_f32 v179, v91, v191\n"
    "v_add_f32 v195, v195, v199\n"
    "v_add_f32 v176, v176, v192\n"
    "v_add_f32 v177, v177, v193\n"
    "v_add_f32 v178, v178, v194\n"
    "v_add_f32 v179, v179, v195\n"
    "s_waitcnt lgkmcnt(0)\n"
    "v_add_f32 v180, v104, v200\n"
    "s_waitcnt lgkmcnt(0)\n"
    "v_add_f32 v204, v204, v208\n"
    "v_add_f32 v181, v105, v201\n"
    "v_add_f32 v205, v205, v209\n"
    "v_add_f32 v182, v106, v202\n"
    "v_add_f32 v206, v206, v210\n"
    "v_add_f32 v183, v107, v203\n"
    "v_add_f32 v207, v207, v211\n"
    "v_add_f32 v180, v180, v204\n"
    "v_add_f32 v181, v181, v205\n"
    "v_add_f32 v182, v182, v206\n"
    "v_add_f32 v183, v183, v207\n"
    "s_waitcnt lgkmcnt(0)\n"
    "v_add_f32 v184, v120, v212\n"
    "s_waitcnt lgkmcnt(0)\n"
    "v_add_f32 v216, v216, v220\n"
    "v_add_f32 v185, v121, v213\n"
    "v_add_f32 v217, v217, v221\n"
    "v_add_f32 v186, v122, v214\n"
    "v_add_f32 v218, v218, v222\n"
    "v_add_f32 v187, v123, v215\n"
    "v_add_f32 v219, v219, v223\n"
    "v_add_f32 v184, v184, v216\n"
    "v_add_f32 v185, v185, v217\n"
    "v_add_f32 v186, v186, v218\n"
    "v_add_f32 v187, v187, v219\n"
    "s_branch L_rs_done_%=\n"
    "L_rs3_%=:\n"
    "ds_write_b128 v4, v[16:19] offset:8192\n"
    "ds_write_b128 v4, v[32:35] offset:9216\n"
    "ds_write_b128 v4, v[48:51] offset:10240\n"
    "ds_write_b128 v4, v[64:67] offset:11264\n"
    "ds_write_b128 v4, v[20:23] offset:16384\n"
    "ds_write_b128 v4, v[36:39] offset:17408\n"
    "ds_write_b128 v4, v[52:55] offset:18432\n"
    "ds_write_b128 v4, v[68:71] offset:19456\n"
    "ds_write_b128 v4, v[24:27] offset:24576\n"
    "ds_write_b128 v4, v[40:43] offset:25600\n"
    "ds_write_b128 v4, v[56:59] offset:26624\n"
    "ds_write_b128 v4, v[72:75] offset:27648\n"
    "s_waitcnt lgkmcnt(0)\n"
    "s_barrier\n"
    "ds_read_b128 v[188:191], v4 offset:36864\n"
    "ds_read_b128 v[192:195], v4 offset:40960\n"
    "ds_read_b128 v[196:199], v4 offset:45056\n"
    "ds_read_b128 v[200:203], v4 offset:37888\n"
    "ds_read_b128 v[204:207], v4 offset:41984\n"
    "ds_read_b128 v[208:211], v4 offset:46080\n"
    "ds_read_b128 v[212:215], v4 offset:38912\n"
    "ds_read_b128 v[216:219], v4 offset:43008\n"
    "ds_read_b128 v[220:223], v4 offset:47104\n"
    "ds_read_b128 v[224:227], v4 offset:39936\n"
    "ds_read_b128 v[228:231], v4 offset:44032\n"
    "ds_read_b128 v[232:235], v4 offset:48128\n"
    "s_waitcnt lgkmcnt(11)\n"
    "v_add_f32 v160, v28, v188\n"
    "s_waitcnt lgkmcnt(9)\n"
    "v_add_f32 v192, v192, v196\n"
    "v_add_f32 v161, v29, v189\n"
    "v_add_f32 v193, v193, v197\n"
    "v_add_f32 v162, v30, v190\n"
    "v_add_f32 v194, v194, v198\n"
    "v_add_f32 v163, v31, v191\n"
    "v_add_f32 v195, v195, v199\n"
    "v_add_f32 v160, v160, v192\n"
    "v_add_f32 v161, v161, v193\n"
    "v_add_f32 v162, v162, v194\n"
    "v_add_f32 v163, v163, v195\n"
    "s_waitcnt lgkmcnt(8)\n"
    "v_add_f32 v164, v44, v200\n"
    "s_waitcnt lgkmcnt(6)\n"
    "v_add_f32 v204, v204, v208\n"
    "v_add_f32 v165, v45, v201\n"
    "v_add_f32 v205, v205, v209\n"
    "v_add_f32 v166, v46, v202\n"
    "v_add_f32 v206, v206, v210\n"
    "v_add_f32 v167, v47, v203\n"
    "v_add_f32 v207, v207, v211\n"
    "v_add_f32 v164, v164, v204\n"
    "v_add_f32 v165, v165, v205\n"
    "v_add_f32 v166, v166, v206\n"
    "v_add_f32 v167, v167, v207\n"
    "s_waitcnt lgkmcnt(5)\n"
    "v_add_f32 v168, v60, v212\n"
    "s_waitcnt lgkmcnt(3)\n"
    "v_add_f32 v216, v216, v220\n"
    "v_add_f32 v169, v61, v213\n"
    "v_add_f32 v217, v217, v221\n"
    "v_add_f32 v170, v62, v214\n"
    "v_add_f32 v218, v218, v222\n"
    "v_add_f32 v171, v63, v215\n"
    "v_add_f32 v219, v219, v223\n"
    "v_add_f32 v168, v168, v216\n"
    "v_add_f32 v169, v169, v217\n"
    "v_add_f32 v170, v170, v218\n"
    "v_add_f32 v171, v171, v219\n"
    "s_waitcnt lgkmcnt(2)\n"
    "v_add_f32 v172, v76, v224\n"
    "s_waitcnt lgkmcnt(0)\n"
    "v_add_f32 v228, v228, v232\n"
    "v_add_f32 v173, v77, v225\n"
    "v_add_f32 v229, v229, v233\n"
    "v_add_f32 v174, v78, v226\n"
    "v_add_f32 v230, v230, v234\n"
    "v_add_f32 v175, v79, v227\n"
    "v_add_f32 v231, v231, v235\n"
    "v_add_f32 v172, v172, v228\n"
    "v_add_f32 v173, v173, v229\n"
    "v_add_f32 v174, v174, v230\n"
    "v_add_f32 v175, v175, v231\n"
    "s_waitcnt lgkmcnt(0)\n"
    "s_barrier\n"
    "ds_write_b128 v4, v[80:83] offset:6144\n"
    "ds_write_b128 v4, v[96:99] offset:7168\n"
    "ds_write_b128 v4, v[112:115] offset:8192\n"
    "ds_write_b128 v4, v[84:87] offset:12288\n"
    "ds_write_b128 v4, v[100:103] offset:13312\n"
    "ds_write_b128 v4, v[116:119] offset:14336\n"
    "ds_write_b128 v4, v[88:91] offset:18432\n"
    "ds_write_b128 v4, v[104:107] offset:19456\n"
    "ds_write_b128 v4, v[120:123] offset:20480\n"
    "s_waitcnt lgkmcnt(0)\n"
    "s_barrier\n"
    "ds_read_b128 v[188:191], v4 offset:27648\n"
    "ds_read_b128 v[192:195], v4 offset:30720\n"
    "ds_read_b128 v[196:199], v4 offset:33792\n"
    "ds_read_b128 v[200:203], v4 offset:28672\n"
    "ds_read_b128 v[204:207], v4 offset:31744\n"
    "ds_read_b128 v[208:211], v4 offset:34816\n"
    "ds_read_b128 v[212:215], v4 offset:29696\n"
    "ds_read_b128 v[216:219], v4 offset:32768\n"
    "ds_read_b128 v[220:223], v4 offset:35840\n"
    "s_waitcnt lgkmcnt(8)\n"
    "v_add_f32 v176, v92, v188\n"
    "s_waitcnt lgkmcnt(6)\n"
    "v_add_f32 v192, v192, v196\n"
    "v_add_f32 v177, v93, v189\n"
    "v_add_f32 v193, v193, v197\n"
    "v_add_f32 v178, v94, v190\n"
    "v_add_f32 v194, v194, v198\n"
    "v_add_f32 v179, v95, v191\n"
    "v_add_f32 v195, v195, v199\n"
    "v_add_f32 v176, v176, v192\n"
    "v_add_f32 v177, v177, v193\n"
    "v_add_f32 v178, v178, v194\n"
    "v_add_f32 v179, v179, v195\n"
    "s_waitcnt lgkmcnt(5)\n"
    "v_add_f32 v180, v108, v200\n"
    "s_waitcnt lgkmcnt(3)\n"
    "v_add_f32 v204, v204, v208\n"
    "v_add_f32 v181, v109, v201\n"
    "v_add_f32 v205, v205, v209\n"
    "v_add_f32 v182, v110, v202\n"
    "v_add_f32 v206, v206, v210\n"
    "v_add_f32 v183, v111, v203\n"
    "v_add_f32 v207, v207, v211\n"
    "v_add_f32 v180, v180, v204\n"
    "v_add_f32 v181, v181, v205\n"
    "v_add_f32 v182, v182, v206\n"
    "v_add_f32 v183, v183, v207\n"
    "s_waitcnt lgkmcnt(2)\n"
    "v_add_f32 v184, v124, v212\n"
    "s_waitcnt lgkmcnt(0)\n"
    "v_add_f32 v216, v216, v220\n"
    "v_add_f32 v185, v125, v213\n"
    "v_add_f32 v217, v217, v221\n"
    "v_add_f32 v186, v126, v214\n"
    "v_add_f32 v218, v218, v222\n"
    "v_add_f32 v187, v127, v215\n"
    "v_add_f32 v219, v219, v223\n"
    "v_add_f32 v184, v184, v216\n"
    "v_add_f32 v185, v185, v217\n"
    "v_add_f32 v186, v186, v218\n"
    "v_add_f32 v187, v187, v219\n"
    "L_rs_done_%=:\n"
    "s_mul_i32 s59, s54, 3584\n"
    "s_add_u32 s59, s59, s65\n"
    "v_lshrrev_b32 v8, 1, v3\n"
    "v_lshlrev_b32 v8, 8, v8\n"
    "v_lshl_add_u32 v8, v2, 4, v8\n"
    "v_and_b32 v9, 1, v3\n"
    "v_lshl_add_u32 v8, v9, 3, v8\n"
    "v_lshlrev_b32 v254, 16, v254\n"
    "v_add_f32 v160, v160, v254\n"
    "v_add_f32 v161, v161, v254\n"
    "v_add_f32 v162, v162, v254\n"
    "v_add_f32 v163, v163, v254\n"
    "v_mul_f32 v160, v160, v132\n"
    "v_mul_f32 v161, v161, v133\n"
    "v_mul_f32 v162, v162, v134\n"
    "v_mul_f32 v163, v163, v135\n"
    "v_cvt_pk_bf16_f32 v160, v160, v161\n"
    "v_cvt_pk_bf16_f32 v161, v162, v163\n"
    "buffer_store_dwordx2 v[160:161], v8, s[40:43], s59 offen offset:0 sc1\n"
    "v_add_f32 v164, v164, v254\n"
    "v_add_f32 v165, v165, v254\n"
    "v_add_f32 v166, v166, v254\n"
    "v_add_f32 v167, v167, v254\n"
    "v_mul_f32 v164, v164, v136\n"
    "v_mul_f32 v165, v165, v137\n"
    "v_mul_f32 v166, v166, v138\n"
    "v_mul_f32 v167, v167, v139\n"
    "v_cvt_pk_bf16_f32 v164, v164, v165\n"
    "v_cvt_pk_bf16_f32 v165, v166, v167\n"
    "buffer_store_dwordx2 v[164:165], v8, s[40:43], s59 offen offset:512 sc1\n"
    "v_add_f32 v168, v168, v254\n"
    "v_add_f32 v169, v169, v254\n"
    "v_add_f32 v170, v170, v254\n"
    "v_add_f32 v171, v171, v254\n"
    "v_mul_f32 v168, v168, v140\n"
    "v_mul_f32 v169, v169, v141\n"
    "v_mul_f32 v170, v170, v142\n"
    "v_mul_f32 v171, v171, v143\n"
    "v_cvt_pk_bf16_f32 v168, v168, v169\n"
    "v_cvt_pk_bf16_f32 v169, v170, v171\n"
    "buffer_store_dwordx2 v[168:169], v8, s[40:43], s59 offen offset:1024 sc1\n"
    "v_add_f32 v172, v172, v254\n"
    "v_add_f32 v173, v173, v254\n"
    "v_add_f32 v174, v174, v254\n"
    "v_add_f32 v175, v175, v254\n"
    "v_mul_f32 v172, v172, v144\n"
    "v_mul_f32 v173, v173, v145\n"
    "v_mul_f32 v174, v174, v146\n"
    "v_mul_f32 v175, v175, v147\n"
    "v_cvt_pk_bf16_f32 v172, v172, v173\n"
    "v_cvt_pk_bf16_f32 v173, v174, v175\n"
    "buffer_store_dwordx2 v[172:173], v8, s[40:43], s59 offen offset:1536 sc1\n"
    "v_add_f32 v176, v176, v254\n"
    "v_add_f32 v177, v177, v254\n"
    "v_add_f32 v178, v178, v254\n"
    "v_add_f32 v179, v179, v254\n"
    "v_mul_f32 v176, v176, v148\n"
    "v_mul_f32 v177, v177, v149\n"
    "v_mul_f32 v178, v178, v150\n"
    "v_mul_f32 v179, v179, v151\n"
    "v_cvt_pk_bf16_f32 v176, v176, v177\n"
    "v_cvt_pk_bf16_f32 v177, v178, v179\n"
    "buffer_store_dwordx2 v[176:177], v8, s[40:43], s59 offen offset:2048 sc1\n"
    "v_add_f32 v180, v180, v254\n"
    "v_add_f32 v181, v181, v254\n"
    "v_add_f32 v182, v182, v254\n"
    "v_add_f32 v183, v183, v254\n"
    "v_mul_f32 v180, v180, v152\n"
    "v_mul_f32 v181, v181, v153\n"
    "v_mul_f32 v182, v182, v154\n"
    "v_mul_f32 v183, v183, v155\n"
    "v_cvt_pk_bf16_f32 v180, v180, v181\n"
    "v_cvt_pk_bf16_f32 v181, v182, v183\n"
    "buffer_store_dwordx2 v[180:181], v8, s[40:43], s59 offen offset:2560 sc1\n"
    "v_add_f32 v184, v184, v254\n"
    "v_add_f32 v185, v185, v254\n"
    "v_add_f32 v186, v186, v254\n"
    "v_add_f32 v187, v187, v254\n"
    "v_mul_f32 v184, v184, v156\n"
    "v_mul_f32 v185, v185, v157\n"
    "v_mul_f32 v186, v186, v158\n"
    "v_mul_f32 v187, v187, v159\n"
    "v_cvt_pk_bf16_f32 v184, v184, v185\n"
    "v_cvt_pk_bf16_f32 v185, v186, v187\n"
    "buffer_store_dwordx2 v[184:185], v8, s[40:43], s59 offen offset:3072 sc1\n"
    "s_cmp_eq_u32 s54, 7\n"
    "s_cbranch_scc1 L_tend_%=\n"
    "v_lshlrev_b32 v9, 4, v3\n"
    "s_lshl_b32 s60, s54, 8\n"
    "s_add_u32 s60, s60, 0x3300\n"
    "buffer_load_dwordx4 v[176:179], v9, s[40:43], s60 offen offset:0\n"
    "buffer_load_dwordx4 v[180:183], v9, s[40:43], s60 offen offset:64\n"
    "buffer_load_dwordx4 v[184:187], v9, s[40:43], s60 offen offset:128\n"
    "buffer_load_dwordx4 v[188:191], v9, s[40:43], s60 offen offset:192\n"
    "s_sleep 18\n"
    "s_mov_b32 s62, 0\n"
    "L_u_%=:\n"
    "buffer_load_dwordx4 v[16:19], v0, s[40:43], s65 offen offset:0 sc1\n"
    "buffer_load_dwordx4 v[20:23], v0, s[40:43], s65 offen offset:1024 sc1\n"
    "buffer_load_dwordx4 v[24:27], v0, s[40:43], s65 offen offset:2048 sc1\n"
    "buffer_load_dwordx4 v[28:31], v0, s[40:43], s65 offen offset:3072 sc1\n"
    "s_add_u32 s70, s65, 0x1000\n"
    "buffer_load_dwordx4 v[32:35], v0, s[40:43], s70 offen offset:0 sc1\n"
    "buffer_load_dwordx4 v[36:39], v0, s[40:43], s70 offen offset:1024 sc1\n"
    "buffer_load_dwordx4 v[40:43], v0, s[40:43], s70 offen offset:2048 sc1\n"
    "buffer_load_dwordx4 v[44:47], v0, s[40:43], s70 offen offset:3072 sc1\n"
    "s_add_u32 s70, s65, 0x2000\n"
    "buffer_load_dwordx4 v[48:51], v0, s[40:43], s70 offen offset:0 sc1\n"
    "buffer_load_dwordx4 v[52:55], v0, s[40:43], s70 offen offset:1024 sc1\n"
    "buffer_load_dwordx4 v[56:59], v0, s[40:43], s70 offen offset:2048 sc1\n"
    "buffer_load_dwordx4 v[60:63], v0, s[40:43], s70 offen offset:3072 sc1\n"
    "s_add_u32 s70, s65, 0x3000\n"
    "buffer_load_dwordx4 v[64:67], v0, s[40:43], s70 offen offset:0 sc1\n"
    "buffer_load_dwordx4 v[68:71], v0, s[40:43], s70 offen offset:1024 sc1\n"
    "buffer_load_dwordx4 v[72:75], v0, s[40:43], s70 offen offset:2048 sc1\n"
    "buffer_load_dwordx4 v[76:79], v0, s[40:43], s70 offen offset:3072 sc1\n"
    "s_add_u32 s70, s65, 0x4000\n"
    "buffer_load_dwordx4 v[80:83], v0, s[40:43], s70 offen offset:0 sc1\n"
    "buffer_load_dwordx4 v[84:87], v0, s[40:43], s70 offen offset:1024 sc1\n"
    "buffer_load_dwordx4 v[88:91], v0, s[40:43], s70 offen offset:2048 sc1\n"
    "buffer_load_dwordx4 v[92:95], v0, s[40:43], s70 offen offset:3072 sc1\n"
    "s_add_u32 s70, s65, 0x5000\n"
    "buffer_load_dwordx4 v[96:99], v0, s[40:43], s70 offen offset:0 sc1\n"
    "buffer_load_dwordx4 v[100:103], v0, s[40:43], s70 offen offset:1024 sc1\n"
    "buffer_load_dwordx4 v[104:107], v0, s[40:43], s70 offen offset:2048 sc1\n"
    "buffer_load_dwordx4 v[108:111], v0, s[40:43], s70 offen offset:3072 sc1\n"
    "s_add_u32 s70, s65, 0x6000\n"
    "buffer_load_dwordx4 v[112:115], v0, s[40:43], s70 offen offset:0 sc1\n"
    "buffer_load_dwordx4 v[116:119], v0, s[40:43], s70 offen offset:1024 sc1\n"
    "buffer_load_dwordx4 v[120:123], v0, s[40:43], s70 offen offset:2048 sc1\n"
    "buffer_load_dwordx4 v[124:127], v0, s[40:43], s70 offen offset:3072 sc1\n"
    "s_waitcnt vmcnt(0)\n"
    "v_max_u32 v7, v16, v17\n"
    "v_max3_u32 v7, v18, v19, v7\n"
    "v_max3_u32 v7, v20, v21, v7\n"
    "v_max3_u32 v7, v22, v23, v7\n"
    "v_max3_u32 v7, v24, v25, v7\n"
    "v_max3_u32 v7, v26, v27, v7\n"
    "v_max3_u32 v7, v28, v29, v7\n"
    "v_max3_u32 v7, v30, v31, v7\n"
    "v_max3_u32 v7, v32, v33, v7\n"
    "v_max3_u32 v7, v34, v35, v7\n"
    "v_max3_u32 v7, v36, v37, v7\n"
    "v_max3_u32 v7, v38, v39, v7\n"
    "v_max3_u32 v7, v40, v41, v7\n"
    "v_max3_u32 v7, v42, v43, v7\n"
    "v_max3_u32 v7, v44, v45, v7\n"
    "v_max3_u32 v7, v46, v47, v7\n"
    "v_max3_u32 v7, v48, v49, v7\n"
    "v_max3_u32 v7, v50, v51, v7\n"
    "v_max3_u32 v7, v52, v53, v7\n"
    "v_max3_u32 v7, v54, v55, v7\n"
    "v_max3_u32 v7, v56, v57, v7\n"
    "v_max3_u32 v7, v58, v59, v7\n"
    "v_max3_u32 v7, v60, v61, v7\n"
    "v_max3_u32 v7, v62, v63, v7\n"
    "v_max3_u32 v7, v64, v65, v7\n"
    "v_max3_u32 v7, v66, v67, v7\n"
    "v_max3_u32 v7, v68, v69, v7\n"
    "v_max3_u32 v7, v70, v71, v7\n"
    "v_max3_u32 v7, v72, v73, v7\n"
    "v_max3_u32 v7, v74, v75, v7\n"
    "v_max3_u32 v7, v76, v77, v7\n"
    "v_max3_u32 v7, v78, v79, v7\n"
    "v_max3_u32 v7, v80, v81, v7\n"
    "v_max3_u32 v7, v82, v83, v7\n"
    "v_max3_u32 v7, v84, v85, v7\n"
    "v_max3_u32 v7, v86, v87, v7\n"
    "v_max3_u32 v7, v88, v89, v7\n"
    "v_max3_u32 v7, v90, v91, v7\n"
    "v_max3_u32 v7, v92, v93, v7\n"
    "v_max3_u32 v7, v94, v95, v7\n"
    "v_max3_u32 v7, v96, v97, v7\n"
    "v_max3_u32 v7, v98, v99, v7\n"
    "v_max3_u32 v7, v100, v101, v7\n"
    "v_max3_u32 v7, v102, v103, v7\n"
    "v_max3_u32 v7, v104, v105, v7\n"
    "v_max3_u32 v7, v106, v107, v7\n"
    "v_max3_u32 v7, v108, v109, v7\n"
    "v_max3_u32 v7, v110, v111, v7\n"
    "v_max3_u32 v7, v112, v113, v7\n"
    "v_max3_u32 v7, v114, v115, v7\n"
    "v_max3_u32 v7, v116, v117, v7\n"
    "v_max3_u32 v7, v118, v119, v7\n"
    "v_max3_u32 v7, v120, v121, v7\n"
    "v_max3_u32 v7, v122, v123, v7\n"
    "v_max3_u32 v7, v124, v125, v7\n"
    "v_max3_u32 v7, v126, v127, v7\n"
    "v_cmp_eq_u32 vcc, -1, v7\n"
    "s_add_u32 s62, s62, 1\n"
    "s_cmp_gt_u32 s62, 0x40000\n"
    "s_cbranch_scc1 L_u_done_%=\n"
    "s_cbranch_vccz L_u_done_%=\n"
    "s_sleep 1\n"
    "s_branch L_u_%=\n"
    "L_u_done_%=:\n"
    "v_add_u32 v11, 0x20000, v4\n"
    "ds_read_b128 v[196:199], v4 offset:49152\n"
    "ds_read_b128 v[200:203], v4 offset:50176\n"
    "ds_read_b128 v[204:207], v4 offset:51200\n"
    "ds_read_b128 v[208:211], v4 offset:52224\n"
    "ds_read_b128 v[212:215], v4 offset:53248\n"
    "ds_read_b128 v[216:219], v4 offset:54272\n"
    "s_waitcnt lgkmcnt(5)\n"
    "v_mfma_f32_16x16x32_bf16 v[160:163], v[196:199], v[16:19], 0\n"
    "ds_read_b128 v[220:223], v4 offset:55296\n"
    "s_waitcnt lgkmcnt(5)\n"
    "v_mfma_f32_16x16x32_bf16 v[164:167], v[200:203], v[16:19], 0\n"
    "ds_read_b128 v[224:227], v4 offset:56320\n"
    "s_waitcnt lgkmcnt(5)\n"
    "v_mfma_f32_16x16x32_bf16 v[168:171], v[204:207], v[16:19], 0\n"
    "ds_read_b128 v[196:199], v4 offset:57344\n"
    "s_waitcnt lgkmcnt(5)\n"
    "v_mfma_f32_16x16x32_bf16 v[172:175], v[208:211], v[16:19], 0\n"
    "ds_read_b128 v[200:203], v4 offset:58368\n"
    "s_waitcnt lgkmcnt(5)\n"
    "v_mfma_f32_16x16x32_bf16 v[160:163], v[212:215], v[20:23], v[160:163]\n"
    "ds_read_b128 v[204:207], v4 offset:59392\n"
    "s_waitcnt lgkmcnt(5)\n"
    "v_mfma_f32_16x16x32_bf16 v[164:167], v[216:219], v[20:23], v[164:167]\n"
    "ds_read_b128 v[208:211], v4 offset:60416\n"
    "s_waitcnt lgkmcnt(5)\n"
    "v_mfma_f32_16x16x32_bf16 v[168:171], v[220:223], v[20:23], v[168:171]\n"
    "ds_read_b128 v[212:215], v4 offset:61440\n"
    "s_waitcnt lgkmcnt(5)\n"
    "v_mfma_f32_16x16x32_bf16 v[172:175], v[224:227], v[20:23], v[172:175]\n"
    "ds_read_b128 v[216:219], v4 offset:62464\n"
    "s_waitcnt lgkmcnt(5)\n"
    "v_mfma_f32_16x16x32_bf16 v[160:163], v[196:199], v[24:27], v[160:163]\n"
    "ds_read_b128 v[220:223], v4 offset:63488\n"
    "s_waitcnt lgkmcnt(5)\n"
    "v_mfma_f32_16x16x32_bf16 v[164:167], v[200:203], v[24:27], v[164:167]\n"
    "ds_read_b128 v[224:227], v4 offset:64512\n"
    "s_waitcnt lgkmcnt(5)\n"
    "v_mfma_f32_16x16x32_bf16 v[168:171], v[204:207], v[24:27], v[168:171]\n"
    "ds_read_b128 v[196:199], v5 offset:0\n"
    "s_waitcnt lgkmcnt(5)\n"
    "v_mfma_f32_16x16x32_bf16 v[172:175], v[208:211], v[24:27], v[172:175]\n"
    "ds_read_b128 v[200:203], v5 offset:1024\n"
    "s_waitcnt lgkmcnt(5)\n"
    "v_mfma_f32_16x16x32_bf16 v[160:163], v[212:215], v[28:31], v[160:163]\n"
    "ds_read_b128 v[204:207], v5 offset:2048\n"
    "s_waitcnt lgkmcnt(5)\n"
    "v_mfma_f32_16x16x32_bf16 v[164:167], v[216:219], v[28:31], v[164:167]\n"
    "ds_read_b128 v[208:211], v5 offset:3072\n"
    "s_waitcnt lgkmcnt(5)\n"
    "v_mfma_f32_16x16x32_bf16 v[168:171], v[220:223], v[28:31], v[168:171]\n"
    "ds_read_b128 v[212:215], v5 offset:4096\n"
    "s_waitcnt lgkmcnt(5)\n"
    "v_mfma_f32_16x16x32_bf16 v[172:175], v[224:227], v[28:31], v[172:175]\n"
    "ds_read_b128 v[216:219], v5 offset:5120\n"
    "s_waitcnt lgkmcnt(5)\n"
    "v_mfma_f32_16x16x32_bf16 v[160:163], v[196:199], v[32:35], v[160:163]\n"
    "ds_read_b128 v[220:223], v5 offset:6144\n"
    "s_waitcnt lgkmcnt(5)\n"
    "v_mfma_f32_16x16x32_bf16 v[164:167], v[200:203], v[32:35], v[164:167]\n"
    "ds_read_b128 v[224:227], v5 offset:7168\n"
    "s_waitcnt lgkmcnt(5)\n"
    "v_mfma_f32_16x16x32_bf16 v[168:171], v[204:207], v[32:35], v[168:171]\n"
    "ds_read_b128 v[196:199], v5 offset:8192\n"
    "s_waitcnt lgkmcnt(5)\n"
    "v_mfma_f32_16x16x32_bf16 v[172:175], v[208:211], v[32:35], v[172:175]\n"
    "ds_read_b128 v[200:203], v5 offset:9216\n"
    "s_waitcnt lgkmcnt(5)\n"
    "v_mfma_f32_16x16x32_bf16 v[160:163], v[212:215], v[36:39], v[160:163]\n"
    "ds_read_b128 v[204:207], v5 offset:10240\n"
    "s_waitcnt lgkmcnt(5)\n"
    "v_mfma_f32_16x16x32_bf16 v[164:167], v[216:219], v[36:39], v[164:167]\n"
    "ds_read_b128 v[208:211], v5 offset:11264\n"
    "s_waitcnt lgkmcnt(5)\n"
    "v_mfma_f32_16x16x32_bf16 v[168:171], v[220:223], v[36:39], v[168:171]\n"
    "ds_read_b128 v[212:215], v5 offset:12288\n"
    "s_waitcnt lgkmcnt(5)\n"
    "v_mfma_f32_16x16x32_bf16 v[172:175], v[224:227], v[36:39], v[172:175]\n"
    "ds_read_b128 v[216:219], v5 offset:13312\n"
    "s_waitcnt lgkmcnt(5)\n"
    "v_mfma_f32_16x16x32_bf16 v[160:163], v[196:199], v[40:43], v[160:163]\n"
    "ds_read_b128 v[220:223], v5 offset:14336\n"
    "s_waitcnt lgkmcnt(5)\n"
    "v_mfma_f32_16x16x32_bf16 v[164:167], v[200:203], v[40:43], v[164:167]\n"
    "ds_read_b128 v[224:227], v5 offset:15360\n"
    "s_waitcnt lgkmcnt(5)\n"
    "v_mfma_f32_16x16x32_bf16 v[168:171], v[204:207], v[40:43], v[168:171]\n"
    "ds_read_b128 v[196:199], v5 offset:16384\n"
    "s_waitcnt lgkmcnt(5)\n"
    "v_mfma_f32_16x16x32_bf16 v[172:175], v[208:211], v[40:43], v[172:175]\n"
    "ds_read_b128 v[200:203], v5 offset:17408\n"
    "s_waitcnt lgkmcnt(5)\n"
    "v_mfma_f32_16x16x32_bf16 v[160:163], v[212:215], v[44:47], v[160:163]\n"
    "ds_read_b128 v[204:207], v5 offset:18432\n"
    "s_waitcnt lgkmcnt(5)\n"
    "v_mfma_f32_16x16x32_bf16 v[164:167], v[216:219], v[44:47], v[164:167]\n"
    "ds_read_b128 v[208:211], v5 offset:19456\n"
    "s_waitcnt lgkmcnt(5)\n"
    "v_mfma_f32_16x16x32_bf16 v[168:171], v[220:223], v[44:47], v[168:171]\n"
    "ds_read_b128 v[212:215], v5 offset:20480\n"
    "s_waitcnt lgkmcnt(5)\n"
    "v_mfma_f32_16x16x32_bf16 v[172:175], v[224:227], v[44:47], v[172:175]\n"
    "ds_read_b128 v[216:219], v5 offset:21504\n"
    "s_waitcnt lgkmcnt(5)\n"
    "v_mfma_f32_16x16x32_bf16 v[160:163], v[196:199], v[48:51], v[160:163]\n"
    "ds_read_b128 v[220:223], v5 offset:22528\n"
    "s_waitcnt lgkmcnt(5)\n"
    "v_mfma_f32_16x16x32_bf16 v[164:167], v[200:203], v[48:51], v[164:167]\n"
    "ds_read_b128 v[224:227], v5 offset:23552\n"
    "s_waitcnt lgkmcnt(5)\n"
    "v_mfma_f32_16x16x32_bf16 v[168:171], v[204:207], v[48:51], v[168:171]\n"
    "ds_read_b128 v[196:199], v5 offset:24576\n"
    "s_waitcnt lgkmcnt(5)\n"
    "v_mfma_f32_16x16x32_bf16 v[172:175], v[208:211], v[48:51], v[172:175]\n"
    "ds_read_b128 v[200:203], v5 offset:25600\n"
    "s_waitcnt lgkmcnt(5)\n"
    "v_mfma_f32_16x16x32_bf16 v[160:163], v[212:215], v[52:55], v[160:163]\n"
    "ds_read_b128 v[204:207], v5 offset:26624\n"
    "s_waitcnt lgkmcnt(5)\n"
    "v_mfma_f32_16x16x32_bf16 v[164:167], v[216:219], v[52:55], v[164:167]\n"
    "ds_read_b128 v[208:211], v5 offset:27648\n"
    "s_waitcnt lgkmcnt(5)\n"
    "v_mfma_f32_16x16x32_bf16 v[168:171], v[220:223], v[52:55], v[168:171]\n"
    "ds_read_b128 v[212:215], v5 offset:28672\n"
    "s_waitcnt lgkmcnt(5)\n"
    "v_mfma_f32_16x16x32_bf16 v[172:175], v[224:227], v[52:55], v[172:175]\n"
    "ds_read_b128 v[216:219], v5 offset:29696\n"
    "s_waitcnt lgkmcnt(5)\n"
    "v_mfma_f32_16x16x32_bf16 v[160:163], v[196:199], v[56:59], v[160:163]\n"
    "ds_read_b128 v[220:223], v5 offset:30720\n"
    "s_waitcnt lgkmcnt(5)\n"
    "v_mfma_f32_16x16x32_bf16 v[164:167], v[200:203], v[56:59], v[164:167]\n"
    "ds_read_b128 v[224:227], v5 offset:31744\n"
    "s_waitcnt lgkmcnt(5)\n"
    "v_mfma_f32_16x16x32_bf16 v[168:171], v[204:207], v[56:59], v[168:171]\n"
    "ds_read_b128 v[196:199], v5 offset:32768\n"
    "s_waitcnt lgkmcnt(5)\n"
    "v_mfma_f32_16x16x32_bf16 v[172:175], v[208:211], v[56:59], v[172:175]\n"
    "ds_read_b128 v[200:203], v5 offset:33792\n"
    "s_waitcnt lgkmcnt(5)\n"
    "v_mfma_f32_16x16x32_bf16 v[160:163], v[212:215], v[60:63], v[160:163]\n"
    "ds_read_b128 v[204:207], v5 offset:34816\n"
    "s_waitcnt lgkmcnt(5)\n"
    "v_mfma_f32_16x16x32_bf16 v[164:167], v[216:219], v[60:63], v[164:167]\n"
    "ds_read_b128 v[208:211], v5 offset:35840\n"
    "s_waitcnt lgkmcnt(5)\n"
    "v_mfma_f32_16x16x32_bf16 v[168:171], v[220:223], v[60:63], v[168:171]\n"
    "ds_read_b128 v[212:215], v5 offset:36864\n"
    "s_waitcnt lgkmcnt(5)\n"
    "v_mfma_f32_16x16x32_bf16 v[172:175], v[224:227], v[60:63], v[172:175]\n"
    "ds_read_b128 v[216:219], v5 offset:37888\n"
    "s_waitcnt lgkmcnt(5)\n"
    "v_mfma_f32_16x16x32_bf16 v[160:163], v[196:199], v[64:67], v[160:163]\n"
    "ds_read_b128 v[220:223], v5 offset:38912\n"
    "s_waitcnt lgkmcnt(5)\n"
    "v_mfma_f32_16x16x32_bf16 v[164:167], v[200:203], v[64:67], v[164:167]\n"
    "ds_read_b128 v[224:227], v5 offset:39936\n"
    "s_waitcnt lgkmcnt(5)\n"
    "v_mfma_f32_16x16x32_bf16 v[168:171], v[204:207], v[64:67], v[168:171]\n"
    "ds_read_b128 v[196:199], v5 offset:40960\n"
    "s_waitcnt lgkmcnt(5)\n"
    "v_mfma_f32_16x16x32_bf16 v[172:175], v[208:211], v[64:67], v[172:175]\n"
    "ds_read_b128 v[200:203], v5 offset:41984\n"
    "s_waitcnt lgkmcnt(5)\n"
    "v_mfma_f32_16x16x32_bf16 v[160:163], v[212:215], v[68:71], v[160:163]\n"
    "ds_read_b128 v[204:207], v5 offset:43008\n"
    "s_waitcnt lgkmcnt(5)\n"
    "v_mfma_f32_16x16x32_bf16 v[164:167], v[216:219], v[68:71], v[164:167]\n"
    "ds_read_b128 v[208:211], v5 offset:44032\n"
    "s_waitcnt lgkmcnt(5)\n"
    "v_mfma_f32_16x16x32_bf16 v[168:171], v[220:223], v[68:71], v[168:171]\n"
    "ds_read_b128 v[212:215], v5 offset:45056\n"
    "s_waitcnt lgkmcnt(5)\n"
    "v_mfma_f32_16x16x32_bf16 v[172:175], v[224:227], v[68:71], v[172:175]\n"
    "ds_read_b128 v[216:219], v5 offset:46080\n"
    "s_waitcnt lgkmcnt(5)\n"
    "v_mfma_f32_16x16x32_bf16 v[160:163], v[196:199], v[72:75], v[160:163]\n"
    "ds_read_b128 v[220:223], v5 offset:47104\n"
    "s_waitcnt lgkmcnt(5)\n"
    "v_mfma_f32_16x16x32_bf16 v[164:167], v[200:203], v[72:75], v[164:167]\n"
    "ds_read_b128 v[224:227], v5 offset:48128\n"
    "s_waitcnt lgkmcnt(5)\n"
    "v_mfma_f32_16x16x32_bf16 v[168:171], v[204:207], v[72:75], v[168:171]\n"
    "ds_read_b128 v[196:199], v5 offset:49152\n"
    "s_waitcnt lgkmcnt(5)\n"
    "v_mfma_f32_16x16x32_bf16 v[172:175], v[208:211], v[72:75], v[172:175]\n"
    "ds_read_b128 v[200:203], v5 offset:50176\n"
    "s_waitcnt lgkmcnt(5)\n"
    "v_mfma_f32_16x16x32_bf16 v[160:163], v[212:215], v[76:79], v[160:163]\n"
    "ds_read_b128 v[204:207], v5 offset:51200\n"
    "s_waitcnt lgkmcnt(5)\n"
    "v_mfma_f32_16x16x32_bf16 v[164:167], v[216:219], v[76:79], v[164:167]\n"
    "ds_read_b128 v[208:211], v5 offset:52224\n"
    "s_waitcnt lgkmcnt(5)\n"
    "v_mfma_f32_16x16x32_bf16 v[168:171], v[220:223], v[76:79], v[168:171]\n"
    "ds_read_b128 v[212:215], v5 offset:53248\n"
    "s_waitcnt lgkmcnt(5)\n"
    "v_mfma_f32_16x16x32_bf16 v[172:175], v[224:227], v[76:79], v[172:175]\n"
    "ds_read_b128 v[216:219], v5 offset:54272\n"
    "s_waitcnt lgkmcnt(5)\n"
    "v_mfma_f32_16x16x32_bf16 v[160:163], v[196:199], v[80:83], v[160:163]\n"
    "ds_read_b128 v[220:223], v5 offset:55296\n"
    "s_waitcnt lgkmcnt(5)\n"
    "v_mfma_f32_16x16x32_bf16 v[164:167], v[200:203], v[80:83], v[164:167]\n"
    "ds_read_b128 v[224:227], v5 offset:56320\n"
    "s_waitcnt lgkmcnt(5)\n"
    "v_mfma_f32_16x16x32_bf16 v[168:171], v[204:207], v[80:83], v[168:171]\n"
    "ds_read_b128 v[196:199], v5 offset:57344\n"
    "s_waitcnt lgkmcnt(5)\n"
    "v_mfma_f32_16x16x32_bf16 v[172:175], v[208:211], v[80:83], v[172:175]\n"
    "ds_read_b128 v[200:203], v5 offset:58368\n"
    "s_waitcnt lgkmcnt(5)\n"
    "v_mfma_f32_16x16x32_bf16 v[160:163], v[212:215], v[84:87], v[160:163]\n"
    "ds_read_b128 v[204:207], v5 offset:59392\n"
    "s_waitcnt lgkmcnt(5)\n"
    "v_mfma_f32_16x16x32_bf16 v[164:167], v[216:219], v[84:87], v[164:167]\n"
    "ds_read_b128 v[208:211], v5 offset:60416\n"
    "s_waitcnt lgkmcnt(5)\n"
    "v_mfma_f32_16x16x32_bf16 v[168:171], v[220:223], v[84:87], v[168:171]\n"
    "ds_read_b128 v[212:215], v5 offset:61440\n"
    "s_waitcnt lgkmcnt(5)\n"
    "v_mfma_f32_16x16x32_bf16 v[172:175], v[224:227], v[84:87], v[172:175]\n"
    "ds_read_b128 v[216:219], v5 offset:62464\n"
    "s_waitcnt lgkmcnt(5)\n"
    "v_mfma_f32_16x16x32_bf16 v[160:163], v[196:199], v[88:91], v[160:163]\n"
    "ds_read_b128 v[220:223], v5 offset:63488\n"
    "s_waitcnt lgkmcnt(5)\n"
    "v_mfma_f32_16x16x32_bf16 v[164:167], v[200:203], v[88:91], v[164:167]\n"
    "ds_read_b128 v[224:227], v5 offset:64512\n"
    "s_waitcnt lgkmcnt(5)\n"
    "v_mfma_f32_16x16x32_bf16 v[168:171], v[204:207], v[88:91], v[168:171]\n"
    "ds_read_b128 v[196:199], v11 offset:0\n"
    "s_waitcnt lgkmcnt(5)\n"
    "v_mfma_f32_16x16x32_bf16 v[172:175], v[208:211], v[88:91], v[172:175]\n"
    "ds_read_b128 v[200:203], v11 offset:1024\n"
    "s_waitcnt lgkmcnt(5)\n"
    "v_mfma_f32_16x16x32_bf16 v[160:163], v[212:215], v[92:95], v[160:163]\n"
    "ds_read_b128 v[204:207], v11 offset:2048\n"
    "s_waitcnt lgkmcnt(5)\n"
    "v_mfma_f32_16x16x32_bf16 v[164:167], v[216:219], v[92:95], v[164:167]\n"
    "ds_read_b128 v[208:211], v11 offset:3072\n"
    "s_waitcnt lgkmcnt(5)\n"
    "v_mfma_f32_16x16x32_bf16 v[168:171], v[220:223], v[92:95], v[168:171]\n"
    "ds_read_b128 v[212:215], v11 offset:4096\n"
    "s_waitcnt lgkmcnt(5)\n"
    "v_mfma_f32_16x16x32_bf16 v[172:175], v[224:227], v[92:95], v[172:175]\n"
    "ds_read_b128 v[216:219], v11 offset:5120\n"
    "s_waitcnt lgkmcnt(5)\n"
    "v_mfma_f32_16x16x32_bf16 v[160:163], v[196:199], v[96:99], v[160:163]\n"
    "ds_read_b128 v[220:223], v11 offset:6144\n"
    "s_waitcnt lgkmcnt(5)\n"
    "v_mfma_f32_16x16x32_bf16 v[164:167], v[200:203], v[96:99], v[164:167]\n"
    "ds_read_b128 v[224:227], v11 offset:7168\n"
    "s_waitcnt lgkmcnt(5)\n"
    "v_mfma_f32_16x16x32_bf16 v[168:171], v[204:207], v[96:99], v[168:171]\n"
    "ds_read_b128 v[196:199], v11 offset:8192\n"
    "s_waitcnt lgkmcnt(5)\n"
    "v_mfma_f32_16x16x32_bf16 v[172:175], v[208:211], v[96:99], v[172:175]\n"
    "ds_read_b128 v[200:203], v11 offset:9216\n"
    "s_waitcnt lgkmcnt(5)\n"
    "v_mfma_f32_16x16x32_bf16 v[160:163], v[212:215], v[100:103], v[160:163]\n"
    "ds_read_b128 v[204:207], v11 offset:10240\n"
    "s_waitcnt lgkmcnt(5)\n"
    "v_mfma_f32_16x16x32_bf16 v[164:167], v[216:219], v[100:103], v[164:167]\n"
    "ds_read_b128 v[208:211], v11 offset:11264\n"
    "s_waitcnt lgkmcnt(5)\n"
    "v_mfma_f32_16x16x32_bf16 v[168:171], v[220:223], v[100:103], v[168:171]\n"
    "ds_read_b128 v[212:215], v11 offset:12288\n"
    "s_waitcnt lgkmcnt(5)\n"
    "v_mfma_f32_16x16x32_bf16 v[172:175], v[224:227], v[100:103], v[172:175]\n"
    "ds_read_b128 v[216:219], v11 offset:13312\n"
    "s_waitcnt lgkmcnt(5)\n"
    "v_mfma_f32_16x16x32_bf16 v[160:163], v[196:199], v[104:107], v[160:163]\n"
    "ds_read_b128 v[220:223], v11 offset:14336\n"
    "s_waitcnt lgkmcnt(5)\n"
    "v_mfma_f32_16x16x32_bf16 v[164:167], v[200:203], v[104:107], v[164:167]\n"
    "ds_read_b128 v[224:227], v11 offset:15360\n"
    "s_waitcnt lgkmcnt(5)\n"
    "v_mfma_f32_16x16x32_bf16 v[168:171], v[204:207], v[104:107], v[168:171]\n"
    "ds_read_b128 v[196:199], v11 offset:16384\n"
    "s_waitcnt lgkmcnt(5)\n"
    "v_mfma_f32_16x16x32_bf16 v[172:175], v[208:211], v[104:107], v[172:175]\n"
    "ds_read_b128 v[200:203], v11 offset:17408\n"
    "s_waitcnt lgkmcnt(5)\n"
    "v_mfma_f32_16x16x32_bf16 v[160:163], v[212:215], v[108:111], v[160:163]\n"
    "ds_read_b128 v[204:207], v11 offset:18432\n"
    "s_waitcnt lgkmcnt(5)\n"
    "v_mfma_f32_16x16x32_bf16 v[164:167], v[216:219], v[108:111], v[164:167]\n"
    "ds_read_b128 v[208:211], v11 offset:19456\n"
    "s_waitcnt lgkmcnt(5)\n"
    "v_mfma_f32_16x16x32_bf16 v[168:171], v[220:223], v[108:111], v[168:171]\n"
    "ds_read_b128 v[212:215], v11 offset:20480\n"
    "s_waitcnt lgkmcnt(5)\n"
    "v_mfma_f32_16x16x32_bf16 v[172:175], v[224:227], v[108:111], v[172:175]\n"
    "ds_read_b128 v[216:219], v11 offset:21504\n"
    "s_waitcnt lgkmcnt(5)\n"
    "v_mfma_f32_16x16x32_bf16 v[160:163], v[196:199], v[112:115], v[160:163]\n"
    "ds_read_b128 v[220:223], v11 offset:22528\n"
    "s_waitcnt lgkmcnt(5)\n"
    "v_mfma_f32_16x16x32_bf16 v[164:167], v[200:203], v[112:115], v[164:167]\n"
    "ds_read_b128 v[224:227], v11 offset:23552\n"
    "s_waitcnt lgkmcnt(5)\n"
    "v_mfma_f32_16x16x32_bf16 v[168:171], v[204:207], v[112:115], v[168:171]\n"
    "ds_read_b128 v[196:199], v11 offset:24576\n"
    "s_waitcnt lgkmcnt(5)\n"
    "v_mfma_f32_16x16x32_bf16 v[172:175], v[208:211], v[112:115], v[172:175]\n"
    "ds_read_b128 v[200:203], v11 offset:25600\n"
    "s_waitcnt lgkmcnt(5)\n"
    "v_mfma_f32_16x16x32_bf16 v[160:163], v[212:215], v[116:119], v[160:163]\n"
    "ds_read_b128 v[204:207], v11 offset:26624\n"
    "s_waitcnt lgkmcnt(5)\n"
    "v_mfma_f32_16x16x32_bf16 v[164:167], v[216:219], v[116:119], v[164:167]\n"
    "ds_read_b128 v[208:211], v11 offset:27648\n"
    "s_waitcnt lgkmcnt(5)\n"
    "v_mfma_f32_16x16x32_bf16 v[168:171], v[220:223], v[116:119], v[168:171]\n"
    "ds_read_b128 v[212:215], v11 offset:28672\n"
    "s_waitcnt lgkmcnt(5)\n"
    "v_mfma_f32_16x16x32_bf16 v[172:175], v[224:227], v[116:119], v[172:175]\n"
    "ds_read_b128 v[216:219], v11 offset:29696\n"
    "s_waitcnt lgkmcnt(5)\n"
    "v_mfma_f32_16x16x32_bf16 v[160:163], v[196:199], v[120:123], v[160:163]\n"
    "ds_read_b128 v[220:223], v11 offset:30720\n"
    "s_waitcnt lgkmcnt(5)\n"
    "v_mfma_f32_16x16x32_bf16 v[164:167], v[200:203], v[120:123], v[164:167]\n"
    "ds_read_b128 v[224:227], v11 offset:31744\n"
    "s_waitcnt lgkmcnt(5)\n"
    "v_mfma_f32_16x16x32_bf16 v[168:171], v[204:207], v[120:123], v[168:171]\n"
    "s_waitcnt lgkmcnt(4)\n"
    "v_mfma_f32_16x16x32_bf16 v[172:175], v[208:211], v[120:123], v[172:175]\n"
    "s_waitcnt lgkmcnt(3)\n"
    "v_mfma_f32_16x16x32_bf16 v[160:163], v[212:215], v[124:127], v[160:163]\n"
    "s_waitcnt lgkmcnt(2)\n"
    "v_mfma_f32_16x16x32_bf16 v[164:167], v[216:219], v[124:127], v[164:167]\n"
    "s_waitcnt lgkmcnt(1)\n"
    "v_mfma_f32_16x16x32_bf16 v[168:171], v[220:223], v[124:127], v[168:171]\n"
    "s_waitcnt lgkmcnt(0)\n"
    "v_mfma_f32_16x16x32_bf16 v[172:175], v[224:227], v[124:127], v[172:175]\n"
    "s_nop 7\n"
    "v_lshlrev_b32 v12, 16, v244\n"
    "v_and_b32 v13, 0xffff0000, v244\n"
    "v_lshlrev_b32 v14, 16, v245\n"
    "v_and_b32 v15, 0xffff0000, v245\n"
    "v_fma_f32 v160, v160, v176, v12\n"
    "v_fma_f32 v161, v161, v177, v13\n"
    "v_fma_f32 v162, v162, v178, v14\n"
    "v_fma_f32 v163, v163, v179, v15\n"
    "v_cvt_pk_bf16_f32 v160, v160, v161\n"
    "v_cvt_pk_bf16_f32 v161, v162, v163\n"
    "buffer_store_dwordx2 v[160:161], v252, s[44:47], s78 offen offset:0\n"
    "v_lshlrev_b32 v12, 16, v246\n"
    "v_and_b32 v13, 0xffff0000, v246\n"
    "v_lshlrev_b32 v14, 16, v247\n"
    "v_and_b32 v15, 0xffff0000, v247\n"
    "v_fma_f32 v164, v164, v180, v12\n"
    "v_fma_f32 v165, v165, v181, v13\n"
    "v_fma_f32 v166, v166, v182, v14\n"
    "v_fma_f32 v167, v167, v183, v15\n"
    "v_cvt_pk_bf16_f32 v164, v164, v165\n"
    "v_cvt_pk_bf16_f32 v165, v166, v167\n"
    "buffer_store_dwordx2 v[164:165], v252, s[44:47], s78 offen offset:32\n"
    "v_lshlrev_b32 v12, 16, v248\n"
    "v_and_b32 v13, 0xffff0000, v248\n"
    "v_lshlrev_b32 v14, 16, v249\n"
    "v_and_b32 v15, 0xffff0000, v249\n"
    "v_fma_f32 v168, v168, v184, v12\n"
    "v_fma_f32 v169, v169, v185, v13\n"
    "v_fma_f32 v170, v170, v186, v14\n"
    "v_fma_f32 v171, v171, v187, v15\n"
    "v_cvt_pk_bf16_f32 v168, v168, v169\n"
    "v_cvt_pk_bf16_f32 v169, v170, v171\n"
    "buffer_store_dwordx2 v[168:169], v252, s[44:47], s78 offen offset:64\n"
    "v_lshlrev_b32 v12, 16, v250\n"
    "v_and_b32 v13, 0xffff0000, v250\n"
    "v_lshlrev_b32 v14, 16, v251\n"
    "v_and_b32 v15, 0xffff0000, v251\n"
    "v_fma_f32 v172, v172, v188, v12\n"
    "v_fma_f32 v173, v173, v189, v13\n"
    "v_fma_f32 v174, v174, v190, v14\n"
    "v_fma_f32 v175, v175, v191, v15\n"
    "v_cvt_pk_bf16_f32 v172, v172, v173\n"
    "v_cvt_pk_bf16_f32 v173, v174, v175\n"
    "buffer_store_dwordx2 v[172:173], v252, s[44:47], s78 offen offset:96\n"
    "L_tend_%=:\n"
    "L_codeend_%=:\n"
    :: "s"(ws), "s"(out), "s"(bsp), "s"(w), "s"(tb), "s"(j), "s"(ldsb), "s"(b), "s"(x)
    : "v0","v1","v2","v3","v4","v5","v6","v7","v8","v9","v10","v11","v12","v13","v14","v15","v16","v17","v18","v19","v20","v21","v22","v23","v24","v25","v26","v27","v28","v29","v30","v31","v32","v33","v34","v35","v36","v37","v38","v39","v40","v41","v42","v43","v44","v45","v46","v47","v48","v49","v50","v51","v52","v53","v54","v55","v56","v57","v58","v59","v60","v61","v62","v63","v64","v65","v66","v67","v68","v69","v70","v71","v72","v73","v74","v75","v76","v77","v78","v79","v80","v81","v82","v83","v84","v85","v86","v87","v88","v89","v90","v91","v92","v93","v94","v95","v96","v97","v98","v99","v100","v101","v102","v103","v104","v105","v106","v107","v108","v109","v110","v111","v112","v113","v114","v115","v116","v117","v118","v119","v120","v121","v122","v123","v124","v125","v126","v127","v128","v129","v130","v131","v132","v133","v134","v135","v136","v137","v138","v139","v140","v141","v142","v143","v144","v145","v146","v147","v148","v149","v150","v151","v152","v153","v154","v155","v156","v157","v158","v159","v160","v161","v162","v163","v164","v165","v166","v167","v168","v169","v170","v171","v172","v173","v174","v175","v176","v177","v178","v179","v180","v181","v182","v183","v184","v185","v186","v187","v188","v189","v190","v191","v192","v193","v194","v195","v196","v197","v198","v199","v200","v201","v202","v203","v204","v205","v206","v207","v208","v209","v210","v211","v212","v213","v214","v215","v216","v217","v218","v219","v220","v221","v222","v223","v224","v225","v226","v227","v228","v229","v230","v231","v232","v233","v234","v235","v236","v237","v238","v239","v240","v241","v242","v243","v244","v245","v246","v247","v248","v249","v250","v251","v252","v253","v254","v255","a0","a1","a2","a3","a4","a5","a6","a7","a8","a9","a10","a11","a12","a13","a14","a15","a16","a17","a18","a19","a20","a21","a22","a23","a24","a25","a26","a27","a28","a29","a30","a31","a32","a33","a34","a35","a36","a37","a38","a39","a40","a41","a42","a43","a44","a45","a46","a47","a48","a49","a50","a51","a52","a53","a54","a55","a56","a57","a58","a59","a60","a61","a62","a63","a64","a65","a66","a67","a68","a69","a70","a71","a72","a73","a74","a75","a76","a77","a78","a79","a80","a81","a82","a83","a84","a85","a86","a87","a88","a89","a90","a91","a92","a93","a94","a95","a96","a97","a98","a99","a100","a101","a102","a103","a104","a105","a106","a107","a108","a109","a110","a111","a112","a113","a114","a115","a116","a117","a118","a119","a120","a121","a122","a123","a124","a125","a126","a127","a128","a129","a130","a131","a132","a133","a134","a135","a136","a137","a138","a139","a140","a141","a142","a143","a144","a145","a146","a147","a148","a149","a150","a151","a152","a153","a154","a155","a156","a157","a158","a159","a160","a161","a162","a163","a164","a165","a166","a167","a168","a169","a170","a171","a172","a173","a174","a175","a176","a177","a178","a179","a180","a181","a182","a183","a184","a185","a186","a187","a188","a189","a190","a191","a192","a193","a194","a195","a196","a197","a198","a199","a200","a201","a202","a203","a204","a205","a206","a207","a208","a209","a210","a211","a212","a213","a214","a215","a216","a217","a218","a219","a220","a221","a222","a223","a224","a225","a226","a227","a228","a229","a230","a231","a232","a233","a234","a235","a236","a237","a238","a239","a240","a241","a242","a243","a244","a245","a246","a247","a248","a249","a250","a251","a252","a253","a254","a255","s40","s41","s42","s43","s44","s45","s46","s47","s48","s49","s50","s51","s52","s53","s54","s55","s56","s57","s58","s59","s60","s61","s62","s63","s64","s65","s66","s67","s68","s69","s70","s71","s72","s73","s74","s75","s76","s77","s78","s79","s80","s81","s82","s83","s84","s85","s86","s87","s88","s89","s90","s91","s92","s93","s94","s95","s96","s97","s98","s99","vcc","memory");

}
extern "C" void kernel_launch(void* const* d_in, const int* in_sizes, int n_in, void* d_out, int out_size, void* d_ws, size_t ws_size, hipStream_t stream) {
  char* ws = (char*)d_ws;
  k0_prep<<<K0_GRID, 256, 0, stream>>>((const unsigned short*)d_in[0], (const unsigned short*)d_in[1], (const unsigned short*)d_in[2], (const unsigned short*)d_in[10], (const unsigned short*)d_in[11], ws, (const unsigned short*)d_in[6], (const unsigned short*)d_in[9]);
  k1_attn<<<256, 256, 0, stream>>>(ws, d_in[3], d_in[4], d_in[7], d_in[6], d_in[9]);
  t_tail<<<256, 256, 0, stream>>>((const unsigned short*)d_in[0], (const unsigned short*)d_in[8], ws, (unsigned short*)d_out);
}
```
